```python
import torch

def _softmax(x, axis=-1):
    return torch.softmax(x, dim=axis)

def _gelu(x):
    return torch.nn.functional.gelu(x, approximate='tanh')

def _silu(x):
    return torch.nn.functional.silu(x)

def _sigmoid(x):
    return torch.sigmoid(x)

def _layernorm(x, axis=-1, eps=1e-05):
    m = x.mean(dim=axis, keepdim=True)
    v = x.var(dim=axis, keepdim=True, unbiased=False)
    return (x - m) / torch.sqrt(v + eps)

def _scatter_add(tgt, idx, src, axis=0):
    return tgt.clone().index_add_(axis, idx, src)

def _segment_sum(src, seg, n):
    out = torch.zeros((n,) + src.shape[1:], dtype=src.dtype)
    return out.index_add_(0, seg, src)

def _pad(x, pw):
    flat = tuple((v for pair in reversed(pw) for v in pair))
    return torch.nn.functional.pad(x, flat)
kernel_name = 'adaLN_cond__compressed_kv_cla__moe_arch_mod__prompt'

def setup_inputs():

    _w = lambda a: (lambda t: t.to(torch.bfloat16) if t.is_floating_point() else t)(torch.as_tensor(a))

    def _A_setup():
        B, H, Sq, Sk, D, M = (1, 32, 2048, 2048, 128, 640)
        Dc = 4 * D
        return {'x': _w((lambda *s: torch.randn(*s, dtype=torch.bfloat16) * 0.1)(B, Sq, M)), 'c_kv': _w((lambda *s: torch.randn(*s, dtype=torch.bfloat16) * 1.6)(B, Sk, Dc)), 'w_q': _w((lambda *s: torch.randn(*s, dtype=torch.bfloat16) * 0.8)(M, H, D) * M ** (-0.5)), 'w_uk': _w((lambda *s: torch.randn(*s, dtype=torch.bfloat16) * 0.8)(Dc, H, D) * Dc ** (-0.5)), 'w_uv': _w((lambda *s: torch.randn(*s, dtype=torch.bfloat16) * 1.6)(Dc, H, D) * Dc ** (-0.5)), 'w_o': _w((lambda *s: torch.randn(*s, dtype=torch.bfloat16) * 1.6)(H, D, M) * (H * D) ** (-0.5))}
    _A_in = _A_setup()

    def _B_setup():
        B, S, D, F, C = (1, 2048, 640, 1728, 8)
        return {'x': _w((lambda *s: torch.randn(*s, dtype=torch.bfloat16) * 0.1)(B, S, D)), 'w_r': _w((lambda *s: torch.randn(*s, dtype=torch.bfloat16) * 0.1)(D)), 'w_gate': _w((lambda *s: torch.randn(*s, dtype=torch.bfloat16) * 0.1)(D, F)), 'w_up': _w((lambda *s: torch.randn(*s, dtype=torch.bfloat16) * 0.1)(D, F)), 'w_down': _w((lambda *s: torch.randn(*s, dtype=torch.bfloat16) * 0.1)(F, D))}
    _B_in = _B_setup()
    return {'A_x': _A_in['x'], 'A_c_kv': _A_in['c_kv'], 'A_w_q': _A_in['w_q'], 'A_w_uk': _A_in['w_uk'], 'A_w_uv': _A_in['w_uv'], 'A_w_o': _A_in['w_o'], 'B_x': _B_in['x'], 'B_w_r': _B_in['w_r'], 'B_w_gate': _B_in['w_gate'], 'B_w_up': _B_in['w_up'], 'B_w_down': _B_in['w_down'], 'c': _w((lambda *s: torch.randn(*s, dtype=torch.bfloat16) * 0.2)(1, 640) * 8.0), 'w_mod': _w((lambda *s: torch.randn(*s, dtype=torch.bfloat16) * 0.2)(640, 6 * 640) * 640 ** (-0.5) * 8.0)}

def reference(A_x, A_c_kv, A_w_q, A_w_uk, A_w_uv, A_w_o, B_x, B_w_r, B_w_gate, B_w_up, B_w_down, c, w_mod):

    def _A_ref(x, c_kv, w_q, w_uk, w_uv, w_o):
        B, Sq, M = (x.shape[0], x.shape[1], x.shape[2])
        Sk = c_kv.shape[1]
        H, D = (w_q.shape[1], w_q.shape[2])
        q = torch.einsum('bqm,mhd->bhqd', x, w_q)
        k = torch.einsum('bkc,chd->bhkd', c_kv, w_uk)
        v = torch.einsum('bkc,chd->bhkd', c_kv, w_uv)
        scores = torch.einsum('bhqd,bhkd->bhqk', q, k) * D ** (-0.5)
        off = Sk - Sq
        qi = torch.arange(Sq)[:, None]
        kj = torch.arange(Sk)[None, :]
        cmask = kj <= qi + off
        scores = torch.where(cmask[None, None], scores, -1000000000.0)
        p = _softmax(scores, axis=-1)
        ctx = torch.einsum('bhqk,bhkd->bhqd', p, v)
        return torch.einsum('bhqd,hdm->bqm', ctx, w_o)

    def _B_ref(x, w_r, w_gate, w_up, w_down):
        B, S, D = x.shape
        C = 8
        r = x @ w_r
        idx = torch.argsort(-r, axis=-1)[:, :C]
        idx_e = (lambda x, n, axis: torch.repeat_interleave(x, n, dim=axis))(idx.reshape(B, C, 1), D, axis=2)
        x_sel = (lambda x, i, axis: torch.take_along_dim(x, i, dim=axis))(x, idx_e, axis=1)
        g = _silu(x_sel @ w_gate)
        u = x_sel @ w_up
        y = g * u @ w_down
        r_sel = _sigmoid((lambda x, i, axis: torch.take_along_dim(x, i, dim=axis))(r, idx, axis=1))
        y = y * r_sel.reshape(B, C, 1)
        b_off = (torch.arange(B) * S).reshape(B, 1)
        flat_idx = (idx + b_off).reshape(B * C)
        zero = (x * 0.0).reshape(B * S, D)
        upd = _scatter_add(zero, flat_idx, y.reshape(B * C, D), axis=0)
        return x + upd.reshape(B, S, D)
    x = A_x
    mod = (_silu(c) @ w_mod).reshape(c.shape[0], 6, 1, -1)
    sa, ca, ga, sb, cb, gb = (mod[:, 0].clone(), mod[:, 1].clone(), mod[:, 2].clone(), mod[:, 3].clone(), mod[:, 4].clone(), mod[:, 5].clone())
    h = _layernorm(x, axis=-1) * (1 + ca) + sa
    x = x + ga * _A_ref(h, A_c_kv, A_w_q, A_w_uk, A_w_uv, A_w_o)
    h = _layernorm(x, axis=-1) * (1 + cb) + sb
    x = x + gb * _B_ref(h, B_w_r, B_w_gate, B_w_up, B_w_down)
    return x
```

```cpp
#include <hip/hip_runtime.h>
#include <stdint.h>

typedef unsigned short u16;
typedef short bf16x8 __attribute__((ext_vector_type(8)));
typedef short s16x4 __attribute__((ext_vector_type(4)));
typedef float f32x16 __attribute__((ext_vector_type(16)));
typedef float f32x4 __attribute__((ext_vector_type(4)));
typedef float f32x2 __attribute__((ext_vector_type(2)));
typedef __bf16 bf16x2_t __attribute__((ext_vector_type(2)));
typedef unsigned u32x4 __attribute__((ext_vector_type(4)));
typedef unsigned u32x2 __attribute__((ext_vector_type(2)));
#define LAS __attribute__((address_space(3)))
#define DI __device__ __forceinline__

constexpr int SEQ = 2048, DM = 640, NH = 32, HD = 128, DC = 512, FF = 1728, TOPK = 8;

constexpr size_t WS_MOD  = 0;
constexpr size_t WS_R    = 16384;
constexpr size_t WS_IDX  = WS_R + 8192;
constexpr size_t WS_ACT  = WS_IDX + 1024;
constexpr size_t WS_HF   = 65536;
constexpr size_t WS_X1   = WS_HF + (size_t)SEQ * DM * 2;
constexpr size_t WS_H2   = WS_X1 + (size_t)SEQ * DM * 2;
constexpr size_t WS_KF   = WS_H2 + (size_t)SEQ * DM * 2;
constexpr size_t WS_VF   = WS_KF + (size_t)NH * SEQ * HD * 2;
constexpr size_t WS_CTX  = WS_VF + (size_t)NH * SEQ * HD * 2;
constexpr size_t WS_PART = WS_CTX + (size_t)NH * SEQ * HD * 2;

DI float bf2f(u16 v) { return __uint_as_float((unsigned)v << 16); }
DI float bflo(unsigned w) { return __uint_as_float(w << 16); }
DI float bfhi(unsigned w) { return __uint_as_float(w & 0xffff0000u); }
DI unsigned cvtpk(float lo, float hi) { f32x2 v = {lo, hi}; bf16x2_t b = __builtin_convertvector(v, bf16x2_t); return __builtin_bit_cast(unsigned, b); }
DI float rb(float x) { return bflo(cvtpk(x, x)); }
DI float rbh(float x) { return __uint_as_float(cvtpk(0.f, x)); }
DI unsigned rfl(unsigned x) { return __builtin_amdgcn_readfirstlane(x); }
#define MFMA32(a, b, c) __builtin_amdgcn_mfma_f32_32x32x16_bf16((a), (b), (c), 0, 0, 0)
#define SRD3 0x00020000

typedef __amdgpu_buffer_rsrc_t srd_t;
DI srd_t mk_srd(const void* p, unsigned bytes) { return __builtin_amdgcn_make_buffer_rsrc((void*)p, (short)0, (int)bytes, SRD3); }
DI void dma16(unsigned ldsdst, unsigned voff, srd_t srd, unsigned soff) {
  asm volatile("s_mov_b32 m0, %0\n\ts_nop 0\n\tbuffer_load_dwordx4 %1, %2, %3 offen lds"
               :: "s"(ldsdst), "v"(voff), "s"(srd), "s"(soff) : "memory");
}
DI bf16x8 lds_rd128(unsigned addr) { return *(const LAS bf16x8*)(uintptr_t)addr; }
typedef short v4i16_t __attribute__((ext_vector_type(4)));
DI s16x4 lds_tr(unsigned addr) { return __builtin_amdgcn_ds_read_tr16_b64_v4i16((LAS v4i16_t*)(uintptr_t)addr); }
DI bf16x8 cat8(s16x4 a, s16x4 b) { return __builtin_shufflevector(a, b, 0, 1, 2, 3, 4, 5, 6, 7); }
#define WAITV(n) asm volatile("s_waitcnt vmcnt(" #n ")" ::: "memory")
#define BARRIER() asm volatile("s_waitcnt lgkmcnt(0)\n\ts_barrier" ::: "memory")
DI u32x4 pack8(const f32x16& a, int s) {
  u32x4 w;
  w[0] = cvtpk(a[8 * s + 0], a[8 * s + 1]); w[1] = cvtpk(a[8 * s + 2], a[8 * s + 3]);
  w[2] = cvtpk(a[8 * s + 4], a[8 * s + 5]); w[3] = cvtpk(a[8 * s + 6], a[8 * s + 7]);
  return w;
}
DI float wave_sum(float v) {
#pragma unroll
  for (int o = 32; o > 0; o >>= 1) v += __shfl_xor(v, o);
  return v;
}
DI float swap32_max(float v) {
  auto r = __builtin_amdgcn_permlane32_swap(__float_as_uint(v), __float_as_uint(v), false, false);
  return fmaxf(__uint_as_float(r[0]), __uint_as_float(r[1]));
}
DI float swap32_sum(float v) {
  auto r = __builtin_amdgcn_permlane32_swap(__float_as_uint(v), __float_as_uint(v), false, false);
  return __uint_as_float(r[0]) + __uint_as_float(r[1]);
}

__global__ __launch_bounds__(256) void k_mod(const u16* __restrict__ c, const u16* __restrict__ wmod, float* __restrict__ mod) {
  __shared__ float s[DM];
  __shared__ float red[64][33];
  const int tid = threadIdx.x;
  const int kg = tid >> 2, c8 = tid & 3;
  u32x4 w[10];
#pragma unroll
  for (int i = 0; i < 10; ++i) w[i] = *(const u32x4*)(wmod + (size_t)(i * 64 + kg) * 3840 + blockIdx.x * 32 + c8 * 8);
  for (int i = tid; i < DM; i += 256) { float x = bf2f(c[i]); s[i] = rb(x / (1.f + __expf(-x))); }
  __syncthreads();
  float acc[8] = {0, 0, 0, 0, 0, 0, 0, 0};
#pragma unroll
  for (int i = 0; i < 10; ++i) {
    const float sv = s[i * 64 + kg];
#pragma unroll
    for (int j = 0; j < 4; ++j) { acc[2 * j] += sv * bflo(w[i][j]); acc[2 * j + 1] += sv * bfhi(w[i][j]); }
  }
#pragma unroll
  for (int j = 0; j < 8; ++j) red[kg][c8 * 8 + j] = acc[j];
  __syncthreads();
  if (tid < 32) {
    float t = 0;
    for (int i = 0; i < 64; ++i) t += red[i][tid];
    mod[blockIdx.x * 32 + tid] = rb(t);
  }
}

DI void ln_stats(const float (&x)[10], float& mean_b, float& den_b) {
  float s = 0;
#pragma unroll
  for (int i = 0; i < 10; ++i) s += x[i];
  s = wave_sum(s);
  const float mean = s * (1.f / DM);
  float v = 0;
#pragma unroll
  for (int i = 0; i < 10; ++i) { const float d = x[i] - mean; v += d * d; }
  v = wave_sum(v) * (1.f / DM);
  mean_b = rb(mean);
  den_b = rb(sqrtf(rb(rb(v) + 1e-5f)));
}

__global__ __launch_bounds__(256) void k_ln1(const u16* __restrict__ x, const float* __restrict__ mod, u16* __restrict__ hf) {
  const int lane = threadIdx.x & 63, row = blockIdx.x * 4 + (threadIdx.x >> 6);
  float v[10];
#pragma unroll
  for (int g = 0; g < 5; ++g) { unsigned w = *(const unsigned*)(x + (size_t)row * DM + g * 128 + 2 * lane); v[2 * g] = bflo(w); v[2 * g + 1] = bfhi(w); }
  float mean, den; ln_stats(v, mean, den);
#pragma unroll
  for (int g = 0; g < 5; ++g) {
    const int col = g * 128 + 2 * lane;
    const f32x2 sa = *(const f32x2*)(mod + col), ca = *(const f32x2*)(mod + DM + col);
    float o[2];
#pragma unroll
    for (int e = 0; e < 2; ++e) { float y = rb(rb(v[2 * g + e] - mean) / den); o[e] = rb(y * rb(1.f + ca[e])) + sa[e]; }
    const int ks = col >> 4, hh = (col >> 3) & 1, j = col & 7;
    *(unsigned*)(hf + ((size_t)((row >> 5) * 40 + ks) * 64 + hh * 32 + (row & 31)) * 8 + j) = cvtpk(o[0], o[1]);
  }
}

constexpr int KV_STAGE = 65536;
template <bool ISV>
DI void kv_main(unsigned ldsb, int h, int mt, int wid, int lane, srd_t sc, srd_t swk, srd_t swv, u16* __restrict__ outF) {
  const int wr = wid >> 1, wc = wid & 1;
  const int r = lane & 31, hh = lane >> 5;
  unsigned vc[2];
#pragma unroll
  for (int par = 0; par < 2; ++par) vc[par] = (lane >> 3) * 1024 + 16 * ((lane & 7) ^ (par * 4 + (lane >> 4)));
  const unsigned vw = ((lane >> 2) & 7) * 8192 + (lane >> 5) * 64 + (lane & 3) * 16;
  auto stage = [&](int t) {
    const unsigned base = ldsb + (t & 1) * KV_STAGE;
#pragma unroll
    for (int i = 0; i < 8; ++i) {
      const int p = wid * 8 + i;
      dma16(rfl(base + p * 1024), vc[i & 1], sc, rfl((unsigned)(mt * 256 * 1024 + p * 8192 + 128 * t)));
    }
#pragma unroll
    for (int i = 0; i < 8; ++i) {
      const int k8 = wid * 2 + (i >> 2), np = i & 3;
      dma16(rfl(base + 32768 + k8 * 4096 + np * 1024), vw, (np < 2) ? swk : swv, rfl((unsigned)((64 * t + 8 * k8) * 8192 + h * 256 + (np & 1) * 128)));
    }
  };
  unsigned ca[4];
#pragma unroll
  for (int ks = 0; ks < 4; ++ks) ca[ks] = ldsb + (wr * 128 + r) * 128 + (((2 * ks + hh) ^ ((r >> 1) & 7)) << 4);
  const unsigned wa = ldsb + 32768 + hh * 4096 + wc * 2048 + ((lane & 15) >> 2) * 64 + ((lane >> 4) & 1) * 32 + (lane & 3) * 8;
  f32x16 acc[4][4];
#pragma unroll
  for (int i = 0; i < 4; ++i)
#pragma unroll
    for (int j = 0; j < 4; ++j) acc[i][j] = f32x16{};
  stage(0); stage(1);
  for (int t = 0; t < 8; ++t) {
    if (t < 7) WAITV(16); else WAITV(0);
    BARRIER();
    const unsigned so = (t & 1) * KV_STAGE;
#pragma unroll
    for (int ks = 0; ks < 4; ++ks) {
      bf16x8 cf[4], wf[4];
#pragma unroll
      for (int i = 0; i < 4; ++i) cf[i] = lds_rd128(ca[ks] + so + i * 4096);
#pragma unroll
      for (int j = 0; j < 4; ++j) wf[j] = cat8(lds_tr(wa + so + ks * 8192 + j * 512), lds_tr(wa + so + ks * 8192 + j * 512 + 256));
#pragma unroll
      for (int i = 0; i < 4; ++i)
#pragma unroll
        for (int j = 0; j < 4; ++j) acc[i][j] = ISV ? MFMA32(cf[i], wf[j], acc[i][j]) : MFMA32(wf[j], cf[i], acc[i][j]);
    }
    BARRIER();
    if (t + 2 < 8) stage(t + 2);
  }
  unsigned lane2; asm volatile("v_mbcnt_lo_u32_b32 %0, -1, 0\n\tv_mbcnt_hi_u32_b32 %0, -1, %0" : "=v"(lane2));
#pragma unroll
  for (int i = 0; i < 4; ++i) {
    const int rbk = mt * 8 + wr * 4 + i;
    const int T = rbk >> 1, kb = rbk & 1;
    u16* base = outF + ((size_t)(h * 32 + T) * 16) * 512 + lane2 * 8;
#pragma unroll
    for (int j = 0; j < 4; ++j)
#pragma unroll
      for (int s = 0; s < 2; ++s) {
        const int f = ISV ? ((2 * kb + s) * 4 + j) : (kb * 8 + 2 * j + s);
        *(u32x4*)(base + f * 512) = pack8(acc[i][j], s);
        __builtin_amdgcn_sched_barrier(0);
      }
  }
}
__global__ __launch_bounds__(256, 1) void k_kv(const u16* __restrict__ ckv, const u16* __restrict__ wuk, const u16* __restrict__ wuv,
                                               u16* __restrict__ KF, u16* __restrict__ VF) {
  extern __shared__ __attribute__((aligned(16))) char smem[];
  const int bid = blockIdx.x, xcd = bid & 7, idx = bid >> 3;
  const int h = xcd * 4 + (idx >> 3), mt = idx & 7;
  const int wid = rfl(threadIdx.x >> 6), lane = threadIdx.x & 63;
  const unsigned ldsb = (unsigned)(uintptr_t)smem;
  srd_t sc = mk_srd(ckv, SEQ * DC * 2), swk = mk_srd(wuk, DC * 4096 * 2), swv = mk_srd(wuv, DC * 4096 * 2);
  if (wid & 1) kv_main<true>(ldsb, h, mt, wid, lane, sc, swk, swv, VF);
  else         kv_main<false>(ldsb, h, mt, wid, lane, sc, swk, swv, KF);
}

constexpr int AT_SLOT = 65536, AT_QB = 131072, AT_LDS = 163840;
__global__ __launch_bounds__(256, 1) void k_attn(const u16* __restrict__ hf, const u16* __restrict__ wq, const u16* __restrict__ KF,
                                                 const u16* __restrict__ VF, u16* __restrict__ ctx) {
  extern __shared__ __attribute__((aligned(16))) char smem[];
  const int bid = blockIdx.x, xcd = bid & 7, idx = bid >> 3;
  const int h = xcd * 4 + (idx >> 3), xq = idx & 7;
  const int wid = rfl(threadIdx.x >> 6), lane = threadIdx.x & 63;
  const int hh = lane >> 5, l32 = lane & 31;
  const unsigned ldsb = (unsigned)(uintptr_t)smem;
  {
    srd_t swq = mk_srd(wq, DM * 4096 * 2);
    const unsigned vw = ((lane >> 2) & 7) * 8192 + (lane >> 5) * 64 + (lane & 3) * 16;
    auto stage = [&](int t) {
      const unsigned base = ldsb + (t & 3) * 16384;
#pragma unroll
      for (int i = 0; i < 4; ++i) {
        const int k8 = wid * 2 + (i >> 1), np = i & 1;
        dma16(rfl(base + k8 * 2048 + np * 1024), vw, swq, rfl((unsigned)((64 * t + 8 * k8) * 8192 + h * 256 + np * 128)));
      }
    };
    const int rowbase = ((wid < 2) ? xq : 15 - xq) * 128 + (wid & 1) * 64;
    const u16* hp = hf + ((size_t)(rowbase >> 5) * 40 * 64 + lane) * 8;
    const unsigned wa = ldsb + hh * 2048 + ((lane & 15) >> 2) * 64 + ((lane >> 4) & 1) * 32 + (lane & 3) * 8;
    f32x16 acc[2][4];
#pragma unroll
    for (int i = 0; i < 2; ++i)
#pragma unroll
      for (int j = 0; j < 4; ++j) acc[i][j] = f32x16{};
    stage(0); stage(1); stage(2);
    for (int t = 0; t < 10; ++t) {
      bf16x8 hfr[2][4];
#pragma unroll
      for (int i = 0; i < 2; ++i)
#pragma unroll
        for (int ks = 0; ks < 4; ++ks) hfr[i][ks] = *(const bf16x8*)(hp + (size_t)(i * 40 + t * 4 + ks) * 512);
      WAITV(0);
      BARRIER();
      if (t + 3 < 10) stage(t + 3);
      const unsigned so = (t & 3) * 16384;
#pragma unroll
      for (int ks = 0; ks < 4; ++ks) {
        bf16x8 wf[4];
#pragma unroll
        for (int j = 0; j < 4; ++j) wf[j] = cat8(lds_tr(wa + so + ks * 4096 + j * 512), lds_tr(wa + so + ks * 4096 + j * 512 + 256));
#pragma unroll
        for (int i = 0; i < 2; ++i)
#pragma unroll
          for (int j = 0; j < 4; ++j) acc[i][j] = MFMA32(wf[j], hfr[i][ks], acc[i][j]);
      }
    }
    BARRIER();
    const unsigned qdst = ldsb + ((wid < 2) ? AT_SLOT : AT_QB) + (wid & 1) * 16384 + lane * 16;
#pragma unroll
    for (int i = 0; i < 2; ++i)
#pragma unroll
      for (int j = 0; j < 4; ++j) {
        f32x16 a = acc[i][j];
#pragma unroll
        for (int s = 0; s < 2; ++s) *(LAS u32x4*)(uintptr_t)(qdst + (i * 8 + 2 * j + s) * 1024) = pack8(a, s);
      }
    BARRIER();
  }
  const int rr = wid & 1, sp = wid >> 1;
  srd_t sk = mk_srd(KF + (size_t)h * 32 * 8192, 32 * 16384), sv = mk_srd(VF + (size_t)h * 32 * 8192, 32 * 16384);
  const unsigned vl = lane * 16;
  auto stage_kv = [&](int step) {
    const unsigned base = ldsb + (step & 1) * AT_SLOT;
#pragma unroll
    for (int i = 0; i < 8; ++i) { const int p = wid * 8 + i; dma16(rfl(base + p * 1024), vl, sk, rfl((unsigned)(step * 32768 + p * 1024))); }
#pragma unroll
    for (int i = 0; i < 8; ++i) { const int p = wid * 8 + i; dma16(rfl(base + 32768 + p * 1024), vl, sv, rfl((unsigned)(step * 32768 + p * 1024))); }
  };
  for (int pass = 0; pass < 2; ++pass) {
    const int qblk = pass ? 15 - xq : xq, nstep = qblk + 1;
    bf16x8 q[2][8];
    {
      const unsigned qsrc = ldsb + (pass ? AT_QB : AT_SLOT) + rr * 16384 + lane * 16;
#pragma unroll
      for (int i = 0; i < 2; ++i)
#pragma unroll
        for (int d0 = 0; d0 < 8; ++d0) q[i][d0] = lds_rd128(qsrc + (i * 8 + d0) * 1024);
    }
    stage_kv(0);
    f32x16 o[2][4];
#pragma unroll
    for (int i = 0; i < 2; ++i)
#pragma unroll
      for (int j = 0; j < 4; ++j) o[i][j] = f32x16{};
    float m[2] = {-1e30f, -1e30f}, l[2] = {0.f, 0.f};
    for (int step = 0; step < nstep; ++step) {
      WAITV(0);
      BARRIER();
      if (step + 1 < nstep) stage_kv(step + 1);
      const unsigned kb_ = ldsb + (step & 1) * AT_SLOT + sp * 16384 + lane * 16;
      const unsigned vb_ = kb_ + 32768;
      f32x16 s[2][2];
#pragma unroll
      for (int kb = 0; kb < 2; ++kb) {
        s[kb][0] = f32x16{}; s[kb][1] = f32x16{};
#pragma unroll
        for (int d0 = 0; d0 < 8; ++d0) {
          const bf16x8 kf = lds_rd128(kb_ + (kb * 8 + d0) * 1024);
          s[kb][0] = MFMA32(kf, q[0][d0], s[kb][0]);
          s[kb][1] = MFMA32(kf, q[1][d0], s[kb][1]);
        }
      }
#pragma unroll
      for (int kb = 0; kb < 2; ++kb)
#pragma unroll
        for (int qb = 0; qb < 2; ++qb)
#pragma unroll
          for (int r = 0; r < 16; ++r) s[kb][qb][r] = rbh(rbh(s[kb][qb][r]) * 0.08838834764831845f) * 1.4426950408889634f;
      if (step == nstep - 1) {
#pragma unroll
        for (int kb = 0; kb < 2; ++kb)
#pragma unroll
          for (int qb = 0; qb < 2; ++qb) {
            const int lim = 64 * rr + 32 * qb + l32 - 64 * sp - 32 * kb - 4 * hh;
#pragma unroll
            for (int r = 0; r < 16; ++r) if ((r & 3) + 8 * (r >> 2) > lim) s[kb][qb][r] = -__builtin_inff();
          }
      }
#pragma unroll
      for (int qb = 0; qb < 2; ++qb) {
        float mx = s[0][qb][0];
#pragma unroll
        for (int r = 1; r < 16; ++r) mx = fmaxf(mx, s[0][qb][r]);
#pragma unroll
        for (int r = 0; r < 16; ++r) mx = fmaxf(mx, s[1][qb][r]);
        mx = swap32_max(mx);
        const float mn = fmaxf(m[qb], mx), al = __builtin_amdgcn_exp2f(m[qb] - mn);
        m[qb] = mn;
        float ps = 0;
#pragma unroll
        for (int kb = 0; kb < 2; ++kb)
#pragma unroll
          for (int r = 0; r < 16; ++r) { const float p = __builtin_amdgcn_exp2f(s[kb][qb][r] - mn); s[kb][qb][r] = p; ps += p; }
        l[qb] = l[qb] * al + ps;
#pragma unroll
        for (int j = 0; j < 4; ++j) o[qb][j] *= al;
      }
#pragma unroll
      for (int ks = 0; ks < 4; ++ks) {
        const bf16x8 p0 = __builtin_bit_cast(bf16x8, pack8(s[ks >> 1][0], ks & 1)), p1 = __builtin_bit_cast(bf16x8, pack8(s[ks >> 1][1], ks & 1));
#pragma unroll
        for (int db = 0; db < 4; ++db) {
          const bf16x8 vf = lds_rd128(vb_ + (ks * 4 + db) * 1024);
          o[0][db] = MFMA32(vf, p0, o[0][db]);
          o[1][db] = MFMA32(vf, p1, o[1][db]);
        }
      }
    }
    l[0] = swap32_sum(l[0]); l[1] = swap32_sum(l[1]);
    BARRIER();
    {
      const unsigned mine = ldsb + wid * 17408 + lane * 16, other = ldsb + (wid ^ 2) * 17408 + lane * 16;
      const float ms = sp ? m[0] : m[1], ls = sp ? l[0] : l[1];
#pragma unroll
      for (int db = 0; db < 4; ++db)
#pragma unroll
        for (int g = 0; g < 4; ++g) {
          f32x4 v;
#pragma unroll
          for (int e = 0; e < 4; ++e) v[e] = sp ? o[0][db][4 * g + e] : o[1][db][4 * g + e];
          *(LAS f32x4*)(uintptr_t)(mine + (db * 4 + g) * 1024) = v;
        }
      *(LAS f32x4*)(uintptr_t)(mine + 16384) = f32x4{ms, ls, 0.f, 0.f};
      BARRIER();
      const f32x4 st = *(const LAS f32x4*)(uintptr_t)(other + 16384);
      const float mk = sp ? m[1] : m[0], lk = sp ? l[1] : l[0];
      const float mt = fmaxf(mk, st[0]);
      const float w0 = __builtin_amdgcn_exp2f(mk - mt), w1 = __builtin_amdgcn_exp2f(st[0] - mt);
      const float inv = 1.f / (lk * w0 + st[1] * w1);
      const float f0 = w0 * inv, f1 = w1 * inv;
      const int rbq = qblk * 4 + rr * 2 + sp;
      u16* cb = ctx + ((size_t)(rbq * 256 + h * 8) * 64 + lane) * 8;
#pragma unroll
      for (int db = 0; db < 4; ++db) {
        f32x16 a;
#pragma unroll
        for (int g = 0; g < 4; ++g) {
          const f32x4 v = *(const LAS f32x4*)(uintptr_t)(other + (db * 4 + g) * 1024);
#pragma unroll
          for (int e = 0; e < 4; ++e) a[4 * g + e] = (sp ? o[1][db][4 * g + e] : o[0][db][4 * g + e]) * f0 + v[e] * f1;
        }
        *(u32x4*)(cb + (2 * db + 0) * 512) = pack8(a, 0);
        *(u32x4*)(cb + (2 * db + 1) * 512) = pack8(a, 1);
      }
      BARRIER();
    }
  }
}

constexpr int OP_STAGE = 40960, OP_LDS = 3 * OP_STAGE;
__global__ __launch_bounds__(256, 1) void k_oproj(const u16* __restrict__ ctx, const u16* __restrict__ wo, u16* __restrict__ part) {
  extern __shared__ __attribute__((aligned(16))) char smem[];
  const int bid = blockIdx.x, xcd = bid & 7, idx = bid >> 3;
  const int ksp = xcd & 3, mt = (xcd >> 2) * 8 + (idx >> 2), nt = idx & 3;
  const int wid = rfl(threadIdx.x >> 6), lane = threadIdx.x & 63;
  const int wm = wid & 1, wk = wid >> 1, hh = lane >> 5, l32 = lane & 31;
  const unsigned ldsb = (unsigned)(uintptr_t)smem;
  srd_t swo = mk_srd(wo, 4096 * DM * 2);
  const unsigned vw = (lane >> 2) * 1280 + (lane & 3) * 16;
  auto stage = [&](int t) {
    const unsigned base = ldsb + (t % 3) * OP_STAGE;
#pragma unroll
    for (int i = 0; i < 10; ++i) {
      const int p = wid * 10 + i, pk = p / 20, k16 = (p % 20) / 5, nb = p % 5;
      dma16(rfl(base + p * 1024), vw, swo, rfl((unsigned)((ksp * 1024 + pk * 512 + t * 64 + k16 * 16) * 1280 + (nt * 160 + nb * 32) * 2)));
    }
  };
  const int rb0 = mt * 4 + wm * 2, kst0 = ksp * 64 + wk * 32;
  const u16* ap = ctx + ((size_t)(rb0 * 256 + kst0) * 64 + lane) * 8;
  const unsigned wa = ldsb + wk * 20480 + (hh * 4 + ((lane & 15) >> 2)) * 64 + ((lane >> 4) & 1) * 32 + (lane & 3) * 8;
  f32x16 acc[2][5];
#pragma unroll
  for (int i = 0; i < 2; ++i)
#pragma unroll
    for (int j = 0; j < 5; ++j) acc[i][j] = f32x16{};
  stage(0); stage(1);
  for (int t = 0; t < 8; ++t) {
    bf16x8 af[2][4];
#pragma unroll
    for (int i = 0; i < 2; ++i)
#pragma unroll
      for (int ks = 0; ks < 4; ++ks) af[i][ks] = *(const bf16x8*)(ap + (size_t)(i * 256 + t * 4 + ks) * 512);
    WAITV(0);
    BARRIER();
    if (t + 2 < 8) stage(t + 2);
    const unsigned so = (t % 3) * OP_STAGE;
#pragma unroll
    for (int ks = 0; ks < 4; ++ks) {
      bf16x8 wf[5];
#pragma unroll
      for (int j = 0; j < 5; ++j) wf[j] = cat8(lds_tr(wa + so + (ks * 5 + j) * 1024), lds_tr(wa + so + (ks * 5 + j) * 1024 + 512));
#pragma unroll
      for (int i = 0; i < 2; ++i)
#pragma unroll
        for (int j = 0; j < 5; ++j) acc[i][j] = MFMA32(wf[j], af[i][ks], acc[i][j]);
    }
  }
  BARRIER();
  {
    const unsigned mine = ldsb + wid * 20480 + lane * 16, other = ldsb + (wid ^ 2) * 20480 + lane * 16;
#pragma unroll
    for (int j = 0; j < 5; ++j)
#pragma unroll
      for (int g = 0; g < 4; ++g) {
        f32x4 v;
#pragma unroll
        for (int e = 0; e < 4; ++e) v[e] = wk ? acc[0][j][4 * g + e] : acc[1][j][4 * g + e];
        *(LAS f32x4*)(uintptr_t)(mine + (j * 4 + g) * 1024) = v;
      }
    BARRIER();
    const int row = (rb0 + wk) * 32 + l32;
    u16* pb = part + ((size_t)ksp * SEQ + row) * DM + nt * 160 + hh * 8;
#pragma unroll
    for (int j = 0; j < 5; ++j) {
      unsigned w[8];
#pragma unroll
      for (int g = 0; g < 4; ++g) {
        const f32x4 v = *(const LAS f32x4*)(uintptr_t)(other + (j * 4 + g) * 1024);
        float a[4];
#pragma unroll
        for (int e = 0; e < 4; ++e) a[e] = (wk ? acc[1][j][4 * g + e] : acc[0][j][4 * g + e]) + v[e];
        w[2 * g] = cvtpk(a[0], a[1]); w[2 * g + 1] = cvtpk(a[2], a[3]);
      }
#pragma unroll
      for (int pr = 0; pr < 2; ++pr) {
        auto r0 = __builtin_amdgcn_permlane32_swap(w[4 * pr + 0], w[4 * pr + 2], false, false);
        auto r1 = __builtin_amdgcn_permlane32_swap(w[4 * pr + 1], w[4 * pr + 3], false, false);
        *(u32x4*)(pb + j * 32 + pr * 16) = u32x4{r0[0], r1[0], r0[1], r1[1]};
      }
    }
  }
}

__global__ __launch_bounds__(256) void k_ln2(const u16* __restrict__ x, const u16* __restrict__ part, const float* __restrict__ mod,
                                             const u16* __restrict__ wr, u16* __restrict__ x1o, u16* __restrict__ h2o,
                                             float* __restrict__ ro, u16* __restrict__ out) {
  const int lane = threadIdx.x & 63, row = blockIdx.x * 4 + (threadIdx.x >> 6);
  float v[10];
#pragma unroll
  for (int g = 0; g < 5; ++g) {
    const int col = g * 128 + 2 * lane;
    float a0 = 0, a1 = 0;
#pragma unroll
    for (int p = 0; p < 4; ++p) { unsigned w = *(const unsigned*)(part + ((size_t)p * SEQ + row) * DM + col); a0 += bflo(w); a1 += bfhi(w); }
    const unsigned xw = *(const unsigned*)(x + (size_t)row * DM + col);
    const f32x2 ga = *(const f32x2*)(mod + 2 * DM + col);
    v[2 * g] = rb(bflo(xw) + rb(ga[0] * rb(a0)));
    v[2 * g + 1] = rb(bfhi(xw) + rb(ga[1] * rb(a1)));
  }
  float mean, den; ln_stats(v, mean, den);
  float rs = 0;
#pragma unroll
  for (int g = 0; g < 5; ++g) {
    const int col = g * 128 + 2 * lane;
    const f32x2 sb = *(const f32x2*)(mod + 3 * DM + col), cb = *(const f32x2*)(mod + 4 * DM + col), gb = *(const f32x2*)(mod + 5 * DM + col);
    const unsigned ww = *(const unsigned*)(wr + col);
    float hv[2], ov[2];
#pragma unroll
    for (int e = 0; e < 2; ++e) {
      const float y = rb(rb(v[2 * g + e] - mean) / den);
      hv[e] = rb(rb(y * rb(1.f + cb[e])) + sb[e]);
      ov[e] = v[2 * g + e] + rb(gb[e] * hv[e]);
    }
    rs += hv[0] * bflo(ww) + hv[1] * bfhi(ww);
    *(unsigned*)(x1o + (size_t)row * DM + col) = cvtpk(v[2 * g], v[2 * g + 1]);
    *(unsigned*)(h2o + (size_t)row * DM + col) = cvtpk(hv[0], hv[1]);
    *(unsigned*)(out + (size_t)row * DM + col) = cvtpk(ov[0], ov[1]);
  }
  rs = wave_sum(rs);
  if (lane == 0) ro[row] = rb(rs);
}

DI void top8(const float* __restrict__ r, int* sel, unsigned* cand) {
  const int tid = threadIdx.x, lane = tid & 63, wid = tid >> 6;
  unsigned key[8];
  const f32x4 a = *(const f32x4*)(r + tid * 8), b = *(const f32x4*)(r + tid * 8 + 4);
#pragma unroll
  for (int i = 0; i < 8; ++i) {
    const unsigned u = __float_as_uint(i < 4 ? a[i & 3] : b[i & 3]) >> 16;
    const unsigned o = (u & 0x8000u) ? (~u & 0xffffu) : (u | 0x8000u);
    key[i] = (o << 11) | (2047u - (unsigned)(tid * 8 + i));
    key[i] += 1u;
  }
  for (int it = 0; it < 8; ++it) {
    unsigned mx = key[0];
#pragma unroll
    for (int i = 1; i < 8; ++i) mx = max(mx, key[i]);
    unsigned wm = mx;
#pragma unroll
    for (int o = 32; o > 0; o >>= 1) wm = max(wm, (unsigned)__shfl_xor((int)wm, o));
#pragma unroll
    for (int i = 0; i < 8; ++i) if (key[i] == wm) key[i] = 0u;
    if (lane == 0) cand[wid * 8 + it] = wm;
  }
  __syncthreads();
  if (tid < 32) {
    const unsigned k = cand[tid];
    int rank = 0;
    for (int i = 0; i < 32; ++i) rank += (cand[i] > k) ? 1 : 0;
    if (rank < 8) sel[rank] = 2047 - (int)((k - 1u) & 2047u);
  }
  __syncthreads();
}

__global__ __launch_bounds__(256, 1) void k_ffn1(const float* __restrict__ r, const u16* __restrict__ h2, const u16* __restrict__ wg,
                                                 const u16* __restrict__ wu, u16* __restrict__ act, int* __restrict__ idxo) {
  extern __shared__ __attribute__((aligned(16))) char smem[];
  const int wid = rfl(threadIdx.x >> 6), lane = threadIdx.x & 63, hh = lane >> 5, l32 = lane & 31, tid = threadIdx.x;
  const unsigned ldsb = (unsigned)(uintptr_t)smem;
  srd_t sg = mk_srd(wg, DM * FF * 2), su = mk_srd(wu, DM * FF * 2);
  const unsigned vw = (lane >> 2) * (FF * 2) + (lane & 3) * 16;
#pragma unroll
  for (int i = 0; i < 10; ++i) {
    const int k16 = wid * 10 + i;
    dma16(rfl(ldsb + k16 * 1024), vw, sg, rfl((unsigned)(k16 * 16 * FF * 2 + blockIdx.x * 64)));
    dma16(rfl(ldsb + 40960 + k16 * 1024), vw, su, rfl((unsigned)(k16 * 16 * FF * 2 + blockIdx.x * 64)));
  }
  float* red = (float*)(smem + 81920);
  int* sel = (int*)(smem + 81920 + 8192);
  unsigned* cand = (unsigned*)(smem + 81920 + 8192 + 64);
  top8(r, sel, cand);
  if (blockIdx.x == 0 && tid < 8) idxo[tid] = sel[tid];
  const int tok = sel[l32 & 7];
  bf16x8 xf[10];
#pragma unroll
  for (int i = 0; i < 10; ++i) {
    bf16x8 v = *(const bf16x8*)(h2 + (size_t)tok * DM + (wid * 10 + i) * 16 + hh * 8);
    xf[i] = v;
  }
  WAITV(0);
  const unsigned wa = ldsb + (hh * 8 + ((lane & 15) >> 2)) * 64 + ((lane >> 4) & 1) * 32 + (lane & 3) * 8 + wid * 10240;
  f32x16 ag = f32x16{}, au = f32x16{};
#pragma unroll
  for (int i = 0; i < 10; ++i) {
    ag = MFMA32(cat8(lds_tr(wa + i * 1024), lds_tr(wa + i * 1024 + 256)), xf[i], ag);
    au = MFMA32(cat8(lds_tr(wa + 40960 + i * 1024), lds_tr(wa + 40960 + i * 1024 + 256)), xf[i], au);
  }
  if (l32 < 8) {
#pragma unroll
    for (int e = 0; e < 16; ++e) {
      red[((wid * 2 + 0) * 32 + e * 2 + hh) * 8 + l32] = ag[e];
      red[((wid * 2 + 1) * 32 + e * 2 + hh) * 8 + l32] = au[e];
    }
  }
  __syncthreads();
  {
    const int t = tid & 7, eh = tid >> 3, e = eh >> 1, h2_ = eh & 1;
    float g = 0, u = 0;
#pragma unroll
    for (int w = 0; w < 4; ++w) { g += red[((w * 2 + 0) * 32 + eh) * 8 + t]; u += red[((w * 2 + 1) * 32 + eh) * 8 + t]; }
    g = rb(g); u = rb(u);
    const float sg_ = rb(g / (1.f + __expf(-g)));
    const int n = (e & 3) + 8 * (e >> 2) + 4 * h2_;
    act[(size_t)t * FF + blockIdx.x * 32 + n] = (u16)(cvtpk(sg_ * u, 0.f) & 0xffffu);
  }
}

__global__ __launch_bounds__(256, 1) void k_ffn2(const float* __restrict__ r, const int* __restrict__ idx, const u16* __restrict__ act,
                                                 const u16* __restrict__ wd, const u16* __restrict__ x1, const u16* __restrict__ h2,
                                                 const float* __restrict__ mod, u16* __restrict__ out) {
  extern __shared__ __attribute__((aligned(16))) char smem[];
  const int wid = rfl(threadIdx.x >> 6), lane = threadIdx.x & 63, hh = lane >> 5, l32 = lane & 31, tid = threadIdx.x;
  const unsigned ldsb = (unsigned)(uintptr_t)smem;
  srd_t sd = mk_srd(wd, DM * FF * 2);
  const unsigned vw = (lane >> 2) * (DM * 2) + (lane & 3) * 16;
#pragma unroll
  for (int i = 0; i < 27; ++i) {
    const int k16 = wid * 27 + i;
    dma16(rfl(ldsb + k16 * 1024), vw, sd, rfl((unsigned)(k16 * 16 * DM * 2 + blockIdx.x * 64)));
  }
  float* red = (float*)(smem + 110592);
  bf16x8 xf[27];
#pragma unroll
  for (int i = 0; i < 27; ++i) xf[i] = *(const bf16x8*)(act + (size_t)(l32 & 7) * FF + (wid * 27 + i) * 16 + hh * 8);
  WAITV(0);
  const unsigned wa = ldsb + (hh * 8 + ((lane & 15) >> 2)) * 64 + ((lane >> 4) & 1) * 32 + (lane & 3) * 8 + wid * 27648;
  f32x16 a = f32x16{};
#pragma unroll
  for (int i = 0; i < 27; ++i) a = MFMA32(cat8(lds_tr(wa + i * 1024), lds_tr(wa + i * 1024 + 256)), xf[i], a);
  if (l32 < 8) {
#pragma unroll
    for (int e = 0; e < 16; ++e) red[(wid * 32 + e * 2 + hh) * 8 + l32] = a[e];
  }
  __syncthreads();
  {
    const int t = tid & 7, eh = tid >> 3, e = eh >> 1, h2_ = eh & 1;
    float y = 0;
#pragma unroll
    for (int w = 0; w < 4; ++w) y += red[(w * 32 + eh) * 8 + t];
    y = rb(y);
    const int row = idx[t], col = blockIdx.x * 32 + (e & 3) + 8 * (e >> 2) + 4 * h2_;
    const float rv = r[row];
    const float rs = rb(1.f / (1.f + __expf(-rv)));
    const float y2 = rb(y * rs);
    const float tt = rb(bf2f(h2[(size_t)row * DM + col]) + y2);
    const float o = bf2f(x1[(size_t)row * DM + col]) + rb(mod[5 * DM + col] * tt);
    out[(size_t)row * DM + col] = (u16)(cvtpk(o, 0.f) & 0xffffu);
  }
}

extern "C" void kernel_launch(void* const* d_in, const int* in_sizes, int n_in, void* d_out, int out_size, void* d_ws, size_t ws_size,
                              hipStream_t stream) {
  const u16* x = (const u16*)d_in[0];
  const u16* ckv = (const u16*)d_in[1];
  const u16* wq = (const u16*)d_in[2];
  const u16* wuk = (const u16*)d_in[3];
  const u16* wuv = (const u16*)d_in[4];
  const u16* wo = (const u16*)d_in[5];
  const u16* wr = (const u16*)d_in[7];
  const u16* wg = (const u16*)d_in[8];
  const u16* wu = (const u16*)d_in[9];
  const u16* wd = (const u16*)d_in[10];
  const u16* c = (const u16*)d_in[11];
  const u16* wmod = (const u16*)d_in[12];
  char* ws = (char*)d_ws;
  float* mod = (float*)(ws + WS_MOD);
  float* r = (float*)(ws + WS_R);
  int* idx = (int*)(ws + WS_IDX);
  u16* act = (u16*)(ws + WS_ACT);
  u16* hf = (u16*)(ws + WS_HF);
  u16* x1 = (u16*)(ws + WS_X1);
  u16* h2 = (u16*)(ws + WS_H2);
  u16* KF = (u16*)(ws + WS_KF);
  u16* VF = (u16*)(ws + WS_VF);
  u16* ctx = (u16*)(ws + WS_CTX);
  u16* part = (u16*)(ws + WS_PART);
  u16* out = (u16*)d_out;
  (void)hipFuncSetAttribute((const void*)k_kv, hipFuncAttributeMaxDynamicSharedMemorySize, 2 * KV_STAGE);
  (void)hipFuncSetAttribute((const void*)k_attn, hipFuncAttributeMaxDynamicSharedMemorySize, AT_LDS);
  (void)hipFuncSetAttribute((const void*)k_oproj, hipFuncAttributeMaxDynamicSharedMemorySize, OP_LDS);
  (void)hipFuncSetAttribute((const void*)k_ffn1, hipFuncAttributeMaxDynamicSharedMemorySize, 92160);
  (void)hipFuncSetAttribute((const void*)k_ffn2, hipFuncAttributeMaxDynamicSharedMemorySize, 116736);
  k_mod<<<120, 256, 0, stream>>>(c, wmod, mod);
  k_ln1<<<512, 256, 0, stream>>>(x, mod, hf);
  k_kv<<<256, 256, 2 * KV_STAGE, stream>>>(ckv, wuk, wuv, KF, VF);
  k_attn<<<256, 256, AT_LDS, stream>>>(hf, wq, KF, VF, ctx);
  k_oproj<<<256, 256, OP_LDS, stream>>>(ctx, wo, part);
  k_ln2<<<512, 256, 0, stream>>>(x, part, mod, wr, x1, h2, r, out);
  k_ffn1<<<54, 256, 92160, stream>>>(r, h2, wg, wu, act, idx);
  k_ffn2<<<20, 256, 116736, stream>>>(r, idx, act, wd, x1, h2, mod, out);
}
```

```cpp
#include <hip/hip_runtime.h>
#include <stdint.h>

typedef unsigned short u16;
typedef short bf16x8 __attribute__((ext_vector_type(8)));
typedef short s16x4 __attribute__((ext_vector_type(4)));
typedef float f32x16 __attribute__((ext_vector_type(16)));
typedef float f32x4 __attribute__((ext_vector_type(4)));
typedef float f32x2 __attribute__((ext_vector_type(2)));
typedef __bf16 bf16x2_t __attribute__((ext_vector_type(2)));
typedef unsigned u32x4 __attribute__((ext_vector_type(4)));
typedef unsigned u32x2 __attribute__((ext_vector_type(2)));
#define LAS __attribute__((address_space(3)))
#define GAS __attribute__((address_space(1)))
#define DI __device__ __forceinline__

constexpr int SEQ = 2048, DM = 640, NH = 32, HD = 128, DC = 512, FF = 1728, TOPK = 8;

constexpr size_t WS_FLG  = 65536;
constexpr size_t WS_HF   = 524288;
constexpr size_t WS_X1   = WS_HF + (size_t)SEQ * DM * 2;
constexpr size_t WS_H2   = WS_X1 + (size_t)SEQ * DM * 2;
constexpr size_t WS_KF   = WS_H2 + (size_t)SEQ * DM * 2;
constexpr size_t WS_VF   = WS_KF + (size_t)NH * SEQ * HD * 2;
constexpr size_t WS_CTX  = WS_VF + (size_t)NH * SEQ * HD * 2;
constexpr size_t WS_PART = WS_CTX + (size_t)NH * SEQ * HD * 2;

DI float bf2f(u16 v) { return __uint_as_float((unsigned)v << 16); }
DI float bflo(unsigned w) { return __uint_as_float(w << 16); }
DI float bfhi(unsigned w) { return __uint_as_float(w & 0xffff0000u); }
DI unsigned cvtpk(float lo, float hi) { f32x2 v = {lo, hi}; bf16x2_t b = __builtin_convertvector(v, bf16x2_t); return __builtin_bit_cast(unsigned, b); }
DI float rb(float x) { return bflo(cvtpk(x, x)); }
DI float rbh(float x) { return __uint_as_float(cvtpk(0.f, x)); }
DI unsigned rfl(unsigned x) { return __builtin_amdgcn_readfirstlane(x); }
#define MFMA32(a, b, c) __builtin_amdgcn_mfma_f32_32x32x16_bf16((a), (b), (c), 0, 0, 0)
#define SRD3 0x00020000

typedef __amdgpu_buffer_rsrc_t srd_t;
DI srd_t mk_srd(const void* p, unsigned bytes) { return __builtin_amdgcn_make_buffer_rsrc((void*)p, (short)0, (int)bytes, SRD3); }
typedef short v4i16_t __attribute__((ext_vector_type(4)));
DI s16x4 lds_tr(unsigned addr) { return __builtin_amdgcn_ds_read_tr16_b64_v4i16((LAS v4i16_t*)(uintptr_t)addr); }
DI bf16x8 cat8(s16x4 a, s16x4 b) { return __builtin_shufflevector(a, b, 0, 1, 2, 3, 4, 5, 6, 7); }
#define WAITV(n) asm volatile("s_waitcnt vmcnt(" #n ")" ::: "memory")
#define BARRIER() asm volatile("s_waitcnt lgkmcnt(0)\n\ts_barrier" ::: "memory")
template <int CTRL, int RMASK> DI float dpp_add(float v) {
  return v + __int_as_float(__builtin_amdgcn_update_dpp(0, __float_as_int(v), CTRL, RMASK, 0xf, false));
}
DI float wave_sum(float v) {
  v = dpp_add<0x111, 0xf>(v); v = dpp_add<0x112, 0xf>(v); v = dpp_add<0x114, 0xf>(v); v = dpp_add<0x118, 0xf>(v);
  v = dpp_add<0x142, 0xa>(v); v = dpp_add<0x143, 0xc>(v);
  return __int_as_float(__builtin_amdgcn_readlane(__float_as_int(v), 63));
}
DI void ln_stats(const float (&x)[10], float& mean_b, float& den_b) {
  float s = 0;
#pragma unroll
  for (int i = 0; i < 10; ++i) s += x[i];
  s = wave_sum(s);
  const float mean = s * (1.f / DM);
  float v = 0;
#pragma unroll
  for (int i = 0; i < 10; ++i) { const float d = x[i] - mean; v += d * d; }
  v = wave_sum(v) * (1.f / DM);
  mean_b = rbh(mean);
  den_b = rbh(__builtin_amdgcn_sqrtf(rbh(rbh(v) + 1e-5f)));
}

DI u32x4 mk_srd4(const void* p, unsigned bytes) {
  const unsigned long long a = (unsigned long long)p;
  return u32x4{rfl((unsigned)a), rfl((unsigned)(a >> 32) & 0xffffu), rfl(bytes), (unsigned)SRD3};
}
typedef unsigned u32x16 __attribute__((ext_vector_type(16)));
typedef unsigned u32x8 __attribute__((ext_vector_type(8)));
constexpr unsigned FL_MG = 0;
constexpr unsigned FL_H = 16384;
constexpr unsigned FL_P = 17408;
constexpr unsigned FL_SEL = 18432;
constexpr unsigned FL_EPOCH = 18688;
constexpr unsigned TAG_STEP = 0x9e3779b9u;
constexpr unsigned FL_KV = 20480;
constexpr unsigned FL_C = 24576;
constexpr unsigned FL_R = 131072;
constexpr unsigned FL_X1 = 49152;
constexpr unsigned FL_ACT = 69632;
constexpr unsigned FL_H2 = 196608;
constexpr unsigned FL_BYTES = 196608 + 8 * 20480;
constexpr int M1_LDS = 163840;

struct M1Args {
  const u16 *x, *ckv, *wq, *wuk, *wuv, *c, *wmod, *wo, *wr, *wg, *wu, *wd;
  u16* out;
  char* ws;
};

DI void front_phase(const M1Args& A, unsigned& tag, int xcd, int idx, int wid, int lane) {
  const int h = xcd * 4 + (idx >> 3), xq = idx & 7, tid = wid * 64 + lane, bid_ = blockIdx.x;
  const int r = lane & 31, hh = lane >> 5, f = (r >> 1) & 7;
  const int wr = (wid >> 1) & 1, wc = (wid & 1) + 2 * (wid >> 2);
  const int sp = wid >> 2, g = sp ? 7 - wid : wid;
  const int rbl = h >> 2, rbg = ((rbl < 4) ? xq : 15 - xq) * 4 + (rbl & 3), row0 = rbg * 32 + (h & 3) * 8;
  u32x4 sx = mk_srd4(A.x, SEQ * DM * 2), sm = mk_srd4(A.wmod, DM * 3840 * 2), scv = mk_srd4(A.c, DM * 2);
  const unsigned chunk = wid * 8 + (lane >> 3);
  const unsigned vx1 = (row0 + wid) * 1280 + lane * 16, vx2 = (lane < 16) ? vx1 + 1024 : 0x7f000000u;
  const unsigned mcol = (idx < 30) ? (unsigned)(xcd * 480 + idx * 16 + (tid & 1) * 8) * 2 : 0x7f000000u;
  const unsigned vm = (tid >> 1) * 7680 + mcol, vcx = (tid >> 1) * 2;
  unsigned c0, c1, c2; u32x4 xb0, xb1, w0, w1, w2;
  asm volatile(
    "s_nop 4\n\tbuffer_load_dwordx4 %3, %12, %9, 0 offen\n\t"
    "buffer_load_dwordx4 %4, %12, %9, %15 offen\n\t"
    "buffer_load_dwordx4 %5, %12, %9, %16 offen\n\t"
    "buffer_load_ushort %0, %13, %10, 0 offen\n\t"
    "buffer_load_ushort %1, %13, %10, 0 offen offset:512\n\t"
    "buffer_load_ushort %2, %13, %10, 0 offen offset:1024\n\t"
    "buffer_load_dwordx4 %6, %11, %8, 0 offen\n\t"
    "buffer_load_dwordx4 %7, %14, %8, 0 offen"
    : "=&v"(c0), "=&v"(c1), "=&v"(c2), "=&v"(w0), "=&v"(w1), "=&v"(w2), "=&v"(xb0), "=&v"(xb1)
    : "s"(sx), "s"(sm), "s"(scv), "v"(vx1), "v"(vm), "v"(vcx), "v"(vx2), "s"(256u * 7680u), "s"(512u * 7680u) : "memory");
  const unsigned lp = ((lane & 15) >> 2) * 64 + ((lane >> 4) & 1) * 32 + (lane & 3) * 8;
  const unsigned bc = wr * 16384 + (r & 15) * 128 + ((hh ^ f) << 4);
  unsigned b0 = bc + (r >> 4) * 32768, b1 = bc + (1 - (r >> 4)) * 131072;
  unsigned bw = hh * 8192 + (wc >> 1) * 4096 + 2048 + (wc & 1) * 1024 + lp;
  unsigned vc0 = (lane >> 3) * 1024 + (((lane & 7) ^ (lane >> 4)) << 4), vc1 = (lane >> 3) * 1024 + (((lane & 7) ^ (4 + (lane >> 4))) << 4) + 8192 - 1024;
  const unsigned vw = ((lane >> 2) & 7) * 8192 + (lane >> 5) * 64 + (lane & 3) * 16;
  unsigned vw0 = vw + 4096 - 2048, vw1 = vw + 128 + 4096 - 3072, doff = lane * 16;
  u32x4 sck = mk_srd4(A.ckv, SEQ * DC * 2);
  u32x4 sw = mk_srd4((const char*)((wid & 1) ? A.wuv : A.wuk) - 4096, DC * 4096 * 2 + 4096);
  unsigned coff = (xq * 256 + wid * 32) * 1024, woff = (8 * (wid >> 1)) * 8192 + h * 256, sbase = wid * 4096;
  asm volatile(
  "s_nop 4\n\t"
  "s_add_u32 m0, s66, 131072\n\t"
  "s_add_u32 s73, s64, 0\n\t"
  "buffer_load_dwordx4 v182, s[40:43], s73 offen lds\n\t"
  "buffer_load_dwordx4 v183, s[40:43], s73 offen offset:1024 lds\n\t"
  "s_add_u32 m0, s66, 0\n\t"
  "s_add_u32 s73, s64, 16384\n\t"
  "buffer_load_dwordx4 v182, s[40:43], s73 offen lds\n\t"
  "buffer_load_dwordx4 v183, s[40:43], s73 offen offset:1024 lds\n\t"
  "s_add_u32 m0, s66, 131072\n\t"
  "s_nop 0\n\t"
  "buffer_load_dwordx4 v184, s[44:47], s65 offen offset:2048 lds\n\t"
  "buffer_load_dwordx4 v185, s[44:47], s65 offen offset:3072 lds\n\t"
  "s_add_u32 s65, s65, 262144\n\t"
  "s_add_u32 m0, s66, 0\n\t"
  "s_nop 0\n\t"
  "buffer_load_dwordx4 v184, s[44:47], s65 offen offset:2048 lds\n\t"
  "buffer_load_dwordx4 v185, s[44:47], s65 offen offset:3072 lds\n\t"
  "s_add_u32 s65, s65, 262144\n\t"
  : "+{s64}"(coff), "+{s65}"(woff)
  : "{v182}"(vc0), "{v183}"(vc1), "{v184}"(vw0), "{v185}"(vw1), "{s[40:43]}"(sck), "{s[44:47]}"(sw), "{s66}"(sbase)
  : "s73", "scc", "memory");
  asm volatile("s_waitcnt vmcnt(8)" : "+v"(c0), "+v"(c1), "+v"(c2), "+v"(w0), "+v"(w1), "+v"(w2), "+v"(xb0), "+v"(xb1) :: "memory");
  LAS float* scr = (LAS float*)(uintptr_t)(3 * 32768);
  {
    const float s0 = bf2f((u16)c0), s1 = bf2f((u16)c1), s2 = bf2f((u16)c2);
    const float e0 = rbh(s0 / (1.f + __expf(-s0))), e1 = rbh(s1 / (1.f + __expf(-s1))), e2 = rbh(s2 / (1.f + __expf(-s2)));
    LAS float* dst = scr + (tid & 1) * 2048 + (tid >> 1);
#pragma unroll
    for (int j = 0; j < 4; ++j) {
      dst[(2 * j) * 256] = e0 * bflo(w0[j]) + e1 * bflo(w1[j]) + e2 * bflo(w2[j]);
      dst[(2 * j + 1) * 256] = e0 * bfhi(w0[j]) + e1 * bfhi(w1[j]) + e2 * bfhi(w2[j]);
    }
    float v[16];
#pragma unroll
    for (int j = 0; j < 4; ++j) { v[2 * j] = bflo(xb0[j]); v[2 * j + 1] = bfhi(xb0[j]); v[8 + 2 * j] = bflo(xb1[j]); v[9 + 2 * j] = bfhi(xb1[j]); }
    float s = 0;
#pragma unroll
    for (int j = 0; j < 16; ++j) s += v[j];
    const float mean = wave_sum(s) * (1.f / DM);
    float q0 = 0, q1 = 0;
#pragma unroll
    for (int j = 0; j < 8; ++j) { const float d0 = v[j] - mean, d1 = v[8 + j] - mean; q0 += d0 * d0; q1 += d1 * d1; }
    const float var = wave_sum(lane < 16 ? q0 + q1 : q0) * (1.f / DM);
    const float mb = rbh(mean), rden = __builtin_amdgcn_rcpf(rbh(sqrtf(rbh(rbh(var) + 1e-5f))));
    u32x4 y0, y1;
#pragma unroll
    for (int j = 0; j < 4; ++j) {
      y0[j] = cvtpk(rbh(v[2 * j] - mb) * rden, rbh(v[2 * j + 1] - mb) * rden);
      y1[j] = cvtpk(rbh(v[8 + 2 * j] - mb) * rden, rbh(v[9 + 2 * j] - mb) * rden);
    }
    *(LAS u32x4*)(uintptr_t)(3 * 32768 + 16384 + (lane * 8 + wid) * 16) = y0;
    if (lane < 16) *(LAS u32x4*)(uintptr_t)(3 * 32768 + 16384 + ((64 + lane) * 8 + wid) * 16) = y1;
  }
  BARRIER();
  asm volatile("s_add_u32 %0, %0, %1" : "+s"(tag) : "i"(TAG_STEP) : "scc");
  u32x4 sf = mk_srd4(A.ws + WS_FLG, FL_BYTES);
  u32x8 y;
  {
    const f32x4 p0 = *(const LAS f32x4*)(scr + (2 * wid) * 256 + lane * 4), p1 = *(const LAS f32x4*)(scr + (2 * wid + 1) * 256 + lane * 4);
    const u32x4 ya = *(const LAS u32x4*)(uintptr_t)(3 * 32768 + 16384 + tid * 16), yb = *(const LAS u32x4*)(uintptr_t)(3 * 32768 + 16384 + 8192 + (wid * 16 + (lane & 15)) * 16);
#pragma unroll
    for (int j = 0; j < 4; ++j) { y[j] = ya[j]; y[4 + j] = yb[j]; }
    float m0_ = rbh(wave_sum((p0[0] + p0[1]) + (p0[2] + p0[3]))), m1_ = rbh(wave_sum((p1[0] + p1[1]) + (p1[2] + p1[3])));
    const int col = xcd * 480 + idx * 16 + 2 * wid;
    if ((col >= DM && col < 2 * DM) || (col >= 4 * DM && col < 5 * DM)) { m0_ += 1.f; m1_ += 1.f; }
    if (idx < 30 && lane == 0)
      __hip_atomic_store((unsigned long long*)(A.ws + WS_FLG + FL_MG) + (col >> 1), ((unsigned long long)tag << 32) | cvtpk(m0_, m1_),
                         __ATOMIC_RELAXED, __HIP_MEMORY_SCOPE_AGENT);
  }
  unsigned pfb = (unsigned)((xq * 20 + (wid & 3) * 5) * 8192 + h * 256 + 4096);
  const unsigned isv = wid >> 2;
  u32x4 so = mk_srd4(A.ws + WS_KF, NH * SEQ * HD * 4);
  unsigned ooff = ((h * 32 + xq * 4 + wr * 2) * 16 + (isv ? 2 : 4) * (wc & 1)) * 1024 + isv * (NH * SEQ * HD * 2);
  unsigned ak0 = h * 524288 + wid * 4096, ak1 = ak0 + (xq ? 32768 : 0);
  unsigned qb0 = g * 4096 + lane * 16, qbw = hh * 8192 + sp * 4096 + 2048 + lp, vqw = vw + 4096 - 2048;
  unsigned vh = rbg * 40960 + chunk * 512 + ((h & 3) * 8 + (lane & 7)) * 16;
  const unsigned chunk1 = 64 + 2 * wid + (lane >> 3);
  unsigned vg = ((lane & 4) ? min(chunk1, 79u) : chunk) * 32 + ((lane >> 1) & 1) * 2560 + (lane & 1) * 16;
  unsigned vh1 = lane < 16 ? rbg * 40960 + chunk1 * 512 + ((h & 3) * 8 + (lane & 7)) * 16 : 0x7f000000u;
  u32x4 sh = mk_srd4(A.ws + WS_HF, SEQ * DM * 2), swq = mk_srd4((const char*)A.wq - 4096, DM * 4096 * 2 + 4096);
  unsigned hoff = (((wid < 4) ? xq : 15 - xq) * 4 + (wid & 3)) * 40960, qoff = (8 * (wid >> 1)) * 8192 + h * 256 + (wid & 1) * 128;
  unsigned hoff2 = ((15 - xq) * 4 + (wid & 3)) * 40960, hofl = (xq * 4 + (wid & 3)) * 40960;
  unsigned qoff2 = (8 * ((wid & 3) >> 1) + 16) * 8192 + h * 256 + (wid & 1) * 128;
  unsigned qdst = 98304 + g * 8192 + sp * 4096, hfl = FL_H + xq * 128 + h * 4, kfl = FL_KV + h * 128 + xq * 4, uw = wid;
  asm volatile(
  "s_nop 4\n\t"
  "s_mov_b32 s82, 0\n\t"
  "s_and_b32 s67, s66, 0x3fff\n\t"
  "s_sub_u32 s76, s79, 131072\n\t"
  "s_cmp_ge_u32 s69, 4\n\t"
  "s_cbranch_scc1 LFrolev_%=\n\t"
  "s_waitcnt vmcnt(2) lgkmcnt(0)\n\t"
  "s_barrier\n\t"
  "s_mov_b32 s68, 0\n\t"
  "v_add_u32 v178, 131072, v181\n\t"
  "v_xad_u32 v176, v180, 0, s68\n\t"
  "v_xor_b32 v177, 32, v176\n\t"
  "ds_read_b64_tr_b16 v[160:161], v178 offset:0\n\t"
  "ds_read_b64_tr_b16 v[162:163], v178 offset:256\n\t"
  "ds_read_b128 v[128:131], v176 offset:0\n\t"
  "ds_read_b64_tr_b16 v[164:165], v178 offset:512\n\t"
  "ds_read_b64_tr_b16 v[166:167], v178 offset:768\n\t"
  "ds_read_b128 v[132:135], v176 offset:4096\n\t"
  "ds_read_b128 v[136:139], v176 offset:8192\n\t"
  "ds_read_b128 v[140:143], v176 offset:12288\n\t"
  "s_waitcnt lgkmcnt(0)\n\t"
  "v_mfma_f32_32x32x16_bf16 v[0:15], v[160:163], v[128:131], 0\n\t"
  "ds_read_b64_tr_b16 v[168:169], v178 offset:16384\n\t"
  "ds_read_b64_tr_b16 v[170:171], v178 offset:16640\n\t"
  "v_mfma_f32_32x32x16_bf16 v[16:31], v[164:167], v[128:131], 0\n\t"
  "ds_read_b128 v[144:147], v177 offset:0\n\t"
  "ds_read_b64_tr_b16 v[172:173], v178 offset:16896\n\t"
  "v_mfma_f32_32x32x16_bf16 v[32:47], v[160:163], v[132:135], 0\n\t"
  "ds_read_b64_tr_b16 v[174:175], v178 offset:17152\n\t"
  "ds_read_b128 v[148:151], v177 offset:4096\n\t"
  "v_mfma_f32_32x32x16_bf16 v[48:63], v[164:167], v[132:135], 0\n\t"
  "ds_read_b128 v[152:155], v177 offset:8192\n\t"
  "ds_read_b128 v[156:159], v177 offset:12288\n\t"
  "v_mfma_f32_32x32x16_bf16 v[64:79], v[160:163], v[136:139], 0\n\t"
  "s_add_u32 m0, s66, 32768\n\t"
  "s_add_u32 s73, s64, 128\n\t"
  "buffer_load_dwordx4 v182, s[40:43], s73 offen lds\n\t"
  "v_mfma_f32_32x32x16_bf16 v[80:95], v[164:167], v[136:139], 0\n\t"
  "buffer_load_dwordx4 v183, s[40:43], s73 offen offset:1024 lds\n\t"
  "v_mfma_f32_32x32x16_bf16 v[96:111], v[160:163], v[140:143], 0\n\t"
  "s_add_u32 m0, s66, 65536\n\t"
  "s_add_u32 s73, s64, 16512\n\t"
  "buffer_load_dwordx4 v182, s[40:43], s73 offen lds\n\t"
  "v_mfma_f32_32x32x16_bf16 v[112:127], v[164:167], v[140:143], 0\n\t"
  "buffer_load_dwordx4 v183, s[40:43], s73 offen offset:1024 lds\n\t"
  "s_waitcnt vmcnt(4) lgkmcnt(0)\n\t"
  "s_barrier\n\t"
  "s_mov_b32 s68, 0\n\t"
  "v_add_u32 v178, 0, v181\n\t"
  "v_xad_u32 v176, v180, 64, s68\n\t"
  "v_xor_b32 v177, 32, v176\n\t"
  "v_mfma_f32_32x32x16_bf16 v[0:15], v[168:171], v[144:147], v[0:15]\n\t"
  "ds_read_b64_tr_b16 v[160:161], v178 offset:0\n\t"
  "ds_read_b64_tr_b16 v[162:163], v178 offset:256\n\t"
  "v_mfma_f32_32x32x16_bf16 v[16:31], v[172:175], v[144:147], v[16:31]\n\t"
  "ds_read_b128 v[128:131], v176 offset:0\n\t"
  "ds_read_b64_tr_b16 v[164:165], v178 offset:512\n\t"
  "v_mfma_f32_32x32x16_bf16 v[32:47], v[168:171], v[148:151], v[32:47]\n\t"
  "ds_read_b64_tr_b16 v[166:167], v178 offset:768\n\t"
  "ds_read_b128 v[132:135], v176 offset:4096\n\t"
  "v_mfma_f32_32x32x16_bf16 v[48:63], v[172:175], v[148:151], v[48:63]\n\t"
  "ds_read_b128 v[136:139], v176 offset:8192\n\t"
  "ds_read_b128 v[140:143], v176 offset:12288\n\t"
  "v_mfma_f32_32x32x16_bf16 v[64:79], v[168:171], v[152:155], v[64:79]\n\t"
  "s_add_u32 m0, s66, 32768\n\t"
  "s_nop 0\n\t"
  "buffer_load_dwordx4 v184, s[44:47], s65 offen offset:2048 lds\n\t"
  "v_mfma_f32_32x32x16_bf16 v[80:95], v[172:175], v[152:155], v[80:95]\n\t"
  "buffer_load_dwordx4 v185, s[44:47], s65 offen offset:3072 lds\n\t"
  "s_add_u32 s65, s65, 262144\n\t"
  "v_mfma_f32_32x32x16_bf16 v[96:111], v[168:171], v[156:159], v[96:111]\n\t"
  "s_add_u32 m0, s66, 65536\n\t"
  "s_nop 0\n\t"
  "buffer_load_dwordx4 v184, s[44:47], s65 offen offset:2048 lds\n\t"
  "v_mfma_f32_32x32x16_bf16 v[112:127], v[172:175], v[156:159], v[112:127]\n\t"
  "buffer_load_dwordx4 v185, s[44:47], s65 offen offset:3072 lds\n\t"
  "s_add_u32 s65, s65, 262144\n\t"
  "s_waitcnt lgkmcnt(0)\n\t"
  "v_mfma_f32_32x32x16_bf16 v[0:15], v[160:163], v[128:131], v[0:15]\n\t"
  "ds_read_b64_tr_b16 v[168:169], v178 offset:16384\n\t"
  "ds_read_b64_tr_b16 v[170:171], v178 offset:16640\n\t"
  "v_mfma_f32_32x32x16_bf16 v[16:31], v[164:167], v[128:131], v[16:31]\n\t"
  "ds_read_b128 v[144:147], v177 offset:0\n\t"
  "ds_read_b64_tr_b16 v[172:173], v178 offset:16896\n\t"
  "v_mfma_f32_32x32x16_bf16 v[32:47], v[160:163], v[132:135], v[32:47]\n\t"
  "ds_read_b64_tr_b16 v[174:175], v178 offset:17152\n\t"
  "ds_read_b128 v[148:151], v177 offset:4096\n\t"
  "v_mfma_f32_32x32x16_bf16 v[48:63], v[164:167], v[132:135], v[48:63]\n\t"
  "ds_read_b128 v[152:155], v177 offset:8192\n\t"
  "ds_read_b128 v[156:159], v177 offset:12288\n\t"
  "v_mfma_f32_32x32x16_bf16 v[64:79], v[160:163], v[136:139], v[64:79]\n\t"
  "s_add_u32 m0, s66, 98304\n\t"
  "s_add_u32 s73, s64, 256\n\t"
  "buffer_load_dwordx4 v182, s[40:43], s73 offen lds\n\t"
  "v_mfma_f32_32x32x16_bf16 v[80:95], v[164:167], v[136:139], v[80:95]\n\t"
  "buffer_load_dwordx4 v183, s[40:43], s73 offen offset:1024 lds\n\t"
  "v_mfma_f32_32x32x16_bf16 v[96:111], v[160:163], v[140:143], v[96:111]\n\t"
  "s_add_u32 m0, s66, 98304\n\t"
  "s_nop 0\n\t"
  "buffer_load_dwordx4 v184, s[44:47], s65 offen offset:2048 lds\n\t"
  "v_mfma_f32_32x32x16_bf16 v[112:127], v[164:167], v[140:143], v[112:127]\n\t"
  "buffer_load_dwordx4 v185, s[44:47], s65 offen offset:3072 lds\n\t"
  "s_add_u32 s65, s65, 262144\n\t"
  "s_waitcnt vmcnt(6) lgkmcnt(0)\n\t"
  "s_barrier\n\t"
  "s_mov_b32 s68, 32768\n\t"
  "v_add_u32 v178, 32768, v181\n\t"
  "v_xad_u32 v176, v179, 0, s68\n\t"
  "v_xor_b32 v177, 32, v176\n\t"
  "v_mfma_f32_32x32x16_bf16 v[0:15], v[168:171], v[144:147], v[0:15]\n\t"
  "ds_read_b64_tr_b16 v[160:161], v178 offset:0\n\t"
  "ds_read_b64_tr_b16 v[162:163], v178 offset:256\n\t"
  "v_mfma_f32_32x32x16_bf16 v[16:31], v[172:175], v[144:147], v[16:31]\n\t"
  "ds_read_b128 v[128:131], v176 offset:0\n\t"
  "ds_read_b64_tr_b16 v[164:165], v178 offset:512\n\t"
  "v_mfma_f32_32x32x16_bf16 v[32:47], v[168:171], v[148:151], v[32:47]\n\t"
  "ds_read_b64_tr_b16 v[166:167], v178 offset:768\n\t"
  "ds_read_b128 v[132:135], v176 offset:4096\n\t"
  "v_mfma_f32_32x32x16_bf16 v[48:63], v[172:175], v[148:151], v[48:63]\n\t"
  "ds_read_b128 v[136:139], v176 offset:8192\n\t"
  "ds_read_b128 v[140:143], v176 offset:12288\n\t"
  "v_mfma_f32_32x32x16_bf16 v[64:79], v[168:171], v[152:155], v[64:79]\n\t"
  "s_add_u32 m0, s66, 131072\n\t"
  "s_add_u32 s73, s64, 16640\n\t"
  "buffer_load_dwordx4 v182, s[40:43], s73 offen lds\n\t"
  "v_mfma_f32_32x32x16_bf16 v[80:95], v[172:175], v[152:155], v[80:95]\n\t"
  "buffer_load_dwordx4 v183, s[40:43], s73 offen offset:1024 lds\n\t"
  "v_mfma_f32_32x32x16_bf16 v[96:111], v[168:171], v[156:159], v[96:111]\n\t"
  "s_add_u32 m0, s66, 131072\n\t"
  "s_nop 0\n\t"
  "buffer_load_dwordx4 v184, s[44:47], s65 offen offset:2048 lds\n\t"
  "v_mfma_f32_32x32x16_bf16 v[112:127], v[172:175], v[156:159], v[112:127]\n\t"
  "buffer_load_dwordx4 v185, s[44:47], s65 offen offset:3072 lds\n\t"
  "s_add_u32 s65, s65, 262144\n\t"
  "s_waitcnt lgkmcnt(0)\n\t"
  "v_mfma_f32_32x32x16_bf16 v[0:15], v[160:163], v[128:131], v[0:15]\n\t"
  "ds_read_b64_tr_b16 v[168:169], v178 offset:16384\n\t"
  "ds_read_b64_tr_b16 v[170:171], v178 offset:16640\n\t"
  "v_mfma_f32_32x32x16_bf16 v[16:31], v[164:167], v[128:131], v[16:31]\n\t"
  "ds_read_b128 v[144:147], v177 offset:0\n\t"
  "ds_read_b64_tr_b16 v[172:173], v178 offset:16896\n\t"
  "v_mfma_f32_32x32x16_bf16 v[32:47], v[160:163], v[132:135], v[32:47]\n\t"
  "ds_read_b64_tr_b16 v[174:175], v178 offset:17152\n\t"
  "ds_read_b128 v[148:151], v177 offset:4096\n\t"
  "v_mfma_f32_32x32x16_bf16 v[48:63], v[164:167], v[132:135], v[48:63]\n\t"
  "ds_read_b128 v[152:155], v177 offset:8192\n\t"
  "ds_read_b128 v[156:159], v177 offset:12288\n\t"
  "s_add_u32 m0, s66, 0\n\t"
  "s_add_u32 s73, s64, 384\n\t"
  "buffer_load_dwordx4 v182, s[40:43], s73 offen lds\n\t"
  "v_mfma_f32_32x32x16_bf16 v[64:79], v[160:163], v[136:139], v[64:79]\n\t"
  "buffer_load_dwordx4 v183, s[40:43], s73 offen offset:1024 lds\n\t"
  "v_mfma_f32_32x32x16_bf16 v[80:95], v[164:167], v[136:139], v[80:95]\n\t"
  "s_add_u32 m0, s66, 0\n\t"
  "s_nop 0\n\t"
  "buffer_load_dwordx4 v184, s[44:47], s65 offen offset:2048 lds\n\t"
  "v_mfma_f32_32x32x16_bf16 v[96:111], v[160:163], v[140:143], v[96:111]\n\t"
  "buffer_load_dwordx4 v185, s[44:47], s65 offen offset:3072 lds\n\t"
  "s_add_u32 s65, s65, 262144\n\t"
  "v_mfma_f32_32x32x16_bf16 v[112:127], v[164:167], v[140:143], v[112:127]\n\t"
  "buffer_load_dwordx4 v[244:247], v190, s[60:63], 0 offen sc1\n\t"
  "s_waitcnt vmcnt(13) lgkmcnt(0)\n\t"
  "s_barrier\n\t"
  "s_mov_b32 s68, 32768\n\t"
  "v_add_u32 v178, 65536, v181\n\t"
  "v_xad_u32 v176, v179, 64, s68\n\t"
  "v_xor_b32 v177, 32, v176\n\t"
  "v_mfma_f32_32x32x16_bf16 v[0:15], v[168:171], v[144:147], v[0:15]\n\t"
  "ds_read_b64_tr_b16 v[160:161], v178 offset:0\n\t"
  "ds_read_b64_tr_b16 v[162:163], v178 offset:256\n\t"
  "v_mfma_f32_32x32x16_bf16 v[16:31], v[172:175], v[144:147], v[16:31]\n\t"
  "ds_read_b128 v[128:131], v176 offset:0\n\t"
  "ds_read_b64_tr_b16 v[164:165], v178 offset:512\n\t"
  "v_mfma_f32_32x32x16_bf16 v[32:47], v[168:171], v[148:151], v[32:47]\n\t"
  "ds_read_b64_tr_b16 v[166:167], v178 offset:768\n\t"
  "ds_read_b128 v[132:135], v176 offset:4096\n\t"
  "v_mfma_f32_32x32x16_bf16 v[48:63], v[172:175], v[148:151], v[48:63]\n\t"
  "ds_read_b128 v[136:139], v176 offset:8192\n\t"
  "ds_read_b128 v[140:143], v176 offset:12288\n\t"
  "v_mfma_f32_32x32x16_bf16 v[64:79], v[168:171], v[152:155], v[64:79]\n\t"
  "v_mfma_f32_32x32x16_bf16 v[80:95], v[172:175], v[152:155], v[80:95]\n\t"
  "v_mfma_f32_32x32x16_bf16 v[96:111], v[168:171], v[156:159], v[96:111]\n\t"
  "s_add_u32 m0, s66, 32768\n\t"
  "s_nop 0\n\t"
  "buffer_load_dwordx4 v184, s[44:47], s65 offen offset:2048 lds\n\t"
  "v_mfma_f32_32x32x16_bf16 v[112:127], v[172:175], v[156:159], v[112:127]\n\t"
  "buffer_load_dwordx4 v185, s[44:47], s65 offen offset:3072 lds\n\t"
  "s_add_u32 s65, s65, 262144\n\t"
  "s_waitcnt lgkmcnt(0)\n\t"
  "v_mfma_f32_32x32x16_bf16 v[0:15], v[160:163], v[128:131], v[0:15]\n\t"
  "ds_read_b64_tr_b16 v[168:169], v178 offset:16384\n\t"
  "ds_read_b64_tr_b16 v[170:171], v178 offset:16640\n\t"
  "v_mfma_f32_32x32x16_bf16 v[16:31], v[164:167], v[128:131], v[16:31]\n\t"
  "ds_read_b128 v[144:147], v177 offset:0\n\t"
  "ds_read_b64_tr_b16 v[172:173], v178 offset:16896\n\t"
  "v_mfma_f32_32x32x16_bf16 v[32:47], v[160:163], v[132:135], v[32:47]\n\t"
  "ds_read_b64_tr_b16 v[174:175], v178 offset:17152\n\t"
  "ds_read_b128 v[148:151], v177 offset:4096\n\t"
  "v_mfma_f32_32x32x16_bf16 v[48:63], v[164:167], v[132:135], v[48:63]\n\t"
  "ds_read_b128 v[152:155], v177 offset:8192\n\t"
  "ds_read_b128 v[156:159], v177 offset:12288\n\t"
  "v_mfma_f32_32x32x16_bf16 v[64:79], v[160:163], v[136:139], v[64:79]\n\t"
  "v_mfma_f32_32x32x16_bf16 v[80:95], v[164:167], v[136:139], v[80:95]\n\t"
  "v_mfma_f32_32x32x16_bf16 v[96:111], v[160:163], v[140:143], v[96:111]\n\t"
  "v_mfma_f32_32x32x16_bf16 v[112:127], v[164:167], v[140:143], v[112:127]\n\t"
  "s_waitcnt vmcnt(9) lgkmcnt(0)\n\t"
  "s_barrier\n\t"
  "s_mov_b32 s68, 98304\n\t"
  "v_add_u32 v178, 98304, v181\n\t"
  "v_xad_u32 v176, v179, 0, s68\n\t"
  "v_xor_b32 v177, 32, v176\n\t"
  "v_mfma_f32_32x32x16_bf16 v[0:15], v[168:171], v[144:147], v[0:15]\n\t"
  "ds_read_b64_tr_b16 v[160:161], v178 offset:0\n\t"
  "ds_read_b64_tr_b16 v[162:163], v178 offset:256\n\t"
  "v_mfma_f32_32x32x16_bf16 v[16:31], v[172:175], v[144:147], v[16:31]\n\t"
  "ds_read_b128 v[128:131], v176 offset:0\n\t"
  "ds_read_b64_tr_b16 v[164:165], v178 offset:512\n\t"
  "v_mfma_f32_32x32x16_bf16 v[32:47], v[168:171], v[148:151], v[32:47]\n\t"
  "ds_read_b64_tr_b16 v[166:167], v178 offset:768\n\t"
  "ds_read_b128 v[132:135], v176 offset:4096\n\t"
  "v_mfma_f32_32x32x16_bf16 v[48:63], v[172:175], v[148:151], v[48:63]\n\t"
  "ds_read_b128 v[136:139], v176 offset:8192\n\t"
  "ds_read_b128 v[140:143], v176 offset:12288\n\t"
  "v_mfma_f32_32x32x16_bf16 v[64:79], v[168:171], v[152:155], v[64:79]\n\t"
  "s_add_u32 m0, s66, 32768\n\t"
  "s_add_u32 s73, s64, 16768\n\t"
  "buffer_load_dwordx4 v182, s[40:43], s73 offen lds\n\t"
  "v_mfma_f32_32x32x16_bf16 v[80:95], v[172:175], v[152:155], v[80:95]\n\t"
  "buffer_load_dwordx4 v183, s[40:43], s73 offen offset:1024 lds\n\t"
  "v_mfma_f32_32x32x16_bf16 v[96:111], v[168:171], v[156:159], v[96:111]\n\t"
  "s_add_u32 m0, s66, 65536\n\t"
  "s_add_u32 s73, s64, 512\n\t"
  "buffer_load_dwordx4 v182, s[40:43], s73 offen lds\n\t"
  "v_mfma_f32_32x32x16_bf16 v[112:127], v[172:175], v[156:159], v[112:127]\n\t"
  "buffer_load_dwordx4 v183, s[40:43], s73 offen offset:1024 lds\n\t"
  "s_waitcnt lgkmcnt(0)\n\t"
  "v_mfma_f32_32x32x16_bf16 v[0:15], v[160:163], v[128:131], v[0:15]\n\t"
  "ds_read_b64_tr_b16 v[168:169], v178 offset:16384\n\t"
  "ds_read_b64_tr_b16 v[170:171], v178 offset:16640\n\t"
  "s_waitcnt vmcnt(6)\n\t"
  "LFgck_%=:\n\t"
  "v_xor_b32 v232, s77, v245\n\t"
  "v_xor_b32 v233, s77, v247\n\t"
  "v_or_b32 v232, v232, v233\n\t"
  "v_cmp_ne_u32 vcc, 0, v232\n\t"
  "s_cbranch_vccnz LFgsk_%=\n\t"
  "LFgok_%=:\n\t"
  "ds_swizzle_b32 v192, v244 offset:0x18\n\t"
  "ds_swizzle_b32 v194, v246 offset:0x18\n\t"
  "ds_swizzle_b32 v196, v244 offset:0x38\n\t"
  "v_mfma_f32_32x32x16_bf16 v[16:31], v[164:167], v[128:131], v[16:31]\n\t"
  "ds_read_b128 v[144:147], v177 offset:0\n\t"
  "ds_read_b64_tr_b16 v[172:173], v178 offset:16896\n\t"
  "ds_swizzle_b32 v198, v246 offset:0x38\n\t"
  "ds_swizzle_b32 v200, v244 offset:0x58\n\t"
  "ds_swizzle_b32 v202, v246 offset:0x58\n\t"
  "ds_swizzle_b32 v204, v244 offset:0x78\n\t"
  "v_mfma_f32_32x32x16_bf16 v[32:47], v[160:163], v[132:135], v[32:47]\n\t"
  "ds_read_b64_tr_b16 v[174:175], v178 offset:17152\n\t"
  "ds_read_b128 v[148:151], v177 offset:4096\n\t"
  "ds_swizzle_b32 v206, v246 offset:0x78\n\t"
  "ds_swizzle_b32 v208, v244 offset:0x98\n\t"
  "ds_swizzle_b32 v210, v246 offset:0x98\n\t"
  "ds_swizzle_b32 v212, v244 offset:0xb8\n\t"
  "v_mfma_f32_32x32x16_bf16 v[48:63], v[164:167], v[132:135], v[48:63]\n\t"
  "ds_read_b128 v[152:155], v177 offset:8192\n\t"
  "ds_read_b128 v[156:159], v177 offset:12288\n\t"
  "ds_swizzle_b32 v214, v246 offset:0xb8\n\t"
  "ds_swizzle_b32 v216, v244 offset:0xd8\n\t"
  "ds_swizzle_b32 v218, v246 offset:0xd8\n\t"
  "ds_swizzle_b32 v220, v244 offset:0xf8\n\t"
  "v_mfma_f32_32x32x16_bf16 v[64:79], v[160:163], v[136:139], v[64:79]\n\t"
  "ds_swizzle_b32 v222, v246 offset:0xf8\n\t"
  "v_mfma_f32_32x32x16_bf16 v[80:95], v[164:167], v[136:139], v[80:95]\n\t"
  "v_mfma_f32_32x32x16_bf16 v[96:111], v[160:163], v[140:143], v[96:111]\n\t"
  "s_add_u32 m0, s66, 65536\n\t"
  "s_nop 0\n\t"
  "buffer_load_dwordx4 v184, s[44:47], s65 offen offset:2048 lds\n\t"
  "v_mfma_f32_32x32x16_bf16 v[112:127], v[164:167], v[140:143], v[112:127]\n\t"
  "buffer_load_dwordx4 v185, s[44:47], s65 offen offset:3072 lds\n\t"
  "s_add_u32 s65, s65, 262144\n\t"
  "s_waitcnt lgkmcnt(0)\n\t"
  "s_barrier\n\t"
  "s_mov_b32 s68, 98304\n\t"
  "v_add_u32 v178, 131072, v181\n\t"
  "v_xad_u32 v176, v179, 64, s68\n\t"
  "v_xor_b32 v177, 32, v176\n\t"
  "v_mfma_f32_32x32x16_bf16 v[0:15], v[168:171], v[144:147], v[0:15]\n\t"
  "ds_read_b64_tr_b16 v[160:161], v178 offset:0\n\t"
  "ds_read_b64_tr_b16 v[162:163], v178 offset:256\n\t"
  "v_lshlrev_b32 v232, 16, v224\n\t"
  "v_and_b32 v233, 0xffff0000, v224\n\t"
  "v_lshlrev_b32 v234, 16, v200\n\t"
  "v_and_b32 v235, 0xffff0000, v200\n\t"
  "v_mfma_f32_32x32x16_bf16 v[16:31], v[172:175], v[144:147], v[16:31]\n\t"
  "ds_read_b128 v[128:131], v176 offset:0\n\t"
  "ds_read_b64_tr_b16 v[164:165], v178 offset:512\n\t"
  "v_lshlrev_b32 v236, 16, v225\n\t"
  "v_and_b32 v237, 0xffff0000, v225\n\t"
  "v_lshlrev_b32 v238, 16, v202\n\t"
  "v_and_b32 v239, 0xffff0000, v202\n\t"
  "v_mfma_f32_32x32x16_bf16 v[32:47], v[168:171], v[148:151], v[32:47]\n\t"
  "ds_read_b64_tr_b16 v[166:167], v178 offset:768\n\t"
  "ds_read_b128 v[132:135], v176 offset:4096\n\t"
  "v_lshlrev_b32 v240, 16, v226\n\t"
  "v_and_b32 v241, 0xffff0000, v226\n\t"
  "v_lshlrev_b32 v242, 16, v204\n\t"
  "v_and_b32 v243, 0xffff0000, v204\n\t"
  "v_mfma_f32_32x32x16_bf16 v[48:63], v[172:175], v[148:151], v[48:63]\n\t"
  "ds_read_b128 v[136:139], v176 offset:8192\n\t"
  "ds_read_b128 v[140:143], v176 offset:12288\n\t"
  "v_lshlrev_b32 v244, 16, v227\n\t"
  "v_and_b32 v245, 0xffff0000, v227\n\t"
  "v_lshlrev_b32 v246, 16, v206\n\t"
  "v_and_b32 v247, 0xffff0000, v206\n\t"
  "v_mfma_f32_32x32x16_bf16 v[64:79], v[168:171], v[152:155], v[64:79]\n\t"
  "v_mul_f32 v232, v232, v234\n\t"
  "v_mul_f32 v233, v233, v235\n\t"
  "v_lshlrev_b32 v234, 16, v192\n\t"
  "v_and_b32 v235, 0xffff0000, v192\n\t"
  "v_mfma_f32_32x32x16_bf16 v[80:95], v[172:175], v[152:155], v[80:95]\n\t"
  "v_cvt_pk_bf16_f32 v232, 0, v232\n\t"
  "v_cvt_pk_bf16_f32 v233, 0, v233\n\t"
  "v_mul_f32 v236, v236, v238\n\t"
  "v_mul_f32 v237, v237, v239\n\t"
  "v_mfma_f32_32x32x16_bf16 v[96:111], v[168:171], v[156:159], v[96:111]\n\t"
  "s_add_u32 m0, s66, 98304\n\t"
  "s_nop 0\n\t"
  "buffer_load_dwordx4 v184, s[44:47], s65 offen offset:2048 lds\n\t"
  "v_lshlrev_b32 v238, 16, v194\n\t"
  "v_and_b32 v239, 0xffff0000, v194\n\t"
  "v_cvt_pk_bf16_f32 v236, 0, v236\n\t"
  "v_cvt_pk_bf16_f32 v237, 0, v237\n\t"
  "v_mfma_f32_32x32x16_bf16 v[112:127], v[172:175], v[156:159], v[112:127]\n\t"
  "buffer_load_dwordx4 v185, s[44:47], s65 offen offset:3072 lds\n\t"
  "s_add_u32 s65, s65, 262144\n\t"
  "v_mul_f32 v240, v240, v242\n\t"
  "v_mul_f32 v241, v241, v243\n\t"
  "v_lshlrev_b32 v242, 16, v196\n\t"
  "v_and_b32 v243, 0xffff0000, v196\n\t"
  "s_waitcnt lgkmcnt(0)\n\t"
  "v_mfma_f32_32x32x16_bf16 v[0:15], v[160:163], v[128:131], v[0:15]\n\t"
  "ds_read_b64_tr_b16 v[168:169], v178 offset:16384\n\t"
  "ds_read_b64_tr_b16 v[170:171], v178 offset:16640\n\t"
  "v_cvt_pk_bf16_f32 v240, 0, v240\n\t"
  "v_cvt_pk_bf16_f32 v241, 0, v241\n\t"
  "v_mul_f32 v244, v244, v246\n\t"
  "v_mul_f32 v245, v245, v247\n\t"
  "v_mfma_f32_32x32x16_bf16 v[16:31], v[164:167], v[128:131], v[16:31]\n\t"
  "ds_read_b128 v[144:147], v177 offset:0\n\t"
  "ds_read_b64_tr_b16 v[172:173], v178 offset:16896\n\t"
  "v_lshlrev_b32 v246, 16, v198\n\t"
  "v_and_b32 v247, 0xffff0000, v198\n\t"
  "v_cvt_pk_bf16_f32 v244, 0, v244\n\t"
  "v_cvt_pk_bf16_f32 v245, 0, v245\n\t"
  "v_mfma_f32_32x32x16_bf16 v[32:47], v[160:163], v[132:135], v[32:47]\n\t"
  "ds_read_b64_tr_b16 v[174:175], v178 offset:17152\n\t"
  "ds_read_b128 v[148:151], v177 offset:4096\n\t"
  "v_add_f32 v232, v232, v234\n\t"
  "v_add_f32 v233, v233, v235\n\t"
  "v_add_f32 v236, v236, v238\n\t"
  "v_add_f32 v237, v237, v239\n\t"
  "v_mfma_f32_32x32x16_bf16 v[48:63], v[164:167], v[132:135], v[48:63]\n\t"
  "ds_read_b128 v[152:155], v177 offset:8192\n\t"
  "ds_read_b128 v[156:159], v177 offset:12288\n\t"
  "v_add_f32 v240, v240, v242\n\t"
  "v_add_f32 v241, v241, v243\n\t"
  "v_add_f32 v244, v244, v246\n\t"
  "v_add_f32 v245, v245, v247\n\t"
  "v_mfma_f32_32x32x16_bf16 v[64:79], v[160:163], v[136:139], v[64:79]\n\t"
  "v_cvt_pk_bf16_f32 v224, v232, v233\n\t"
  "v_cvt_pk_bf16_f32 v225, v236, v237\n\t"
  "v_cvt_pk_bf16_f32 v226, v240, v241\n\t"
  "v_cvt_pk_bf16_f32 v227, v244, v245\n\t"
  "v_mfma_f32_32x32x16_bf16 v[80:95], v[164:167], v[136:139], v[80:95]\n\t"
  "buffer_store_dwordx4 v[224:227], v191, s[52:55], 0 offen sc1\n\t"
  "v_lshlrev_b32 v232, 16, v228\n\t"
  "v_and_b32 v233, 0xffff0000, v228\n\t"
  "v_lshlrev_b32 v234, 16, v216\n\t"
  "v_mfma_f32_32x32x16_bf16 v[96:111], v[160:163], v[140:143], v[96:111]\n\t"
  "v_and_b32 v235, 0xffff0000, v216\n\t"
  "v_lshlrev_b32 v236, 16, v229\n\t"
  "v_and_b32 v237, 0xffff0000, v229\n\t"
  "v_lshlrev_b32 v238, 16, v218\n\t"
  "v_mfma_f32_32x32x16_bf16 v[112:127], v[164:167], v[140:143], v[112:127]\n\t"
  "v_and_b32 v239, 0xffff0000, v218\n\t"
  "v_lshlrev_b32 v240, 16, v230\n\t"
  "v_and_b32 v241, 0xffff0000, v230\n\t"
  "v_lshlrev_b32 v242, 16, v220\n\t"
  "s_waitcnt vmcnt(7) lgkmcnt(0)\n\t"
  "s_barrier\n\t"
  "s_mov_b32 s68, 0\n\t"
  "v_add_u32 v178, 0, v181\n\t"
  "v_xad_u32 v176, v179, 0, s68\n\t"
  "v_xor_b32 v177, 32, v176\n\t"
  "v_mfma_f32_32x32x16_bf16 v[0:15], v[168:171], v[144:147], v[0:15]\n\t"
  "ds_read_b64_tr_b16 v[160:161], v178 offset:0\n\t"
  "ds_read_b64_tr_b16 v[162:163], v178 offset:256\n\t"
  "v_and_b32 v243, 0xffff0000, v220\n\t"
  "v_lshlrev_b32 v244, 16, v231\n\t"
  "v_and_b32 v245, 0xffff0000, v231\n\t"
  "v_lshlrev_b32 v246, 16, v222\n\t"
  "v_mfma_f32_32x32x16_bf16 v[16:31], v[172:175], v[144:147], v[16:31]\n\t"
  "ds_read_b128 v[128:131], v176 offset:0\n\t"
  "ds_read_b64_tr_b16 v[164:165], v178 offset:512\n\t"
  "v_and_b32 v247, 0xffff0000, v222\n\t"
  "v_mul_f32 v232, v232, v234\n\t"
  "v_mul_f32 v233, v233, v235\n\t"
  "v_lshlrev_b32 v234, 16, v208\n\t"
  "v_mfma_f32_32x32x16_bf16 v[32:47], v[168:171], v[148:151], v[32:47]\n\t"
  "ds_read_b64_tr_b16 v[166:167], v178 offset:768\n\t"
  "ds_read_b128 v[132:135], v176 offset:4096\n\t"
  "v_and_b32 v235, 0xffff0000, v208\n\t"
  "v_cvt_pk_bf16_f32 v232, 0, v232\n\t"
  "v_cvt_pk_bf16_f32 v233, 0, v233\n\t"
  "v_mul_f32 v236, v236, v238\n\t"
  "v_mfma_f32_32x32x16_bf16 v[48:63], v[172:175], v[148:151], v[48:63]\n\t"
  "ds_read_b128 v[136:139], v176 offset:8192\n\t"
  "ds_read_b128 v[140:143], v176 offset:12288\n\t"
  "v_mul_f32 v237, v237, v239\n\t"
  "v_lshlrev_b32 v238, 16, v210\n\t"
  "v_and_b32 v239, 0xffff0000, v210\n\t"
  "v_cvt_pk_bf16_f32 v236, 0, v236\n\t"
  "v_mfma_f32_32x32x16_bf16 v[64:79], v[168:171], v[152:155], v[64:79]\n\t"
  "s_add_u32 m0, s66, 98304\n\t"
  "s_add_u32 s73, s64, 16896\n\t"
  "buffer_load_dwordx4 v182, s[40:43], s73 offen lds\n\t"
  "v_cvt_pk_bf16_f32 v237, 0, v237\n\t"
  "v_mul_f32 v240, v240, v242\n\t"
  "v_mul_f32 v241, v241, v243\n\t"
  "v_lshlrev_b32 v242, 16, v212\n\t"
  "v_mfma_f32_32x32x16_bf16 v[80:95], v[172:175], v[152:155], v[80:95]\n\t"
  "buffer_load_dwordx4 v183, s[40:43], s73 offen offset:1024 lds\n\t"
  "v_and_b32 v243, 0xffff0000, v212\n\t"
  "v_cvt_pk_bf16_f32 v240, 0, v240\n\t"
  "v_cvt_pk_bf16_f32 v241, 0, v241\n\t"
  "v_mul_f32 v244, v244, v246\n\t"
  "v_mfma_f32_32x32x16_bf16 v[96:111], v[168:171], v[156:159], v[96:111]\n\t"
  "s_add_u32 m0, s66, 131072\n\t"
  "s_add_u32 s73, s64, 640\n\t"
  "buffer_load_dwordx4 v182, s[40:43], s73 offen lds\n\t"
  "v_mul_f32 v245, v245, v247\n\t"
  "v_lshlrev_b32 v246, 16, v214\n\t"
  "v_and_b32 v247, 0xffff0000, v214\n\t"
  "v_cvt_pk_bf16_f32 v244, 0, v244\n\t"
  "v_mfma_f32_32x32x16_bf16 v[112:127], v[172:175], v[156:159], v[112:127]\n\t"
  "buffer_load_dwordx4 v183, s[40:43], s73 offen offset:1024 lds\n\t"
  "v_cvt_pk_bf16_f32 v245, 0, v245\n\t"
  "v_add_f32 v232, v232, v234\n\t"
  "v_add_f32 v233, v233, v235\n\t"
  "v_add_f32 v236, v236, v238\n\t"
  "s_waitcnt lgkmcnt(0)\n\t"
  "v_mfma_f32_32x32x16_bf16 v[0:15], v[160:163], v[128:131], v[0:15]\n\t"
  "ds_read_b64_tr_b16 v[168:169], v178 offset:16384\n\t"
  "ds_read_b64_tr_b16 v[170:171], v178 offset:16640\n\t"
  "v_add_f32 v237, v237, v239\n\t"
  "v_add_f32 v240, v240, v242\n\t"
  "v_add_f32 v241, v241, v243\n\t"
  "v_add_f32 v244, v244, v246\n\t"
  "v_mfma_f32_32x32x16_bf16 v[16:31], v[164:167], v[128:131], v[16:31]\n\t"
  "ds_read_b128 v[144:147], v177 offset:0\n\t"
  "ds_read_b64_tr_b16 v[172:173], v178 offset:16896\n\t"
  "v_add_f32 v245, v245, v247\n\t"
  "v_cvt_pk_bf16_f32 v228, v232, v233\n\t"
  "v_cvt_pk_bf16_f32 v229, v236, v237\n\t"
  "v_cvt_pk_bf16_f32 v230, v240, v241\n\t"
  "v_mfma_f32_32x32x16_bf16 v[32:47], v[160:163], v[132:135], v[32:47]\n\t"
  "ds_read_b64_tr_b16 v[174:175], v178 offset:17152\n\t"
  "ds_read_b128 v[148:151], v177 offset:4096\n\t"
  "v_cvt_pk_bf16_f32 v231, v244, v245\n\t"
  "buffer_store_dwordx4 v[228:231], v249, s[52:55], 0 offen sc1\n\t"
  "v_mfma_f32_32x32x16_bf16 v[48:63], v[164:167], v[132:135], v[48:63]\n\t"
  "ds_read_b128 v[152:155], v177 offset:8192\n\t"
  "ds_read_b128 v[156:159], v177 offset:12288\n\t"
  "v_mfma_f32_32x32x16_bf16 v[64:79], v[160:163], v[136:139], v[64:79]\n\t"
  "v_mfma_f32_32x32x16_bf16 v[80:95], v[164:167], v[136:139], v[80:95]\n\t"
  "v_mfma_f32_32x32x16_bf16 v[96:111], v[160:163], v[140:143], v[96:111]\n\t"
  "s_add_u32 m0, s66, 131072\n\t"
  "s_nop 0\n\t"
  "buffer_load_dwordx4 v184, s[44:47], s65 offen offset:2048 lds\n\t"
  "v_mfma_f32_32x32x16_bf16 v[112:127], v[164:167], v[140:143], v[112:127]\n\t"
  "buffer_load_dwordx4 v185, s[44:47], s65 offen offset:3072 lds\n\t"
  "s_add_u32 s65, s65, 262144\n\t"
  "s_waitcnt lgkmcnt(0)\n\t"
  "s_barrier\n\t"
  "s_mov_b32 s68, 0\n\t"
  "v_add_u32 v178, 32768, v181\n\t"
  "v_xad_u32 v176, v179, 64, s68\n\t"
  "v_xor_b32 v177, 32, v176\n\t"
  "v_mfma_f32_32x32x16_bf16 v[0:15], v[168:171], v[144:147], v[0:15]\n\t"
  "ds_read_b64_tr_b16 v[160:161], v178 offset:0\n\t"
  "ds_read_b64_tr_b16 v[162:163], v178 offset:256\n\t"
  "v_mfma_f32_32x32x16_bf16 v[16:31], v[172:175], v[144:147], v[16:31]\n\t"
  "ds_read_b128 v[128:131], v176 offset:0\n\t"
  "ds_read_b64_tr_b16 v[164:165], v178 offset:512\n\t"
  "v_mfma_f32_32x32x16_bf16 v[32:47], v[168:171], v[148:151], v[32:47]\n\t"
  "ds_read_b64_tr_b16 v[166:167], v178 offset:768\n\t"
  "ds_read_b128 v[132:135], v176 offset:4096\n\t"
  "v_mfma_f32_32x32x16_bf16 v[48:63], v[172:175], v[148:151], v[48:63]\n\t"
  "ds_read_b128 v[136:139], v176 offset:8192\n\t"
  "ds_read_b128 v[140:143], v176 offset:12288\n\t"
  "v_mfma_f32_32x32x16_bf16 v[64:79], v[168:171], v[152:155], v[64:79]\n\t"
  "v_mfma_f32_32x32x16_bf16 v[80:95], v[172:175], v[152:155], v[80:95]\n\t"
  "v_mfma_f32_32x32x16_bf16 v[96:111], v[168:171], v[156:159], v[96:111]\n\t"
  "s_add_u32 m0, s66, 0\n\t"
  "s_nop 0\n\t"
  "buffer_load_dwordx4 v184, s[44:47], s65 offen offset:2048 lds\n\t"
  "v_mfma_f32_32x32x16_bf16 v[112:127], v[172:175], v[156:159], v[112:127]\n\t"
  "buffer_load_dwordx4 v185, s[44:47], s65 offen offset:3072 lds\n\t"
  "s_add_u32 s65, s65, 262144\n\t"
  "s_waitcnt lgkmcnt(0)\n\t"
  "v_mfma_f32_32x32x16_bf16 v[0:15], v[160:163], v[128:131], v[0:15]\n\t"
  "ds_read_b64_tr_b16 v[168:169], v178 offset:16384\n\t"
  "ds_read_b64_tr_b16 v[170:171], v178 offset:16640\n\t"
  "v_mfma_f32_32x32x16_bf16 v[16:31], v[164:167], v[128:131], v[16:31]\n\t"
  "ds_read_b128 v[144:147], v177 offset:0\n\t"
  "ds_read_b64_tr_b16 v[172:173], v178 offset:16896\n\t"
  "v_mfma_f32_32x32x16_bf16 v[32:47], v[160:163], v[132:135], v[32:47]\n\t"
  "ds_read_b64_tr_b16 v[174:175], v178 offset:17152\n\t"
  "ds_read_b128 v[148:151], v177 offset:4096\n\t"
  "v_mfma_f32_32x32x16_bf16 v[48:63], v[164:167], v[132:135], v[48:63]\n\t"
  "ds_read_b128 v[152:155], v177 offset:8192\n\t"
  "ds_read_b128 v[156:159], v177 offset:12288\n\t"
  "v_mfma_f32_32x32x16_bf16 v[64:79], v[160:163], v[136:139], v[64:79]\n\t"
  "v_mfma_f32_32x32x16_bf16 v[80:95], v[164:167], v[136:139], v[80:95]\n\t"
  "v_mfma_f32_32x32x16_bf16 v[96:111], v[160:163], v[140:143], v[96:111]\n\t"
  "v_mfma_f32_32x32x16_bf16 v[112:127], v[164:167], v[140:143], v[112:127]\n\t"
  "s_waitcnt vmcnt(4)\n\t"
  "s_waitcnt lgkmcnt(0)\n\t"
  "s_barrier\n\t"
  "s_mov_b32 s68, 65536\n\t"
  "v_add_u32 v178, 65536, v181\n\t"
  "v_xad_u32 v176, v179, 0, s68\n\t"
  "v_xor_b32 v177, 32, v176\n\t"
  "v_mfma_f32_32x32x16_bf16 v[0:15], v[168:171], v[144:147], v[0:15]\n\t"
  "ds_read_b64_tr_b16 v[160:161], v178 offset:0\n\t"
  "ds_read_b64_tr_b16 v[162:163], v178 offset:256\n\t"
  "v_mfma_f32_32x32x16_bf16 v[16:31], v[172:175], v[144:147], v[16:31]\n\t"
  "ds_read_b128 v[128:131], v176 offset:0\n\t"
  "ds_read_b64_tr_b16 v[164:165], v178 offset:512\n\t"
  "v_mfma_f32_32x32x16_bf16 v[32:47], v[168:171], v[148:151], v[32:47]\n\t"
  "ds_read_b64_tr_b16 v[166:167], v178 offset:768\n\t"
  "ds_read_b128 v[132:135], v176 offset:4096\n\t"
  "v_mfma_f32_32x32x16_bf16 v[48:63], v[172:175], v[148:151], v[48:63]\n\t"
  "ds_read_b128 v[136:139], v176 offset:8192\n\t"
  "ds_read_b128 v[140:143], v176 offset:12288\n\t"
  "s_cmp_lg_u32 s69, 0\n\t"
  "s_cbranch_scc1 LFhf_%=\n\t"
  "v_mov_b32 v236, s77\n\t"
  "v_mov_b32 v237, 0\n\t"
  "s_mov_b64 exec, 1\n\t"
  "buffer_store_dword v236, v237, s[60:63], s80 offen offset:0 sc1\n\t"
  "s_mov_b64 exec, -1\n\t"
  "LFhf_%=:\n\t"
  "v_mfma_f32_32x32x16_bf16 v[64:79], v[168:171], v[152:155], v[64:79]\n\t"
  "s_add_u32 m0, s66, 0\n\t"
  "s_add_u32 s73, s64, 17024\n\t"
  "buffer_load_dwordx4 v182, s[40:43], s73 offen lds\n\t"
  "v_mfma_f32_32x32x16_bf16 v[80:95], v[172:175], v[152:155], v[80:95]\n\t"
  "buffer_load_dwordx4 v183, s[40:43], s73 offen offset:1024 lds\n\t"
  "v_mfma_f32_32x32x16_bf16 v[96:111], v[168:171], v[156:159], v[96:111]\n\t"
  "s_add_u32 m0, s66, 32768\n\t"
  "s_add_u32 s73, s64, 768\n\t"
  "buffer_load_dwordx4 v182, s[40:43], s73 offen lds\n\t"
  "v_mfma_f32_32x32x16_bf16 v[112:127], v[172:175], v[156:159], v[112:127]\n\t"
  "buffer_load_dwordx4 v183, s[40:43], s73 offen offset:1024 lds\n\t"
  "s_waitcnt lgkmcnt(0)\n\t"
  "v_mfma_f32_32x32x16_bf16 v[0:15], v[160:163], v[128:131], v[0:15]\n\t"
  "ds_read_b64_tr_b16 v[168:169], v178 offset:16384\n\t"
  "ds_read_b64_tr_b16 v[170:171], v178 offset:16640\n\t"
  "v_mfma_f32_32x32x16_bf16 v[16:31], v[164:167], v[128:131], v[16:31]\n\t"
  "ds_read_b128 v[144:147], v177 offset:0\n\t"
  "ds_read_b64_tr_b16 v[172:173], v178 offset:16896\n\t"
  "v_mfma_f32_32x32x16_bf16 v[32:47], v[160:163], v[132:135], v[32:47]\n\t"
  "ds_read_b64_tr_b16 v[174:175], v178 offset:17152\n\t"
  "ds_read_b128 v[148:151], v177 offset:4096\n\t"
  "v_mfma_f32_32x32x16_bf16 v[48:63], v[164:167], v[132:135], v[48:63]\n\t"
  "ds_read_b128 v[152:155], v177 offset:8192\n\t"
  "ds_read_b128 v[156:159], v177 offset:12288\n\t"
  "v_mfma_f32_32x32x16_bf16 v[64:79], v[160:163], v[136:139], v[64:79]\n\t"
  "v_mfma_f32_32x32x16_bf16 v[80:95], v[164:167], v[136:139], v[80:95]\n\t"
  "v_mfma_f32_32x32x16_bf16 v[96:111], v[160:163], v[140:143], v[96:111]\n\t"
  "s_add_u32 m0, s66, 32768\n\t"
  "s_nop 0\n\t"
  "buffer_load_dwordx4 v184, s[44:47], s65 offen offset:2048 lds\n\t"
  "v_mfma_f32_32x32x16_bf16 v[112:127], v[164:167], v[140:143], v[112:127]\n\t"
  "buffer_load_dwordx4 v185, s[44:47], s65 offen offset:3072 lds\n\t"
  "s_add_u32 s65, s65, 262144\n\t"
  "s_waitcnt lgkmcnt(0)\n\t"
  "s_barrier\n\t"
  "s_mov_b32 s68, 65536\n\t"
  "v_add_u32 v178, 98304, v181\n\t"
  "v_xad_u32 v176, v179, 64, s68\n\t"
  "v_xor_b32 v177, 32, v176\n\t"
  "v_mfma_f32_32x32x16_bf16 v[0:15], v[168:171], v[144:147], v[0:15]\n\t"
  "ds_read_b64_tr_b16 v[160:161], v178 offset:0\n\t"
  "ds_read_b64_tr_b16 v[162:163], v178 offset:256\n\t"
  "v_mfma_f32_32x32x16_bf16 v[16:31], v[172:175], v[144:147], v[16:31]\n\t"
  "ds_read_b128 v[128:131], v176 offset:0\n\t"
  "ds_read_b64_tr_b16 v[164:165], v178 offset:512\n\t"
  "v_mfma_f32_32x32x16_bf16 v[32:47], v[168:171], v[148:151], v[32:47]\n\t"
  "ds_read_b64_tr_b16 v[166:167], v178 offset:768\n\t"
  "ds_read_b128 v[132:135], v176 offset:4096\n\t"
  "v_mfma_f32_32x32x16_bf16 v[48:63], v[172:175], v[148:151], v[48:63]\n\t"
  "ds_read_b128 v[136:139], v176 offset:8192\n\t"
  "ds_read_b128 v[140:143], v176 offset:12288\n\t"
  "v_mfma_f32_32x32x16_bf16 v[64:79], v[168:171], v[152:155], v[64:79]\n\t"
  "v_mfma_f32_32x32x16_bf16 v[80:95], v[172:175], v[152:155], v[80:95]\n\t"
  "v_mfma_f32_32x32x16_bf16 v[96:111], v[168:171], v[156:159], v[96:111]\n\t"
  "s_add_u32 m0, s66, 65536\n\t"
  "s_nop 0\n\t"
  "buffer_load_dwordx4 v184, s[44:47], s65 offen offset:2048 lds\n\t"
  "v_mfma_f32_32x32x16_bf16 v[112:127], v[172:175], v[156:159], v[112:127]\n\t"
  "buffer_load_dwordx4 v185, s[44:47], s65 offen offset:3072 lds\n\t"
  "s_add_u32 s65, s65, 262144\n\t"
  "s_waitcnt lgkmcnt(0)\n\t"
  "v_mfma_f32_32x32x16_bf16 v[0:15], v[160:163], v[128:131], v[0:15]\n\t"
  "ds_read_b64_tr_b16 v[168:169], v178 offset:16384\n\t"
  "ds_read_b64_tr_b16 v[170:171], v178 offset:16640\n\t"
  "v_mfma_f32_32x32x16_bf16 v[16:31], v[164:167], v[128:131], v[16:31]\n\t"
  "ds_read_b128 v[144:147], v177 offset:0\n\t"
  "ds_read_b64_tr_b16 v[172:173], v178 offset:16896\n\t"
  "v_mfma_f32_32x32x16_bf16 v[32:47], v[160:163], v[132:135], v[32:47]\n\t"
  "ds_read_b64_tr_b16 v[174:175], v178 offset:17152\n\t"
  "ds_read_b128 v[148:151], v177 offset:4096\n\t"
  "v_mfma_f32_32x32x16_bf16 v[48:63], v[164:167], v[132:135], v[48:63]\n\t"
  "ds_read_b128 v[152:155], v177 offset:8192\n\t"
  "ds_read_b128 v[156:159], v177 offset:12288\n\t"
  "v_mfma_f32_32x32x16_bf16 v[64:79], v[160:163], v[136:139], v[64:79]\n\t"
  "v_mfma_f32_32x32x16_bf16 v[80:95], v[164:167], v[136:139], v[80:95]\n\t"
  "v_mfma_f32_32x32x16_bf16 v[96:111], v[160:163], v[140:143], v[96:111]\n\t"
  "v_mfma_f32_32x32x16_bf16 v[112:127], v[164:167], v[140:143], v[112:127]\n\t"
  "s_and_b32 s83, s80, 0xffffff80\n\t"
  "v_lshrrev_b32 v240, 2, v186\n\t"
  "v_and_b32 v240, 124, v240\n\t"
  "buffer_load_dword v192, v240, s[60:63], s83 offen sc1\n\t"
  "s_waitcnt vmcnt(7) lgkmcnt(0)\n\t"
  "s_barrier\n\t"
  "s_mov_b32 s68, 0\n\t"
  "v_add_u32 v178, 131072, v181\n\t"
  "v_xad_u32 v176, v180, 0, s68\n\t"
  "v_xor_b32 v177, 32, v176\n\t"
  "v_mfma_f32_32x32x16_bf16 v[0:15], v[168:171], v[144:147], v[0:15]\n\t"
  "ds_read_b64_tr_b16 v[160:161], v178 offset:0\n\t"
  "ds_read_b64_tr_b16 v[162:163], v178 offset:256\n\t"
  "v_mfma_f32_32x32x16_bf16 v[16:31], v[172:175], v[144:147], v[16:31]\n\t"
  "ds_read_b128 v[128:131], v176 offset:0\n\t"
  "ds_read_b64_tr_b16 v[164:165], v178 offset:512\n\t"
  "v_mfma_f32_32x32x16_bf16 v[32:47], v[168:171], v[148:151], v[32:47]\n\t"
  "ds_read_b64_tr_b16 v[166:167], v178 offset:768\n\t"
  "ds_read_b128 v[132:135], v176 offset:4096\n\t"
  "v_mfma_f32_32x32x16_bf16 v[48:63], v[172:175], v[148:151], v[48:63]\n\t"
  "ds_read_b128 v[136:139], v176 offset:8192\n\t"
  "ds_read_b128 v[140:143], v176 offset:12288\n\t"
  "v_mfma_f32_32x32x16_bf16 v[64:79], v[168:171], v[152:155], v[64:79]\n\t"
  "s_add_u32 m0, s66, 65536\n\t"
  "s_add_u32 s73, s64, 17152\n\t"
  "buffer_load_dwordx4 v182, s[40:43], s73 offen lds\n\t"
  "v_mfma_f32_32x32x16_bf16 v[80:95], v[172:175], v[152:155], v[80:95]\n\t"
  "buffer_load_dwordx4 v183, s[40:43], s73 offen offset:1024 lds\n\t"
  "v_mfma_f32_32x32x16_bf16 v[96:111], v[168:171], v[156:159], v[96:111]\n\t"
  "s_add_u32 m0, s66, 98304\n\t"
  "s_add_u32 s73, s64, 896\n\t"
  "buffer_load_dwordx4 v182, s[40:43], s73 offen lds\n\t"
  "v_mfma_f32_32x32x16_bf16 v[112:127], v[172:175], v[156:159], v[112:127]\n\t"
  "buffer_load_dwordx4 v183, s[40:43], s73 offen offset:1024 lds\n\t"
  "s_waitcnt lgkmcnt(0)\n\t"
  "v_mfma_f32_32x32x16_bf16 v[0:15], v[160:163], v[128:131], v[0:15]\n\t"
  "ds_read_b64_tr_b16 v[168:169], v178 offset:16384\n\t"
  "ds_read_b64_tr_b16 v[170:171], v178 offset:16640\n\t"
  "v_mfma_f32_32x32x16_bf16 v[16:31], v[164:167], v[128:131], v[16:31]\n\t"
  "ds_read_b128 v[144:147], v177 offset:0\n\t"
  "ds_read_b64_tr_b16 v[172:173], v178 offset:16896\n\t"
  "v_mfma_f32_32x32x16_bf16 v[32:47], v[160:163], v[132:135], v[32:47]\n\t"
  "ds_read_b64_tr_b16 v[174:175], v178 offset:17152\n\t"
  "ds_read_b128 v[148:151], v177 offset:4096\n\t"
  "v_mfma_f32_32x32x16_bf16 v[48:63], v[164:167], v[132:135], v[48:63]\n\t"
  "ds_read_b128 v[152:155], v177 offset:8192\n\t"
  "ds_read_b128 v[156:159], v177 offset:12288\n\t"
  "v_mfma_f32_32x32x16_bf16 v[64:79], v[160:163], v[136:139], v[64:79]\n\t"
  "v_mfma_f32_32x32x16_bf16 v[80:95], v[164:167], v[136:139], v[80:95]\n\t"
  "v_mfma_f32_32x32x16_bf16 v[96:111], v[160:163], v[140:143], v[96:111]\n\t"
  "s_add_u32 m0, s66, 98304\n\t"
  "s_nop 0\n\t"
  "buffer_load_dwordx4 v184, s[44:47], s65 offen offset:2048 lds\n\t"
  "v_mfma_f32_32x32x16_bf16 v[112:127], v[164:167], v[140:143], v[112:127]\n\t"
  "buffer_load_dwordx4 v185, s[44:47], s65 offen offset:3072 lds\n\t"
  "s_add_u32 s65, s65, 262144\n\t"
  "s_waitcnt lgkmcnt(0)\n\t"
  "s_barrier\n\t"
  "s_mov_b32 s68, 0\n\t"
  "v_add_u32 v178, 0, v181\n\t"
  "v_xad_u32 v176, v180, 64, s68\n\t"
  "v_xor_b32 v177, 32, v176\n\t"
  "v_mfma_f32_32x32x16_bf16 v[0:15], v[168:171], v[144:147], v[0:15]\n\t"
  "ds_read_b64_tr_b16 v[160:161], v178 offset:0\n\t"
  "ds_read_b64_tr_b16 v[162:163], v178 offset:256\n\t"
  "v_mfma_f32_32x32x16_bf16 v[16:31], v[172:175], v[144:147], v[16:31]\n\t"
  "ds_read_b128 v[128:131], v176 offset:0\n\t"
  "ds_read_b64_tr_b16 v[164:165], v178 offset:512\n\t"
  "v_mfma_f32_32x32x16_bf16 v[32:47], v[168:171], v[148:151], v[32:47]\n\t"
  "ds_read_b64_tr_b16 v[166:167], v178 offset:768\n\t"
  "ds_read_b128 v[132:135], v176 offset:4096\n\t"
  "v_mfma_f32_32x32x16_bf16 v[48:63], v[172:175], v[148:151], v[48:63]\n\t"
  "ds_read_b128 v[136:139], v176 offset:8192\n\t"
  "ds_read_b128 v[140:143], v176 offset:12288\n\t"
  "v_mfma_f32_32x32x16_bf16 v[64:79], v[168:171], v[152:155], v[64:79]\n\t"
  "v_mfma_f32_32x32x16_bf16 v[80:95], v[172:175], v[152:155], v[80:95]\n\t"
  "v_mfma_f32_32x32x16_bf16 v[96:111], v[168:171], v[156:159], v[96:111]\n\t"
  "s_add_u32 m0, s66, 131072\n\t"
  "s_nop 0\n\t"
  "buffer_load_dwordx4 v184, s[44:47], s65 offen offset:2048 lds\n\t"
  "v_mfma_f32_32x32x16_bf16 v[112:127], v[172:175], v[156:159], v[112:127]\n\t"
  "buffer_load_dwordx4 v185, s[44:47], s65 offen offset:3072 lds\n\t"
  "s_add_u32 s65, s65, 262144\n\t"
  "s_waitcnt lgkmcnt(0)\n\t"
  "v_mfma_f32_32x32x16_bf16 v[0:15], v[160:163], v[128:131], v[0:15]\n\t"
  "ds_read_b64_tr_b16 v[168:169], v178 offset:16384\n\t"
  "ds_read_b64_tr_b16 v[170:171], v178 offset:16640\n\t"
  "v_mfma_f32_32x32x16_bf16 v[16:31], v[164:167], v[128:131], v[16:31]\n\t"
  "ds_read_b128 v[144:147], v177 offset:0\n\t"
  "ds_read_b64_tr_b16 v[172:173], v178 offset:16896\n\t"
  "v_mfma_f32_32x32x16_bf16 v[32:47], v[160:163], v[132:135], v[32:47]\n\t"
  "ds_read_b64_tr_b16 v[174:175], v178 offset:17152\n\t"
  "ds_read_b128 v[148:151], v177 offset:4096\n\t"
  "v_mfma_f32_32x32x16_bf16 v[48:63], v[164:167], v[132:135], v[48:63]\n\t"
  "ds_read_b128 v[152:155], v177 offset:8192\n\t"
  "ds_read_b128 v[156:159], v177 offset:12288\n\t"
  "v_mfma_f32_32x32x16_bf16 v[64:79], v[160:163], v[136:139], v[64:79]\n\t"
  "v_mfma_f32_32x32x16_bf16 v[80:95], v[164:167], v[136:139], v[80:95]\n\t"
  "v_mfma_f32_32x32x16_bf16 v[96:111], v[160:163], v[140:143], v[96:111]\n\t"
  "v_mfma_f32_32x32x16_bf16 v[112:127], v[164:167], v[140:143], v[112:127]\n\t"
  "s_waitcnt vmcnt(8)\n\t"
  "LFhck_%=:\n\t"
  "v_cmp_ne_u32 vcc, s77, v192\n\t"
  "s_cbranch_vccnz LFhsk_%=\n\t"
  "LFhok_%=:\n\t"
  "s_waitcnt vmcnt(6) lgkmcnt(0)\n\t"
  "s_barrier\n\t"
  "s_mov_b32 s68, 32768\n\t"
  "v_add_u32 v178, 32768, v181\n\t"
  "v_xad_u32 v176, v179, 0, s68\n\t"
  "v_xor_b32 v177, 32, v176\n\t"
  "v_mfma_f32_32x32x16_bf16 v[0:15], v[168:171], v[144:147], v[0:15]\n\t"
  "ds_read_b64_tr_b16 v[160:161], v178 offset:0\n\t"
  "ds_read_b64_tr_b16 v[162:163], v178 offset:256\n\t"
  "v_mfma_f32_32x32x16_bf16 v[16:31], v[172:175], v[144:147], v[16:31]\n\t"
  "ds_read_b128 v[128:131], v176 offset:0\n\t"
  "ds_read_b64_tr_b16 v[164:165], v178 offset:512\n\t"
  "v_mfma_f32_32x32x16_bf16 v[32:47], v[168:171], v[148:151], v[32:47]\n\t"
  "ds_read_b64_tr_b16 v[166:167], v178 offset:768\n\t"
  "ds_read_b128 v[132:135], v176 offset:4096\n\t"
  "v_mfma_f32_32x32x16_bf16 v[48:63], v[172:175], v[148:151], v[48:63]\n\t"
  "ds_read_b128 v[136:139], v176 offset:8192\n\t"
  "ds_read_b128 v[140:143], v176 offset:12288\n\t"
  "s_add_u32 m0, s66, 131072\n\t"
  "s_add_u32 s73, s64, 17280\n\t"
  "buffer_load_dwordx4 v182, s[40:43], s73 offen lds\n\t"
  "v_mfma_f32_32x32x16_bf16 v[64:79], v[168:171], v[152:155], v[64:79]\n\t"
  "buffer_load_dwordx4 v183, s[40:43], s73 offen offset:1024 lds\n\t"
  "v_mfma_f32_32x32x16_bf16 v[80:95], v[172:175], v[152:155], v[80:95]\n\t"
  "s_add_u32 m0, s66, 0\n\t"
  "s_add_u32 s73, s71, 0\n\t"
  "buffer_load_dwordx4 v186, s[52:55], s73 offen sc1 lds\n\t"
  "v_mfma_f32_32x32x16_bf16 v[96:111], v[168:171], v[156:159], v[96:111]\n\t"
  "buffer_load_dwordx4 v186, s[52:55], s73 offen offset:1024 sc1 lds\n\t"
  "s_add_u32 s73, s72, 0\n\t"
  "v_mfma_f32_32x32x16_bf16 v[112:127], v[172:175], v[156:159], v[112:127]\n\t"
  "buffer_load_dwordx4 v189, s[56:59], s73 offen offset:2048 lds\n\t"
  "s_waitcnt lgkmcnt(0)\n\t"
  "v_mfma_f32_32x32x16_bf16 v[0:15], v[160:163], v[128:131], v[0:15]\n\t"
  "ds_read_b64_tr_b16 v[168:169], v178 offset:16384\n\t"
  "ds_read_b64_tr_b16 v[170:171], v178 offset:16640\n\t"
  "v_mfma_f32_32x32x16_bf16 v[16:31], v[164:167], v[128:131], v[16:31]\n\t"
  "ds_read_b128 v[144:147], v177 offset:0\n\t"
  "ds_read_b64_tr_b16 v[172:173], v178 offset:16896\n\t"
  "v_mfma_f32_32x32x16_bf16 v[32:47], v[160:163], v[132:135], v[32:47]\n\t"
  "ds_read_b64_tr_b16 v[174:175], v178 offset:17152\n\t"
  "ds_read_b128 v[148:151], v177 offset:4096\n\t"
  "v_mfma_f32_32x32x16_bf16 v[48:63], v[164:167], v[132:135], v[48:63]\n\t"
  "ds_read_b128 v[152:155], v177 offset:8192\n\t"
  "ds_read_b128 v[156:159], v177 offset:12288\n\t"
  "v_mfma_f32_32x32x16_bf16 v[64:79], v[160:163], v[136:139], v[64:79]\n\t"
  "v_mfma_f32_32x32x16_bf16 v[80:95], v[164:167], v[136:139], v[80:95]\n\t"
  "v_mfma_f32_32x32x16_bf16 v[96:111], v[160:163], v[140:143], v[96:111]\n\t"
  "v_mfma_f32_32x32x16_bf16 v[112:127], v[164:167], v[140:143], v[112:127]\n\t"
  "s_waitcnt lgkmcnt(0)\n\t"
  "s_barrier\n\t"
  "s_mov_b32 s68, 32768\n\t"
  "v_add_u32 v178, 65536, v181\n\t"
  "v_xad_u32 v176, v179, 64, s68\n\t"
  "v_xor_b32 v177, 32, v176\n\t"
  "v_mfma_f32_32x32x16_bf16 v[0:15], v[168:171], v[144:147], v[0:15]\n\t"
  "ds_read_b64_tr_b16 v[160:161], v178 offset:0\n\t"
  "ds_read_b64_tr_b16 v[162:163], v178 offset:256\n\t"
  "v_mfma_f32_32x32x16_bf16 v[16:31], v[172:175], v[144:147], v[16:31]\n\t"
  "ds_read_b128 v[128:131], v176 offset:0\n\t"
  "ds_read_b64_tr_b16 v[164:165], v178 offset:512\n\t"
  "v_mfma_f32_32x32x16_bf16 v[32:47], v[168:171], v[148:151], v[32:47]\n\t"
  "ds_read_b64_tr_b16 v[166:167], v178 offset:768\n\t"
  "ds_read_b128 v[132:135], v176 offset:4096\n\t"
  "v_mfma_f32_32x32x16_bf16 v[48:63], v[172:175], v[148:151], v[48:63]\n\t"
  "ds_read_b128 v[136:139], v176 offset:8192\n\t"
  "ds_read_b128 v[140:143], v176 offset:12288\n\t"
  "v_mfma_f32_32x32x16_bf16 v[64:79], v[168:171], v[152:155], v[64:79]\n\t"
  "v_mfma_f32_32x32x16_bf16 v[80:95], v[172:175], v[152:155], v[80:95]\n\t"
  "v_mfma_f32_32x32x16_bf16 v[96:111], v[168:171], v[156:159], v[96:111]\n\t"
  "v_mfma_f32_32x32x16_bf16 v[112:127], v[172:175], v[156:159], v[112:127]\n\t"
  "s_waitcnt lgkmcnt(0)\n\t"
  "v_mfma_f32_32x32x16_bf16 v[0:15], v[160:163], v[128:131], v[0:15]\n\t"
  "ds_read_b64_tr_b16 v[168:169], v178 offset:16384\n\t"
  "ds_read_b64_tr_b16 v[170:171], v178 offset:16640\n\t"
  "v_mfma_f32_32x32x16_bf16 v[16:31], v[164:167], v[128:131], v[16:31]\n\t"
  "ds_read_b128 v[144:147], v177 offset:0\n\t"
  "ds_read_b64_tr_b16 v[172:173], v178 offset:16896\n\t"
  "v_mfma_f32_32x32x16_bf16 v[32:47], v[160:163], v[132:135], v[32:47]\n\t"
  "ds_read_b64_tr_b16 v[174:175], v178 offset:17152\n\t"
  "ds_read_b128 v[148:151], v177 offset:4096\n\t"
  "v_mfma_f32_32x32x16_bf16 v[48:63], v[164:167], v[132:135], v[48:63]\n\t"
  "ds_read_b128 v[152:155], v177 offset:8192\n\t"
  "ds_read_b128 v[156:159], v177 offset:12288\n\t"
  "v_mfma_f32_32x32x16_bf16 v[64:79], v[160:163], v[136:139], v[64:79]\n\t"
  "v_mfma_f32_32x32x16_bf16 v[80:95], v[164:167], v[136:139], v[80:95]\n\t"
  "v_mfma_f32_32x32x16_bf16 v[96:111], v[160:163], v[140:143], v[96:111]\n\t"
  "v_mfma_f32_32x32x16_bf16 v[112:127], v[164:167], v[140:143], v[112:127]\n\t"
  "s_waitcnt vmcnt(3) lgkmcnt(0)\n\t"
  "s_barrier\n\t"
  "s_mov_b32 s68, 98304\n\t"
  "v_add_u32 v178, 98304, v181\n\t"
  "v_xad_u32 v176, v179, 0, s68\n\t"
  "v_xor_b32 v177, 32, v176\n\t"
  "v_mfma_f32_32x32x16_bf16 v[0:15], v[168:171], v[144:147], v[0:15]\n\t"
  "ds_read_b64_tr_b16 v[160:161], v178 offset:0\n\t"
  "ds_read_b64_tr_b16 v[162:163], v178 offset:256\n\t"
  "v_mfma_f32_32x32x16_bf16 v[16:31], v[172:175], v[144:147], v[16:31]\n\t"
  "ds_read_b128 v[128:131], v176 offset:0\n\t"
  "ds_read_b64_tr_b16 v[164:165], v178 offset:512\n\t"
  "v_mfma_f32_32x32x16_bf16 v[32:47], v[168:171], v[148:151], v[32:47]\n\t"
  "ds_read_b64_tr_b16 v[166:167], v178 offset:768\n\t"
  "ds_read_b128 v[132:135], v176 offset:4096\n\t"
  "s_add_u32 m0, s66, 32768\n\t"
  "s_add_u32 s73, s71, 2048\n\t"
  "buffer_load_dwordx4 v186, s[52:55], s73 offen sc1 lds\n\t"
  "v_mfma_f32_32x32x16_bf16 v[48:63], v[172:175], v[148:151], v[48:63]\n\t"
  "ds_read_b128 v[136:139], v176 offset:8192\n\t"
  "ds_read_b128 v[140:143], v176 offset:12288\n\t"
  "buffer_load_dwordx4 v186, s[52:55], s73 offen offset:1024 sc1 lds\n\t"
  "s_add_u32 s73, s72, 262144\n\t"
  "v_mfma_f32_32x32x16_bf16 v[64:79], v[168:171], v[152:155], v[64:79]\n\t"
  "buffer_load_dwordx4 v189, s[56:59], s73 offen offset:2048 lds\n\t"
  "v_mfma_f32_32x32x16_bf16 v[80:95], v[172:175], v[152:155], v[80:95]\n\t"
  "s_add_u32 m0, s66, 65536\n\t"
  "s_add_u32 s73, s71, 4096\n\t"
  "buffer_load_dwordx4 v186, s[52:55], s73 offen sc1 lds\n\t"
  "v_mfma_f32_32x32x16_bf16 v[96:111], v[168:171], v[156:159], v[96:111]\n\t"
  "buffer_load_dwordx4 v186, s[52:55], s73 offen offset:1024 sc1 lds\n\t"
  "s_add_u32 s73, s72, 524288\n\t"
  "v_mfma_f32_32x32x16_bf16 v[112:127], v[172:175], v[156:159], v[112:127]\n\t"
  "buffer_load_dwordx4 v189, s[56:59], s73 offen offset:2048 lds\n\t"
  "s_waitcnt lgkmcnt(0)\n\t"
  "v_mfma_f32_32x32x16_bf16 v[0:15], v[160:163], v[128:131], v[0:15]\n\t"
  "ds_read_b64_tr_b16 v[168:169], v178 offset:16384\n\t"
  "ds_read_b64_tr_b16 v[170:171], v178 offset:16640\n\t"
  "v_mfma_f32_32x32x16_bf16 v[16:31], v[164:167], v[128:131], v[16:31]\n\t"
  "ds_read_b128 v[144:147], v177 offset:0\n\t"
  "ds_read_b64_tr_b16 v[172:173], v178 offset:16896\n\t"
  "v_mfma_f32_32x32x16_bf16 v[32:47], v[160:163], v[132:135], v[32:47]\n\t"
  "ds_read_b64_tr_b16 v[174:175], v178 offset:17152\n\t"
  "ds_read_b128 v[148:151], v177 offset:4096\n\t"
  "v_mfma_f32_32x32x16_bf16 v[48:63], v[164:167], v[132:135], v[48:63]\n\t"
  "ds_read_b128 v[152:155], v177 offset:8192\n\t"
  "ds_read_b128 v[156:159], v177 offset:12288\n\t"
  "v_mfma_f32_32x32x16_bf16 v[64:79], v[160:163], v[136:139], v[64:79]\n\t"
  "v_mfma_f32_32x32x16_bf16 v[80:95], v[164:167], v[136:139], v[80:95]\n\t"
  "v_mfma_f32_32x32x16_bf16 v[96:111], v[160:163], v[140:143], v[96:111]\n\t"
  "v_mfma_f32_32x32x16_bf16 v[112:127], v[164:167], v[140:143], v[112:127]\n\t"
  "s_waitcnt lgkmcnt(0)\n\t"
  "s_barrier\n\t"
  "s_mov_b32 s68, 98304\n\t"
  "v_add_u32 v178, 131072, v181\n\t"
  "v_xad_u32 v176, v179, 64, s68\n\t"
  "v_xor_b32 v177, 32, v176\n\t"
  "v_mfma_f32_32x32x16_bf16 v[0:15], v[168:171], v[144:147], v[0:15]\n\t"
  "ds_read_b64_tr_b16 v[160:161], v178 offset:0\n\t"
  "ds_read_b64_tr_b16 v[162:163], v178 offset:256\n\t"
  "v_mfma_f32_32x32x16_bf16 v[16:31], v[172:175], v[144:147], v[16:31]\n\t"
  "ds_read_b128 v[128:131], v176 offset:0\n\t"
  "ds_read_b64_tr_b16 v[164:165], v178 offset:512\n\t"
  "v_mfma_f32_32x32x16_bf16 v[32:47], v[168:171], v[148:151], v[32:47]\n\t"
  "ds_read_b64_tr_b16 v[166:167], v178 offset:768\n\t"
  "ds_read_b128 v[132:135], v176 offset:4096\n\t"
  "v_mfma_f32_32x32x16_bf16 v[48:63], v[172:175], v[148:151], v[48:63]\n\t"
  "ds_read_b128 v[136:139], v176 offset:8192\n\t"
  "ds_read_b128 v[140:143], v176 offset:12288\n\t"
  "v_mfma_f32_32x32x16_bf16 v[64:79], v[168:171], v[152:155], v[64:79]\n\t"
  "v_mfma_f32_32x32x16_bf16 v[80:95], v[172:175], v[152:155], v[80:95]\n\t"
  "v_mfma_f32_32x32x16_bf16 v[96:111], v[168:171], v[156:159], v[96:111]\n\t"
  "v_mfma_f32_32x32x16_bf16 v[112:127], v[172:175], v[156:159], v[112:127]\n\t"
  "s_waitcnt lgkmcnt(0)\n\t"
  "v_mfma_f32_32x32x16_bf16 v[0:15], v[160:163], v[128:131], v[0:15]\n\t"
  "ds_read_b64_tr_b16 v[168:169], v178 offset:16384\n\t"
  "ds_read_b64_tr_b16 v[170:171], v178 offset:16640\n\t"
  "v_mfma_f32_32x32x16_bf16 v[16:31], v[164:167], v[128:131], v[16:31]\n\t"
  "ds_read_b128 v[144:147], v177 offset:0\n\t"
  "ds_read_b64_tr_b16 v[172:173], v178 offset:16896\n\t"
  "v_mfma_f32_32x32x16_bf16 v[32:47], v[160:163], v[132:135], v[32:47]\n\t"
  "ds_read_b64_tr_b16 v[174:175], v178 offset:17152\n\t"
  "ds_read_b128 v[148:151], v177 offset:4096\n\t"
  "v_mfma_f32_32x32x16_bf16 v[48:63], v[164:167], v[132:135], v[48:63]\n\t"
  "ds_read_b128 v[152:155], v177 offset:8192\n\t"
  "ds_read_b128 v[156:159], v177 offset:12288\n\t"
  "v_mfma_f32_32x32x16_bf16 v[64:79], v[160:163], v[136:139], v[64:79]\n\t"
  "v_mfma_f32_32x32x16_bf16 v[80:95], v[164:167], v[136:139], v[80:95]\n\t"
  "v_mfma_f32_32x32x16_bf16 v[96:111], v[160:163], v[140:143], v[96:111]\n\t"
  "v_mfma_f32_32x32x16_bf16 v[112:127], v[164:167], v[140:143], v[112:127]\n\t"
  "s_waitcnt vmcnt(6) lgkmcnt(0)\n\t"
  "s_barrier\n\t"
  "v_add_u32 v176, 0, v187\n\t"
  "v_add_u32 v178, 0, v188\n\t"
  "v_mfma_f32_32x32x16_bf16 v[0:15], v[168:171], v[144:147], v[0:15]\n\t"
  "ds_read_b64_tr_b16 v[160:161], v178 offset:0\n\t"
  "ds_read_b64_tr_b16 v[162:163], v178 offset:256\n\t"
  "v_mfma_f32_32x32x16_bf16 v[16:31], v[172:175], v[144:147], v[16:31]\n\t"
  "ds_read_b128 v[128:131], v176 offset:0\n\t"
  "ds_read_b64_tr_b16 v[164:165], v178 offset:512\n\t"
  "v_mfma_f32_32x32x16_bf16 v[32:47], v[168:171], v[148:151], v[32:47]\n\t"
  "ds_read_b64_tr_b16 v[166:167], v178 offset:768\n\t"
  "ds_read_b128 v[132:135], v176 offset:16384\n\t"
  "s_add_u32 m0, s66, 98304\n\t"
  "s_add_u32 s73, s71, 6144\n\t"
  "buffer_load_dwordx4 v186, s[52:55], s73 offen sc1 lds\n\t"
  "v_mfma_f32_32x32x16_bf16 v[48:63], v[172:175], v[148:151], v[48:63]\n\t"
  "buffer_load_dwordx4 v186, s[52:55], s73 offen offset:1024 sc1 lds\n\t"
  "s_add_u32 s73, s72, 786432\n\t"
  "v_mfma_f32_32x32x16_bf16 v[64:79], v[168:171], v[152:155], v[64:79]\n\t"
  "buffer_load_dwordx4 v189, s[56:59], s73 offen offset:2048 lds\n\t"
  "v_mfma_f32_32x32x16_bf16 v[80:95], v[172:175], v[152:155], v[80:95]\n\t"
  "s_add_u32 m0, s66, 131072\n\t"
  "s_add_u32 s73, s71, 8192\n\t"
  "buffer_load_dwordx4 v186, s[52:55], s73 offen sc1 lds\n\t"
  "v_mfma_f32_32x32x16_bf16 v[96:111], v[168:171], v[156:159], v[96:111]\n\t"
  "buffer_load_dwordx4 v186, s[52:55], s73 offen offset:1024 sc1 lds\n\t"
  "s_add_u32 s73, s72, 1048576\n\t"
  "v_mfma_f32_32x32x16_bf16 v[112:127], v[172:175], v[156:159], v[112:127]\n\t"
  "buffer_load_dwordx4 v189, s[56:59], s73 offen offset:2048 lds\n\t"
  "v_add_u32 v233, 0x1000, v186\n\t"
  "v_add_u32 v234, 0x2000, v186\n\t"
  "v_add_u32 v235, 0x3000, v186\n\t"
  "v_mov_b32 v232, v186\n\t"
  "s_add_u32 s68, s70, 0x4000\n\t"
  "s_nop 3\n\t"
  "v_cvt_pk_bf16_f32 v64, v64, v65\n\t"
  "v_cvt_pk_bf16_f32 v65, v66, v67\n\t"
  "v_cvt_pk_bf16_f32 v66, v68, v69\n\t"
  "v_cvt_pk_bf16_f32 v67, v70, v71\n\t"
  "v_cvt_pk_bf16_f32 v68, v72, v73\n\t"
  "v_cvt_pk_bf16_f32 v69, v74, v75\n\t"
  "v_cvt_pk_bf16_f32 v70, v76, v77\n\t"
  "v_cvt_pk_bf16_f32 v71, v78, v79\n\t"
  "v_cvt_pk_bf16_f32 v72, v80, v81\n\t"
  "v_cvt_pk_bf16_f32 v73, v82, v83\n\t"
  "v_cvt_pk_bf16_f32 v74, v84, v85\n\t"
  "v_cvt_pk_bf16_f32 v75, v86, v87\n\t"
  "v_cvt_pk_bf16_f32 v76, v88, v89\n\t"
  "v_cvt_pk_bf16_f32 v77, v90, v91\n\t"
  "v_cvt_pk_bf16_f32 v78, v92, v93\n\t"
  "v_cvt_pk_bf16_f32 v79, v94, v95\n\t"
  "v_cvt_pk_bf16_f32 v80, v96, v97\n\t"
  "v_cvt_pk_bf16_f32 v81, v98, v99\n\t"
  "v_cvt_pk_bf16_f32 v82, v100, v101\n\t"
  "v_cvt_pk_bf16_f32 v83, v102, v103\n\t"
  "v_cvt_pk_bf16_f32 v84, v104, v105\n\t"
  "v_cvt_pk_bf16_f32 v85, v106, v107\n\t"
  "v_cvt_pk_bf16_f32 v86, v108, v109\n\t"
  "v_cvt_pk_bf16_f32 v87, v110, v111\n\t"
  "v_cvt_pk_bf16_f32 v88, v112, v113\n\t"
  "v_cvt_pk_bf16_f32 v89, v114, v115\n\t"
  "v_cvt_pk_bf16_f32 v90, v116, v117\n\t"
  "v_cvt_pk_bf16_f32 v91, v118, v119\n\t"
  "v_cvt_pk_bf16_f32 v92, v120, v121\n\t"
  "v_cvt_pk_bf16_f32 v93, v122, v123\n\t"
  "v_cvt_pk_bf16_f32 v94, v124, v125\n\t"
  "v_cvt_pk_bf16_f32 v95, v126, v127\n\t"
  "v_cvt_pk_bf16_f32 v96, v0, v1\n\t"
  "v_cvt_pk_bf16_f32 v97, v2, v3\n\t"
  "v_cvt_pk_bf16_f32 v98, v4, v5\n\t"
  "v_cvt_pk_bf16_f32 v99, v6, v7\n\t"
  "v_cvt_pk_bf16_f32 v100, v8, v9\n\t"
  "v_cvt_pk_bf16_f32 v101, v10, v11\n\t"
  "v_cvt_pk_bf16_f32 v102, v12, v13\n\t"
  "v_cvt_pk_bf16_f32 v103, v14, v15\n\t"
  "v_cvt_pk_bf16_f32 v104, v16, v17\n\t"
  "v_cvt_pk_bf16_f32 v105, v18, v19\n\t"
  "v_cvt_pk_bf16_f32 v106, v20, v21\n\t"
  "v_cvt_pk_bf16_f32 v107, v22, v23\n\t"
  "v_cvt_pk_bf16_f32 v108, v24, v25\n\t"
  "v_cvt_pk_bf16_f32 v109, v26, v27\n\t"
  "v_cvt_pk_bf16_f32 v110, v28, v29\n\t"
  "v_cvt_pk_bf16_f32 v111, v30, v31\n\t"
  "v_cvt_pk_bf16_f32 v112, v32, v33\n\t"
  "v_cvt_pk_bf16_f32 v113, v34, v35\n\t"
  "v_cvt_pk_bf16_f32 v114, v36, v37\n\t"
  "v_cvt_pk_bf16_f32 v115, v38, v39\n\t"
  "v_cvt_pk_bf16_f32 v116, v40, v41\n\t"
  "v_cvt_pk_bf16_f32 v117, v42, v43\n\t"
  "v_cvt_pk_bf16_f32 v118, v44, v45\n\t"
  "v_cvt_pk_bf16_f32 v119, v46, v47\n\t"
  "v_cvt_pk_bf16_f32 v120, v48, v49\n\t"
  "v_cvt_pk_bf16_f32 v121, v50, v51\n\t"
  "v_cvt_pk_bf16_f32 v122, v52, v53\n\t"
  "v_cvt_pk_bf16_f32 v123, v54, v55\n\t"
  "v_cvt_pk_bf16_f32 v124, v56, v57\n\t"
  "v_cvt_pk_bf16_f32 v125, v58, v59\n\t"
  "v_cvt_pk_bf16_f32 v126, v60, v61\n\t"
  "v_cvt_pk_bf16_f32 v127, v62, v63\n\t"
  "s_waitcnt lgkmcnt(3)\n\t"
  "v_mfma_f32_32x32x16_bf16 v[0:15], v[160:163], v[128:131], 0\n\t"
  "ds_read_b64_tr_b16 v[168:169], v178 offset:16384\n\t"
  "ds_read_b64_tr_b16 v[170:171], v178 offset:16640\n\t"
  "s_waitcnt lgkmcnt(3)\n\t"
  "v_mfma_f32_32x32x16_bf16 v[16:31], v[164:167], v[128:131], 0\n\t"
  "ds_read_b128 v[144:147], v176 offset:1024\n\t"
  "ds_read_b64_tr_b16 v[172:173], v178 offset:16896\n\t"
  "s_waitcnt lgkmcnt(4)\n\t"
  "v_mfma_f32_32x32x16_bf16 v[32:47], v[160:163], v[132:135], 0\n\t"
  "ds_read_b64_tr_b16 v[174:175], v178 offset:17152\n\t"
  "ds_read_b128 v[148:151], v176 offset:17408\n\t"
  "v_mfma_f32_32x32x16_bf16 v[48:63], v[164:167], v[132:135], 0\n\t"
  "buffer_store_dwordx4 v[96:99], v232, s[48:51], s70 offen offset:0 sc1\n\t"
  "s_waitcnt vmcnt(10) lgkmcnt(0)\n\t"
  "s_barrier\n\t"
  "v_add_u32 v176, 32768, v187\n\t"
  "v_add_u32 v178, 32768, v188\n\t"
  "v_mfma_f32_32x32x16_bf16 v[0:15], v[168:171], v[144:147], v[0:15]\n\t"
  "ds_read_b64_tr_b16 v[160:161], v178 offset:0\n\t"
  "ds_read_b64_tr_b16 v[162:163], v178 offset:256\n\t"
  "v_mfma_f32_32x32x16_bf16 v[16:31], v[172:175], v[144:147], v[16:31]\n\t"
  "ds_read_b128 v[128:131], v176 offset:0\n\t"
  "ds_read_b64_tr_b16 v[164:165], v178 offset:512\n\t"
  "v_mfma_f32_32x32x16_bf16 v[32:47], v[168:171], v[148:151], v[32:47]\n\t"
  "ds_read_b64_tr_b16 v[166:167], v178 offset:768\n\t"
  "ds_read_b128 v[132:135], v176 offset:16384\n\t"
  "v_mfma_f32_32x32x16_bf16 v[48:63], v[172:175], v[148:151], v[48:63]\n\t"
  "buffer_store_dwordx4 v[100:103], v232, s[48:51], s70 offen offset:1024 sc1\n\t"
  "s_waitcnt lgkmcnt(3)\n\t"
  "v_mfma_f32_32x32x16_bf16 v[0:15], v[160:163], v[128:131], v[0:15]\n\t"
  "ds_read_b64_tr_b16 v[168:169], v178 offset:16384\n\t"
  "ds_read_b64_tr_b16 v[170:171], v178 offset:16640\n\t"
  "s_waitcnt lgkmcnt(3)\n\t"
  "v_mfma_f32_32x32x16_bf16 v[16:31], v[164:167], v[128:131], v[16:31]\n\t"
  "ds_read_b128 v[144:147], v176 offset:1024\n\t"
  "ds_read_b64_tr_b16 v[172:173], v178 offset:16896\n\t"
  "s_waitcnt lgkmcnt(4)\n\t"
  "v_mfma_f32_32x32x16_bf16 v[32:47], v[160:163], v[132:135], v[32:47]\n\t"
  "ds_read_b64_tr_b16 v[174:175], v178 offset:17152\n\t"
  "ds_read_b128 v[148:151], v176 offset:17408\n\t"
  "v_mfma_f32_32x32x16_bf16 v[48:63], v[164:167], v[132:135], v[48:63]\n\t"
  "buffer_store_dwordx4 v[104:107], v232, s[48:51], s70 offen offset:2048 sc1\n\t"
  "s_waitcnt vmcnt(9) lgkmcnt(0)\n\t"
  "s_barrier\n\t"
  "v_add_u32 v176, 65536, v187\n\t"
  "v_add_u32 v178, 65536, v188\n\t"
  "v_mfma_f32_32x32x16_bf16 v[0:15], v[168:171], v[144:147], v[0:15]\n\t"
  "ds_read_b64_tr_b16 v[160:161], v178 offset:0\n\t"
  "ds_read_b64_tr_b16 v[162:163], v178 offset:256\n\t"
  "v_mfma_f32_32x32x16_bf16 v[16:31], v[172:175], v[144:147], v[16:31]\n\t"
  "ds_read_b128 v[128:131], v176 offset:0\n\t"
  "ds_read_b64_tr_b16 v[164:165], v178 offset:512\n\t"
  "v_mfma_f32_32x32x16_bf16 v[32:47], v[168:171], v[148:151], v[32:47]\n\t"
  "ds_read_b64_tr_b16 v[166:167], v178 offset:768\n\t"
  "ds_read_b128 v[132:135], v176 offset:16384\n\t"
  "v_mfma_f32_32x32x16_bf16 v[48:63], v[172:175], v[148:151], v[48:63]\n\t"
  "buffer_store_dwordx4 v[108:111], v232, s[48:51], s70 offen offset:3072 sc1\n\t"
  "s_waitcnt lgkmcnt(3)\n\t"
  "v_mfma_f32_32x32x16_bf16 v[0:15], v[160:163], v[128:131], v[0:15]\n\t"
  "ds_read_b64_tr_b16 v[168:169], v178 offset:16384\n\t"
  "ds_read_b64_tr_b16 v[170:171], v178 offset:16640\n\t"
  "s_waitcnt lgkmcnt(3)\n\t"
  "v_mfma_f32_32x32x16_bf16 v[16:31], v[164:167], v[128:131], v[16:31]\n\t"
  "ds_read_b128 v[144:147], v176 offset:1024\n\t"
  "ds_read_b64_tr_b16 v[172:173], v178 offset:16896\n\t"
  "s_waitcnt lgkmcnt(4)\n\t"
  "v_mfma_f32_32x32x16_bf16 v[32:47], v[160:163], v[132:135], v[32:47]\n\t"
  "ds_read_b64_tr_b16 v[174:175], v178 offset:17152\n\t"
  "ds_read_b128 v[148:151], v176 offset:17408\n\t"
  "v_mfma_f32_32x32x16_bf16 v[48:63], v[164:167], v[132:135], v[48:63]\n\t"
  "buffer_store_dwordx4 v[112:115], v234, s[48:51], s70 offen offset:0 sc1\n\t"
  "s_waitcnt vmcnt(8) lgkmcnt(0)\n\t"
  "s_barrier\n\t"
  "v_add_u32 v176, 98304, v187\n\t"
  "v_add_u32 v178, 98304, v188\n\t"
  "v_mfma_f32_32x32x16_bf16 v[0:15], v[168:171], v[144:147], v[0:15]\n\t"
  "ds_read_b64_tr_b16 v[160:161], v178 offset:0\n\t"
  "ds_read_b64_tr_b16 v[162:163], v178 offset:256\n\t"
  "v_mfma_f32_32x32x16_bf16 v[16:31], v[172:175], v[144:147], v[16:31]\n\t"
  "ds_read_b128 v[128:131], v176 offset:0\n\t"
  "ds_read_b64_tr_b16 v[164:165], v178 offset:512\n\t"
  "v_mfma_f32_32x32x16_bf16 v[32:47], v[168:171], v[148:151], v[32:47]\n\t"
  "ds_read_b64_tr_b16 v[166:167], v178 offset:768\n\t"
  "ds_read_b128 v[132:135], v176 offset:16384\n\t"
  "v_mfma_f32_32x32x16_bf16 v[48:63], v[172:175], v[148:151], v[48:63]\n\t"
  "buffer_store_dwordx4 v[116:119], v234, s[48:51], s70 offen offset:1024 sc1\n\t"
  "s_waitcnt lgkmcnt(3)\n\t"
  "v_mfma_f32_32x32x16_bf16 v[0:15], v[160:163], v[128:131], v[0:15]\n\t"
  "ds_read_b64_tr_b16 v[168:169], v178 offset:16384\n\t"
  "ds_read_b64_tr_b16 v[170:171], v178 offset:16640\n\t"
  "s_waitcnt lgkmcnt(3)\n\t"
  "v_mfma_f32_32x32x16_bf16 v[16:31], v[164:167], v[128:131], v[16:31]\n\t"
  "ds_read_b128 v[144:147], v176 offset:1024\n\t"
  "ds_read_b64_tr_b16 v[172:173], v178 offset:16896\n\t"
  "s_waitcnt lgkmcnt(4)\n\t"
  "v_mfma_f32_32x32x16_bf16 v[32:47], v[160:163], v[132:135], v[32:47]\n\t"
  "ds_read_b64_tr_b16 v[174:175], v178 offset:17152\n\t"
  "ds_read_b128 v[148:151], v176 offset:17408\n\t"
  "v_mfma_f32_32x32x16_bf16 v[48:63], v[164:167], v[132:135], v[48:63]\n\t"
  "buffer_store_dwordx4 v[120:123], v234, s[48:51], s70 offen offset:2048 sc1\n\t"
  "s_waitcnt vmcnt(7) lgkmcnt(0)\n\t"
  "s_barrier\n\t"
  "v_add_u32 v176, 131072, v187\n\t"
  "v_add_u32 v178, 131072, v188\n\t"
  "v_mfma_f32_32x32x16_bf16 v[0:15], v[168:171], v[144:147], v[0:15]\n\t"
  "ds_read_b64_tr_b16 v[160:161], v178 offset:0\n\t"
  "ds_read_b64_tr_b16 v[162:163], v178 offset:256\n\t"
  "v_mfma_f32_32x32x16_bf16 v[16:31], v[172:175], v[144:147], v[16:31]\n\t"
  "ds_read_b128 v[128:131], v176 offset:0\n\t"
  "ds_read_b64_tr_b16 v[164:165], v178 offset:512\n\t"
  "v_mfma_f32_32x32x16_bf16 v[32:47], v[168:171], v[148:151], v[32:47]\n\t"
  "ds_read_b64_tr_b16 v[166:167], v178 offset:768\n\t"
  "ds_read_b128 v[132:135], v176 offset:16384\n\t"
  "v_mfma_f32_32x32x16_bf16 v[48:63], v[172:175], v[148:151], v[48:63]\n\t"
  "buffer_store_dwordx4 v[124:127], v234, s[48:51], s70 offen offset:3072 sc1\n\t"
  "s_waitcnt lgkmcnt(3)\n\t"
  "v_mfma_f32_32x32x16_bf16 v[0:15], v[160:163], v[128:131], v[0:15]\n\t"
  "ds_read_b64_tr_b16 v[168:169], v178 offset:16384\n\t"
  "ds_read_b64_tr_b16 v[170:171], v178 offset:16640\n\t"
  "s_waitcnt lgkmcnt(3)\n\t"
  "v_mfma_f32_32x32x16_bf16 v[16:31], v[164:167], v[128:131], v[16:31]\n\t"
  "ds_read_b128 v[144:147], v176 offset:1024\n\t"
  "ds_read_b64_tr_b16 v[172:173], v178 offset:16896\n\t"
  "s_waitcnt lgkmcnt(4)\n\t"
  "v_mfma_f32_32x32x16_bf16 v[32:47], v[160:163], v[132:135], v[32:47]\n\t"
  "ds_read_b64_tr_b16 v[174:175], v178 offset:17152\n\t"
  "ds_read_b128 v[148:151], v176 offset:17408\n\t"
  "v_mfma_f32_32x32x16_bf16 v[48:63], v[164:167], v[132:135], v[48:63]\n\t"
  "buffer_store_dwordx4 v[64:67], v232, s[48:51], s68 offen offset:0 sc1\n\t"
  "s_waitcnt lgkmcnt(0)\n\t"
  "s_barrier\n\t"
  "v_add_u32 v176, 0, v187\n\t"
  "v_add_u32 v178, 0, v188\n\t"
  "v_mfma_f32_32x32x16_bf16 v[0:15], v[168:171], v[144:147], v[0:15]\n\t"
  "ds_read_b64_tr_b16 v[160:161], v178 offset:0\n\t"
  "ds_read_b64_tr_b16 v[162:163], v178 offset:256\n\t"
  "v_mfma_f32_32x32x16_bf16 v[16:31], v[172:175], v[144:147], v[16:31]\n\t"
  "ds_read_b128 v[128:131], v176 offset:0\n\t"
  "ds_read_b64_tr_b16 v[164:165], v178 offset:512\n\t"
  "v_mfma_f32_32x32x16_bf16 v[32:47], v[168:171], v[148:151], v[32:47]\n\t"
  "ds_read_b64_tr_b16 v[166:167], v178 offset:768\n\t"
  "ds_read_b128 v[132:135], v176 offset:16384\n\t"
  "v_mfma_f32_32x32x16_bf16 v[48:63], v[172:175], v[148:151], v[48:63]\n\t"
  "buffer_store_dwordx4 v[68:71], v232, s[48:51], s68 offen offset:1024 sc1\n\t"
  "s_waitcnt lgkmcnt(3)\n\t"
  "v_mfma_f32_32x32x16_bf16 v[0:15], v[160:163], v[128:131], v[0:15]\n\t"
  "ds_read_b64_tr_b16 v[168:169], v178 offset:16384\n\t"
  "ds_read_b64_tr_b16 v[170:171], v178 offset:16640\n\t"
  "s_waitcnt lgkmcnt(3)\n\t"
  "v_mfma_f32_32x32x16_bf16 v[16:31], v[164:167], v[128:131], v[16:31]\n\t"
  "ds_read_b128 v[144:147], v176 offset:1024\n\t"
  "ds_read_b64_tr_b16 v[172:173], v178 offset:16896\n\t"
  "s_waitcnt lgkmcnt(4)\n\t"
  "v_mfma_f32_32x32x16_bf16 v[32:47], v[160:163], v[132:135], v[32:47]\n\t"
  "ds_read_b64_tr_b16 v[174:175], v178 offset:17152\n\t"
  "ds_read_b128 v[148:151], v176 offset:17408\n\t"
  "v_mfma_f32_32x32x16_bf16 v[48:63], v[164:167], v[132:135], v[48:63]\n\t"
  "buffer_store_dwordx4 v[72:75], v232, s[48:51], s68 offen offset:2048 sc1\n\t"
  "s_waitcnt lgkmcnt(0)\n\t"
  "s_barrier\n\t"
  "v_add_u32 v176, 32768, v187\n\t"
  "v_add_u32 v178, 32768, v188\n\t"
  "v_mfma_f32_32x32x16_bf16 v[0:15], v[168:171], v[144:147], v[0:15]\n\t"
  "ds_read_b64_tr_b16 v[160:161], v178 offset:0\n\t"
  "ds_read_b64_tr_b16 v[162:163], v178 offset:256\n\t"
  "v_mfma_f32_32x32x16_bf16 v[16:31], v[172:175], v[144:147], v[16:31]\n\t"
  "ds_read_b128 v[128:131], v176 offset:0\n\t"
  "ds_read_b64_tr_b16 v[164:165], v178 offset:512\n\t"
  "v_mfma_f32_32x32x16_bf16 v[32:47], v[168:171], v[148:151], v[32:47]\n\t"
  "ds_read_b64_tr_b16 v[166:167], v178 offset:768\n\t"
  "ds_read_b128 v[132:135], v176 offset:16384\n\t"
  "v_mfma_f32_32x32x16_bf16 v[48:63], v[172:175], v[148:151], v[48:63]\n\t"
  "buffer_store_dwordx4 v[76:79], v232, s[48:51], s68 offen offset:3072 sc1\n\t"
  "s_waitcnt lgkmcnt(3)\n\t"
  "v_mfma_f32_32x32x16_bf16 v[0:15], v[160:163], v[128:131], v[0:15]\n\t"
  "ds_read_b64_tr_b16 v[168:169], v178 offset:16384\n\t"
  "ds_read_b64_tr_b16 v[170:171], v178 offset:16640\n\t"
  "s_waitcnt lgkmcnt(3)\n\t"
  "v_mfma_f32_32x32x16_bf16 v[16:31], v[164:167], v[128:131], v[16:31]\n\t"
  "ds_read_b128 v[144:147], v176 offset:1024\n\t"
  "ds_read_b64_tr_b16 v[172:173], v178 offset:16896\n\t"
  "s_waitcnt lgkmcnt(4)\n\t"
  "v_mfma_f32_32x32x16_bf16 v[32:47], v[160:163], v[132:135], v[32:47]\n\t"
  "ds_read_b64_tr_b16 v[174:175], v178 offset:17152\n\t"
  "ds_read_b128 v[148:151], v176 offset:17408\n\t"
  "v_mfma_f32_32x32x16_bf16 v[48:63], v[164:167], v[132:135], v[48:63]\n\t"
  "buffer_store_dwordx4 v[80:83], v234, s[48:51], s68 offen offset:0 sc1\n\t"
  "s_waitcnt lgkmcnt(0)\n\t"
  "s_barrier\n\t"
  "v_add_u32 v176, 65536, v187\n\t"
  "v_add_u32 v178, 65536, v188\n\t"
  "v_mfma_f32_32x32x16_bf16 v[0:15], v[168:171], v[144:147], v[0:15]\n\t"
  "ds_read_b64_tr_b16 v[160:161], v178 offset:0\n\t"
  "ds_read_b64_tr_b16 v[162:163], v178 offset:256\n\t"
  "v_mfma_f32_32x32x16_bf16 v[16:31], v[172:175], v[144:147], v[16:31]\n\t"
  "ds_read_b128 v[128:131], v176 offset:0\n\t"
  "ds_read_b64_tr_b16 v[164:165], v178 offset:512\n\t"
  "v_mfma_f32_32x32x16_bf16 v[32:47], v[168:171], v[148:151], v[32:47]\n\t"
  "ds_read_b64_tr_b16 v[166:167], v178 offset:768\n\t"
  "ds_read_b128 v[132:135], v176 offset:16384\n\t"
  "v_mfma_f32_32x32x16_bf16 v[48:63], v[172:175], v[148:151], v[48:63]\n\t"
  "buffer_store_dwordx4 v[84:87], v234, s[48:51], s68 offen offset:1024 sc1\n\t"
  "s_waitcnt lgkmcnt(3)\n\t"
  "v_mfma_f32_32x32x16_bf16 v[0:15], v[160:163], v[128:131], v[0:15]\n\t"
  "ds_read_b64_tr_b16 v[168:169], v178 offset:16384\n\t"
  "ds_read_b64_tr_b16 v[170:171], v178 offset:16640\n\t"
  "s_waitcnt lgkmcnt(3)\n\t"
  "v_mfma_f32_32x32x16_bf16 v[16:31], v[164:167], v[128:131], v[16:31]\n\t"
  "ds_read_b128 v[144:147], v176 offset:1024\n\t"
  "ds_read_b64_tr_b16 v[172:173], v178 offset:16896\n\t"
  "s_waitcnt lgkmcnt(4)\n\t"
  "v_mfma_f32_32x32x16_bf16 v[32:47], v[160:163], v[132:135], v[32:47]\n\t"
  "ds_read_b64_tr_b16 v[174:175], v178 offset:17152\n\t"
  "ds_read_b128 v[148:151], v176 offset:17408\n\t"
  "v_mfma_f32_32x32x16_bf16 v[48:63], v[164:167], v[132:135], v[48:63]\n\t"
  "buffer_store_dwordx4 v[88:91], v234, s[48:51], s68 offen offset:2048 sc1\n\t"
  "s_waitcnt lgkmcnt(0)\n\t"
  "s_barrier\n\t"
  "v_add_u32 v176, 98304, v187\n\t"
  "v_add_u32 v178, 98304, v188\n\t"
  "v_mfma_f32_32x32x16_bf16 v[0:15], v[168:171], v[144:147], v[0:15]\n\t"
  "ds_read_b64_tr_b16 v[160:161], v178 offset:0\n\t"
  "ds_read_b64_tr_b16 v[162:163], v178 offset:256\n\t"
  "v_mfma_f32_32x32x16_bf16 v[16:31], v[172:175], v[144:147], v[16:31]\n\t"
  "ds_read_b128 v[128:131], v176 offset:0\n\t"
  "ds_read_b64_tr_b16 v[164:165], v178 offset:512\n\t"
  "v_mfma_f32_32x32x16_bf16 v[32:47], v[168:171], v[148:151], v[32:47]\n\t"
  "ds_read_b64_tr_b16 v[166:167], v178 offset:768\n\t"
  "ds_read_b128 v[132:135], v176 offset:16384\n\t"
  "v_mfma_f32_32x32x16_bf16 v[48:63], v[172:175], v[148:151], v[48:63]\n\t"
  "buffer_store_dwordx4 v[92:95], v234, s[48:51], s68 offen offset:3072 sc1\n\t"
  "s_waitcnt lgkmcnt(3)\n\t"
  "v_mfma_f32_32x32x16_bf16 v[0:15], v[160:163], v[128:131], v[0:15]\n\t"
  "ds_read_b64_tr_b16 v[168:169], v178 offset:16384\n\t"
  "ds_read_b64_tr_b16 v[170:171], v178 offset:16640\n\t"
  "s_waitcnt lgkmcnt(3)\n\t"
  "v_mfma_f32_32x32x16_bf16 v[16:31], v[164:167], v[128:131], v[16:31]\n\t"
  "ds_read_b128 v[144:147], v176 offset:1024\n\t"
  "ds_read_b64_tr_b16 v[172:173], v178 offset:16896\n\t"
  "s_waitcnt lgkmcnt(4)\n\t"
  "v_mfma_f32_32x32x16_bf16 v[32:47], v[160:163], v[132:135], v[32:47]\n\t"
  "ds_read_b64_tr_b16 v[174:175], v178 offset:17152\n\t"
  "ds_read_b128 v[148:151], v176 offset:17408\n\t"
  "v_mfma_f32_32x32x16_bf16 v[48:63], v[164:167], v[132:135], v[48:63]\n\t"
  "s_waitcnt lgkmcnt(0)\n\t"
  "s_barrier\n\t"
  "v_add_u32 v176, 131072, v187\n\t"
  "v_add_u32 v178, 131072, v188\n\t"
  "v_mfma_f32_32x32x16_bf16 v[0:15], v[168:171], v[144:147], v[0:15]\n\t"
  "ds_read_b64_tr_b16 v[160:161], v178 offset:0\n\t"
  "ds_read_b64_tr_b16 v[162:163], v178 offset:256\n\t"
  "v_mfma_f32_32x32x16_bf16 v[16:31], v[172:175], v[144:147], v[16:31]\n\t"
  "ds_read_b128 v[128:131], v176 offset:0\n\t"
  "ds_read_b64_tr_b16 v[164:165], v178 offset:512\n\t"
  "s_add_u32 m0, s67, 98304\n\t"
  "s_add_u32 s73, s75, 26624\n\t"
  "buffer_load_dwordx4 v186, s[52:55], s73 offen sc1 lds\n\t"
  "v_mfma_f32_32x32x16_bf16 v[32:47], v[168:171], v[148:151], v[32:47]\n\t"
  "ds_read_b64_tr_b16 v[166:167], v178 offset:768\n\t"
  "ds_read_b128 v[132:135], v176 offset:16384\n\t"
  "buffer_load_dwordx4 v186, s[52:55], s73 offen offset:1024 sc1 lds\n\t"
  "s_add_u32 s73, s76, 3407872\n\t"
  "v_mfma_f32_32x32x16_bf16 v[48:63], v[172:175], v[148:151], v[48:63]\n\t"
  "buffer_load_dwordx4 v189, s[56:59], s73 offen offset:2048 lds\n\t"
  "s_waitcnt lgkmcnt(3)\n\t"
  "v_mfma_f32_32x32x16_bf16 v[0:15], v[160:163], v[128:131], v[0:15]\n\t"
  "ds_read_b64_tr_b16 v[168:169], v178 offset:16384\n\t"
  "ds_read_b64_tr_b16 v[170:171], v178 offset:16640\n\t"
  "s_waitcnt lgkmcnt(3)\n\t"
  "v_mfma_f32_32x32x16_bf16 v[16:31], v[164:167], v[128:131], v[16:31]\n\t"
  "ds_read_b128 v[144:147], v176 offset:1024\n\t"
  "ds_read_b64_tr_b16 v[172:173], v178 offset:16896\n\t"
  "s_add_u32 m0, s67, 114688\n\t"
  "s_add_u32 s73, s78, 26624\n\t"
  "buffer_load_dwordx4 v186, s[52:55], s73 offen sc1 lds\n\t"
  "s_waitcnt lgkmcnt(4)\n\t"
  "v_mfma_f32_32x32x16_bf16 v[32:47], v[160:163], v[132:135], v[32:47]\n\t"
  "ds_read_b64_tr_b16 v[174:175], v178 offset:17152\n\t"
  "ds_read_b128 v[148:151], v176 offset:17408\n\t"
  "buffer_load_dwordx4 v186, s[52:55], s73 offen offset:1024 sc1 lds\n\t"
  "s_add_u32 s73, s79, 3407872\n\t"
  "v_mfma_f32_32x32x16_bf16 v[48:63], v[164:167], v[132:135], v[48:63]\n\t"
  "buffer_load_dwordx4 v189, s[56:59], s73 offen offset:2048 lds\n\t"
  "s_waitcnt lgkmcnt(0)\n\t"
  "s_barrier\n\t"
  "v_add_u32 v176, 0, v187\n\t"
  "v_add_u32 v178, 0, v188\n\t"
  "v_mfma_f32_32x32x16_bf16 v[0:15], v[168:171], v[144:147], v[0:15]\n\t"
  "ds_read_b64_tr_b16 v[160:161], v178 offset:0\n\t"
  "ds_read_b64_tr_b16 v[162:163], v178 offset:256\n\t"
  "v_mfma_f32_32x32x16_bf16 v[16:31], v[172:175], v[144:147], v[16:31]\n\t"
  "ds_read_b128 v[128:131], v176 offset:0\n\t"
  "ds_read_b64_tr_b16 v[164:165], v178 offset:512\n\t"
  "s_add_u32 m0, s67, 131072\n\t"
  "s_add_u32 s73, s75, 28672\n\t"
  "buffer_load_dwordx4 v186, s[52:55], s73 offen sc1 lds\n\t"
  "v_mfma_f32_32x32x16_bf16 v[32:47], v[168:171], v[148:151], v[32:47]\n\t"
  "ds_read_b64_tr_b16 v[166:167], v178 offset:768\n\t"
  "ds_read_b128 v[132:135], v176 offset:16384\n\t"
  "buffer_load_dwordx4 v186, s[52:55], s73 offen offset:1024 sc1 lds\n\t"
  "s_add_u32 s73, s76, 3670016\n\t"
  "v_mfma_f32_32x32x16_bf16 v[48:63], v[172:175], v[148:151], v[48:63]\n\t"
  "buffer_load_dwordx4 v189, s[56:59], s73 offen offset:2048 lds\n\t"
  "s_waitcnt lgkmcnt(3)\n\t"
  "v_mfma_f32_32x32x16_bf16 v[0:15], v[160:163], v[128:131], v[0:15]\n\t"
  "ds_read_b64_tr_b16 v[168:169], v178 offset:16384\n\t"
  "ds_read_b64_tr_b16 v[170:171], v178 offset:16640\n\t"
  "s_waitcnt lgkmcnt(3)\n\t"
  "v_mfma_f32_32x32x16_bf16 v[16:31], v[164:167], v[128:131], v[16:31]\n\t"
  "ds_read_b128 v[144:147], v176 offset:1024\n\t"
  "ds_read_b64_tr_b16 v[172:173], v178 offset:16896\n\t"
  "s_add_u32 m0, s67, 147456\n\t"
  "s_add_u32 s73, s78, 28672\n\t"
  "buffer_load_dwordx4 v186, s[52:55], s73 offen sc1 lds\n\t"
  "s_waitcnt lgkmcnt(4)\n\t"
  "v_mfma_f32_32x32x16_bf16 v[32:47], v[160:163], v[132:135], v[32:47]\n\t"
  "ds_read_b64_tr_b16 v[174:175], v178 offset:17152\n\t"
  "ds_read_b128 v[148:151], v176 offset:17408\n\t"
  "buffer_load_dwordx4 v186, s[52:55], s73 offen offset:1024 sc1 lds\n\t"
  "s_add_u32 s73, s79, 3670016\n\t"
  "v_mfma_f32_32x32x16_bf16 v[48:63], v[164:167], v[132:135], v[48:63]\n\t"
  "buffer_load_dwordx4 v189, s[56:59], s73 offen offset:2048 lds\n\t"
  "s_waitcnt lgkmcnt(0)\n\t"
  "s_barrier\n\t"
  "v_add_u32 v176, 32768, v187\n\t"
  "v_add_u32 v178, 32768, v188\n\t"
  "v_mfma_f32_32x32x16_bf16 v[0:15], v[168:171], v[144:147], v[0:15]\n\t"
  "ds_read_b64_tr_b16 v[160:161], v178 offset:0\n\t"
  "ds_read_b64_tr_b16 v[162:163], v178 offset:256\n\t"
  "v_mfma_f32_32x32x16_bf16 v[16:31], v[172:175], v[144:147], v[16:31]\n\t"
  "ds_read_b128 v[128:131], v176 offset:0\n\t"
  "ds_read_b64_tr_b16 v[164:165], v178 offset:512\n\t"
  "s_add_u32 m0, s67, 0\n\t"
  "s_add_u32 s73, s75, 30720\n\t"
  "buffer_load_dwordx4 v186, s[52:55], s73 offen sc1 lds\n\t"
  "v_mfma_f32_32x32x16_bf16 v[32:47], v[168:171], v[148:151], v[32:47]\n\t"
  "ds_read_b64_tr_b16 v[166:167], v178 offset:768\n\t"
  "ds_read_b128 v[132:135], v176 offset:16384\n\t"
  "buffer_load_dwordx4 v186, s[52:55], s73 offen offset:1024 sc1 lds\n\t"
  "s_add_u32 s73, s76, 3932160\n\t"
  "v_mfma_f32_32x32x16_bf16 v[48:63], v[172:175], v[148:151], v[48:63]\n\t"
  "buffer_load_dwordx4 v189, s[56:59], s73 offen offset:2048 lds\n\t"
  "s_waitcnt lgkmcnt(3)\n\t"
  "v_mfma_f32_32x32x16_bf16 v[0:15], v[160:163], v[128:131], v[0:15]\n\t"
  "ds_read_b64_tr_b16 v[168:169], v178 offset:16384\n\t"
  "ds_read_b64_tr_b16 v[170:171], v178 offset:16640\n\t"
  "s_waitcnt lgkmcnt(3)\n\t"
  "v_mfma_f32_32x32x16_bf16 v[16:31], v[164:167], v[128:131], v[16:31]\n\t"
  "ds_read_b128 v[144:147], v176 offset:1024\n\t"
  "ds_read_b64_tr_b16 v[172:173], v178 offset:16896\n\t"
  "s_add_u32 m0, s67, 16384\n\t"
  "s_add_u32 s73, s78, 30720\n\t"
  "buffer_load_dwordx4 v186, s[52:55], s73 offen sc1 lds\n\t"
  "s_waitcnt lgkmcnt(4)\n\t"
  "v_mfma_f32_32x32x16_bf16 v[32:47], v[160:163], v[132:135], v[32:47]\n\t"
  "ds_read_b64_tr_b16 v[174:175], v178 offset:17152\n\t"
  "ds_read_b128 v[148:151], v176 offset:17408\n\t"
  "buffer_load_dwordx4 v186, s[52:55], s73 offen offset:1024 sc1 lds\n\t"
  "s_add_u32 s73, s79, 3932160\n\t"
  "v_mfma_f32_32x32x16_bf16 v[48:63], v[164:167], v[132:135], v[48:63]\n\t"
  "buffer_load_dwordx4 v189, s[56:59], s73 offen offset:2048 lds\n\t"
  "s_waitcnt lgkmcnt(0)\n\t"
  "s_barrier\n\t"
  "v_add_u32 v176, 65536, v187\n\t"
  "v_add_u32 v178, 65536, v188\n\t"
  "v_mfma_f32_32x32x16_bf16 v[0:15], v[168:171], v[144:147], v[0:15]\n\t"
  "ds_read_b64_tr_b16 v[160:161], v178 offset:0\n\t"
  "ds_read_b64_tr_b16 v[162:163], v178 offset:256\n\t"
  "v_mfma_f32_32x32x16_bf16 v[16:31], v[172:175], v[144:147], v[16:31]\n\t"
  "ds_read_b128 v[128:131], v176 offset:0\n\t"
  "ds_read_b64_tr_b16 v[164:165], v178 offset:512\n\t"
  "s_add_u32 m0, s67, 32768\n\t"
  "s_add_u32 s73, s75, 32768\n\t"
  "buffer_load_dwordx4 v186, s[52:55], s73 offen sc1 lds\n\t"
  "v_mfma_f32_32x32x16_bf16 v[32:47], v[168:171], v[148:151], v[32:47]\n\t"
  "ds_read_b64_tr_b16 v[166:167], v178 offset:768\n\t"
  "ds_read_b128 v[132:135], v176 offset:16384\n\t"
  "buffer_load_dwordx4 v186, s[52:55], s73 offen offset:1024 sc1 lds\n\t"
  "s_add_u32 s73, s76, 4194304\n\t"
  "v_mfma_f32_32x32x16_bf16 v[48:63], v[172:175], v[148:151], v[48:63]\n\t"
  "buffer_load_dwordx4 v189, s[56:59], s73 offen offset:2048 lds\n\t"
  "s_waitcnt lgkmcnt(3)\n\t"
  "v_mfma_f32_32x32x16_bf16 v[0:15], v[160:163], v[128:131], v[0:15]\n\t"
  "ds_read_b64_tr_b16 v[168:169], v178 offset:16384\n\t"
  "ds_read_b64_tr_b16 v[170:171], v178 offset:16640\n\t"
  "s_waitcnt lgkmcnt(3)\n\t"
  "v_mfma_f32_32x32x16_bf16 v[16:31], v[164:167], v[128:131], v[16:31]\n\t"
  "ds_read_b128 v[144:147], v176 offset:1024\n\t"
  "ds_read_b64_tr_b16 v[172:173], v178 offset:16896\n\t"
  "s_add_u32 m0, s67, 49152\n\t"
  "s_add_u32 s73, s78, 32768\n\t"
  "buffer_load_dwordx4 v186, s[52:55], s73 offen sc1 lds\n\t"
  "s_waitcnt lgkmcnt(4)\n\t"
  "v_mfma_f32_32x32x16_bf16 v[32:47], v[160:163], v[132:135], v[32:47]\n\t"
  "ds_read_b64_tr_b16 v[174:175], v178 offset:17152\n\t"
  "ds_read_b128 v[148:151], v176 offset:17408\n\t"
  "buffer_load_dwordx4 v186, s[52:55], s73 offen offset:1024 sc1 lds\n\t"
  "s_add_u32 s73, s79, 4194304\n\t"
  "v_mfma_f32_32x32x16_bf16 v[48:63], v[164:167], v[132:135], v[48:63]\n\t"
  "buffer_load_dwordx4 v189, s[56:59], s73 offen offset:2048 lds\n\t"
  "s_waitcnt vmcnt(18) lgkmcnt(0)\n\t"
  "s_barrier\n\t"
  "v_add_u32 v176, 98304, v187\n\t"
  "v_add_u32 v178, 98304, v188\n\t"
  "v_mfma_f32_32x32x16_bf16 v[0:15], v[168:171], v[144:147], v[0:15]\n\t"
  "ds_read_b64_tr_b16 v[160:161], v178 offset:0\n\t"
  "ds_read_b64_tr_b16 v[162:163], v178 offset:256\n\t"
  "v_mfma_f32_32x32x16_bf16 v[16:31], v[172:175], v[144:147], v[16:31]\n\t"
  "ds_read_b128 v[128:131], v176 offset:0\n\t"
  "ds_read_b64_tr_b16 v[164:165], v178 offset:512\n\t"
  "s_add_u32 m0, s67, 65536\n\t"
  "s_add_u32 s73, s75, 34816\n\t"
  "buffer_load_dwordx4 v186, s[52:55], s73 offen sc1 lds\n\t"
  "v_mfma_f32_32x32x16_bf16 v[32:47], v[168:171], v[148:151], v[32:47]\n\t"
  "ds_read_b64_tr_b16 v[166:167], v178 offset:768\n\t"
  "ds_read_b128 v[132:135], v176 offset:16384\n\t"
  "buffer_load_dwordx4 v186, s[52:55], s73 offen offset:1024 sc1 lds\n\t"
  "s_add_u32 s73, s76, 4456448\n\t"
  "v_mfma_f32_32x32x16_bf16 v[48:63], v[172:175], v[148:151], v[48:63]\n\t"
  "buffer_load_dwordx4 v189, s[56:59], s73 offen offset:2048 lds\n\t"
  "s_waitcnt lgkmcnt(3)\n\t"
  "v_mfma_f32_32x32x16_bf16 v[0:15], v[160:163], v[128:131], v[0:15]\n\t"
  "ds_read_b64_tr_b16 v[168:169], v178 offset:16384\n\t"
  "ds_read_b64_tr_b16 v[170:171], v178 offset:16640\n\t"
  "s_waitcnt lgkmcnt(3)\n\t"
  "v_mfma_f32_32x32x16_bf16 v[16:31], v[164:167], v[128:131], v[16:31]\n\t"
  "ds_read_b128 v[144:147], v176 offset:1024\n\t"
  "ds_read_b64_tr_b16 v[172:173], v178 offset:16896\n\t"
  "s_add_u32 m0, s67, 81920\n\t"
  "s_add_u32 s73, s78, 34816\n\t"
  "buffer_load_dwordx4 v186, s[52:55], s73 offen sc1 lds\n\t"
  "s_waitcnt lgkmcnt(4)\n\t"
  "v_mfma_f32_32x32x16_bf16 v[32:47], v[160:163], v[132:135], v[32:47]\n\t"
  "ds_read_b64_tr_b16 v[174:175], v178 offset:17152\n\t"
  "ds_read_b128 v[148:151], v176 offset:17408\n\t"
  "buffer_load_dwordx4 v186, s[52:55], s73 offen offset:1024 sc1 lds\n\t"
  "s_add_u32 s73, s79, 4456448\n\t"
  "v_mfma_f32_32x32x16_bf16 v[48:63], v[164:167], v[132:135], v[48:63]\n\t"
  "buffer_load_dwordx4 v189, s[56:59], s73 offen offset:2048 lds\n\t"
  "s_waitcnt vmcnt(18) lgkmcnt(0)\n\t"
  "s_barrier\n\t"
  "v_add_u32 v176, 131072, v187\n\t"
  "v_add_u32 v178, 131072, v188\n\t"
  "v_mfma_f32_32x32x16_bf16 v[0:15], v[168:171], v[144:147], v[0:15]\n\t"
  "ds_read_b64_tr_b16 v[160:161], v178 offset:0\n\t"
  "ds_read_b64_tr_b16 v[162:163], v178 offset:256\n\t"
  "v_mfma_f32_32x32x16_bf16 v[16:31], v[172:175], v[144:147], v[16:31]\n\t"
  "ds_read_b128 v[128:131], v176 offset:0\n\t"
  "ds_read_b64_tr_b16 v[164:165], v178 offset:512\n\t"
  "s_add_u32 m0, s67, 98304\n\t"
  "s_add_u32 s73, s75, 36864\n\t"
  "buffer_load_dwordx4 v186, s[52:55], s73 offen sc1 lds\n\t"
  "v_mfma_f32_32x32x16_bf16 v[32:47], v[168:171], v[148:151], v[32:47]\n\t"
  "ds_read_b64_tr_b16 v[166:167], v178 offset:768\n\t"
  "ds_read_b128 v[132:135], v176 offset:16384\n\t"
  "buffer_load_dwordx4 v186, s[52:55], s73 offen offset:1024 sc1 lds\n\t"
  "s_add_u32 s73, s76, 4718592\n\t"
  "v_mfma_f32_32x32x16_bf16 v[48:63], v[172:175], v[148:151], v[48:63]\n\t"
  "buffer_load_dwordx4 v189, s[56:59], s73 offen offset:2048 lds\n\t"
  "s_waitcnt lgkmcnt(3)\n\t"
  "v_mfma_f32_32x32x16_bf16 v[0:15], v[160:163], v[128:131], v[0:15]\n\t"
  "ds_read_b64_tr_b16 v[168:169], v178 offset:16384\n\t"
  "ds_read_b64_tr_b16 v[170:171], v178 offset:16640\n\t"
  "s_waitcnt lgkmcnt(3)\n\t"
  "v_mfma_f32_32x32x16_bf16 v[16:31], v[164:167], v[128:131], v[16:31]\n\t"
  "ds_read_b128 v[144:147], v176 offset:1024\n\t"
  "ds_read_b64_tr_b16 v[172:173], v178 offset:16896\n\t"
  "s_add_u32 m0, s67, 114688\n\t"
  "s_add_u32 s73, s78, 36864\n\t"
  "buffer_load_dwordx4 v186, s[52:55], s73 offen sc1 lds\n\t"
  "s_waitcnt lgkmcnt(4)\n\t"
  "v_mfma_f32_32x32x16_bf16 v[32:47], v[160:163], v[132:135], v[32:47]\n\t"
  "ds_read_b64_tr_b16 v[174:175], v178 offset:17152\n\t"
  "ds_read_b128 v[148:151], v176 offset:17408\n\t"
  "buffer_load_dwordx4 v186, s[52:55], s73 offen offset:1024 sc1 lds\n\t"
  "s_add_u32 s73, s79, 4718592\n\t"
  "v_mfma_f32_32x32x16_bf16 v[48:63], v[164:167], v[132:135], v[48:63]\n\t"
  "buffer_load_dwordx4 v189, s[56:59], s73 offen offset:2048 lds\n\t"
  "s_waitcnt vmcnt(18) lgkmcnt(0)\n\t"
  "s_barrier\n\t"
  "v_add_u32 v176, 0, v187\n\t"
  "v_add_u32 v178, 0, v188\n\t"
  "v_mfma_f32_32x32x16_bf16 v[0:15], v[168:171], v[144:147], v[0:15]\n\t"
  "ds_read_b64_tr_b16 v[160:161], v178 offset:0\n\t"
  "ds_read_b64_tr_b16 v[162:163], v178 offset:256\n\t"
  "v_mfma_f32_32x32x16_bf16 v[16:31], v[172:175], v[144:147], v[16:31]\n\t"
  "ds_read_b128 v[128:131], v176 offset:0\n\t"
  "ds_read_b64_tr_b16 v[164:165], v178 offset:512\n\t"
  "s_add_u32 m0, s67, 131072\n\t"
  "s_add_u32 s73, s75, 38912\n\t"
  "buffer_load_dwordx4 v186, s[52:55], s73 offen sc1 lds\n\t"
  "v_mfma_f32_32x32x16_bf16 v[32:47], v[168:171], v[148:151], v[32:47]\n\t"
  "ds_read_b64_tr_b16 v[166:167], v178 offset:768\n\t"
  "ds_read_b128 v[132:135], v176 offset:16384\n\t"
  "buffer_load_dwordx4 v186, s[52:55], s73 offen offset:1024 sc1 lds\n\t"
  "s_add_u32 s73, s76, 4980736\n\t"
  "v_mfma_f32_32x32x16_bf16 v[48:63], v[172:175], v[148:151], v[48:63]\n\t"
  "buffer_load_dwordx4 v189, s[56:59], s73 offen offset:2048 lds\n\t"
  "s_waitcnt lgkmcnt(3)\n\t"
  "v_mfma_f32_32x32x16_bf16 v[0:15], v[160:163], v[128:131], v[0:15]\n\t"
  "ds_read_b64_tr_b16 v[168:169], v178 offset:16384\n\t"
  "ds_read_b64_tr_b16 v[170:171], v178 offset:16640\n\t"
  "s_waitcnt lgkmcnt(3)\n\t"
  "v_mfma_f32_32x32x16_bf16 v[16:31], v[164:167], v[128:131], v[16:31]\n\t"
  "ds_read_b128 v[144:147], v176 offset:1024\n\t"
  "ds_read_b64_tr_b16 v[172:173], v178 offset:16896\n\t"
  "s_add_u32 m0, s67, 147456\n\t"
  "s_add_u32 s73, s78, 38912\n\t"
  "buffer_load_dwordx4 v186, s[52:55], s73 offen sc1 lds\n\t"
  "s_waitcnt lgkmcnt(4)\n\t"
  "v_mfma_f32_32x32x16_bf16 v[32:47], v[160:163], v[132:135], v[32:47]\n\t"
  "ds_read_b64_tr_b16 v[174:175], v178 offset:17152\n\t"
  "ds_read_b128 v[148:151], v176 offset:17408\n\t"
  "buffer_load_dwordx4 v186, s[52:55], s73 offen offset:1024 sc1 lds\n\t"
  "s_add_u32 s73, s79, 4980736\n\t"
  "v_mfma_f32_32x32x16_bf16 v[48:63], v[164:167], v[132:135], v[48:63]\n\t"
  "buffer_load_dwordx4 v189, s[56:59], s73 offen offset:2048 lds\n\t"
  "s_waitcnt vmcnt(18) lgkmcnt(0)\n\t"
  "s_barrier\n\t"
  "v_add_u32 v176, 32768, v187\n\t"
  "v_add_u32 v178, 32768, v188\n\t"
  "v_mfma_f32_32x32x16_bf16 v[0:15], v[168:171], v[144:147], v[0:15]\n\t"
  "ds_read_b64_tr_b16 v[160:161], v178 offset:0\n\t"
  "ds_read_b64_tr_b16 v[162:163], v178 offset:256\n\t"
  "v_mfma_f32_32x32x16_bf16 v[16:31], v[172:175], v[144:147], v[16:31]\n\t"
  "ds_read_b128 v[128:131], v176 offset:0\n\t"
  "ds_read_b64_tr_b16 v[164:165], v178 offset:512\n\t"
  "v_mfma_f32_32x32x16_bf16 v[32:47], v[168:171], v[148:151], v[32:47]\n\t"
  "ds_read_b64_tr_b16 v[166:167], v178 offset:768\n\t"
  "ds_read_b128 v[132:135], v176 offset:16384\n\t"
  "v_mfma_f32_32x32x16_bf16 v[48:63], v[172:175], v[148:151], v[48:63]\n\t"
  "s_waitcnt lgkmcnt(3)\n\t"
  "v_mfma_f32_32x32x16_bf16 v[0:15], v[160:163], v[128:131], v[0:15]\n\t"
  "ds_read_b64_tr_b16 v[168:169], v178 offset:16384\n\t"
  "ds_read_b64_tr_b16 v[170:171], v178 offset:16640\n\t"
  "s_waitcnt lgkmcnt(3)\n\t"
  "v_mfma_f32_32x32x16_bf16 v[16:31], v[164:167], v[128:131], v[16:31]\n\t"
  "ds_read_b128 v[144:147], v176 offset:1024\n\t"
  "ds_read_b64_tr_b16 v[172:173], v178 offset:16896\n\t"
  "s_waitcnt lgkmcnt(4)\n\t"
  "v_mfma_f32_32x32x16_bf16 v[32:47], v[160:163], v[132:135], v[32:47]\n\t"
  "ds_read_b64_tr_b16 v[174:175], v178 offset:17152\n\t"
  "ds_read_b128 v[148:151], v176 offset:17408\n\t"
  "v_mfma_f32_32x32x16_bf16 v[48:63], v[164:167], v[132:135], v[48:63]\n\t"
  "s_waitcnt vmcnt(12) lgkmcnt(0)\n\t"
  "s_barrier\n\t"
  "v_add_u32 v176, 65536, v187\n\t"
  "v_add_u32 v178, 65536, v188\n\t"
  "v_mfma_f32_32x32x16_bf16 v[0:15], v[168:171], v[144:147], v[0:15]\n\t"
  "ds_read_b64_tr_b16 v[160:161], v178 offset:0\n\t"
  "ds_read_b64_tr_b16 v[162:163], v178 offset:256\n\t"
  "v_mfma_f32_32x32x16_bf16 v[16:31], v[172:175], v[144:147], v[16:31]\n\t"
  "ds_read_b128 v[128:131], v176 offset:0\n\t"
  "ds_read_b64_tr_b16 v[164:165], v178 offset:512\n\t"
  "v_mfma_f32_32x32x16_bf16 v[32:47], v[168:171], v[148:151], v[32:47]\n\t"
  "ds_read_b64_tr_b16 v[166:167], v178 offset:768\n\t"
  "ds_read_b128 v[132:135], v176 offset:16384\n\t"
  "v_mfma_f32_32x32x16_bf16 v[48:63], v[172:175], v[148:151], v[48:63]\n\t"
  "s_waitcnt lgkmcnt(3)\n\t"
  "v_mfma_f32_32x32x16_bf16 v[0:15], v[160:163], v[128:131], v[0:15]\n\t"
  "ds_read_b64_tr_b16 v[168:169], v178 offset:16384\n\t"
  "ds_read_b64_tr_b16 v[170:171], v178 offset:16640\n\t"
  "s_waitcnt lgkmcnt(3)\n\t"
  "v_mfma_f32_32x32x16_bf16 v[16:31], v[164:167], v[128:131], v[16:31]\n\t"
  "ds_read_b128 v[144:147], v176 offset:1024\n\t"
  "ds_read_b64_tr_b16 v[172:173], v178 offset:16896\n\t"
  "s_waitcnt lgkmcnt(4)\n\t"
  "v_mfma_f32_32x32x16_bf16 v[32:47], v[160:163], v[132:135], v[32:47]\n\t"
  "ds_read_b64_tr_b16 v[174:175], v178 offset:17152\n\t"
  "ds_read_b128 v[148:151], v176 offset:17408\n\t"
  "v_mfma_f32_32x32x16_bf16 v[48:63], v[164:167], v[132:135], v[48:63]\n\t"
  "s_waitcnt vmcnt(6) lgkmcnt(0)\n\t"
  "s_barrier\n\t"
  "v_add_u32 v176, 98304, v187\n\t"
  "v_add_u32 v178, 98304, v188\n\t"
  "v_mfma_f32_32x32x16_bf16 v[0:15], v[168:171], v[144:147], v[0:15]\n\t"
  "ds_read_b64_tr_b16 v[160:161], v178 offset:0\n\t"
  "ds_read_b64_tr_b16 v[162:163], v178 offset:256\n\t"
  "v_mfma_f32_32x32x16_bf16 v[16:31], v[172:175], v[144:147], v[16:31]\n\t"
  "ds_read_b128 v[128:131], v176 offset:0\n\t"
  "ds_read_b64_tr_b16 v[164:165], v178 offset:512\n\t"
  "v_mfma_f32_32x32x16_bf16 v[32:47], v[168:171], v[148:151], v[32:47]\n\t"
  "ds_read_b64_tr_b16 v[166:167], v178 offset:768\n\t"
  "ds_read_b128 v[132:135], v176 offset:16384\n\t"
  "v_mfma_f32_32x32x16_bf16 v[48:63], v[172:175], v[148:151], v[48:63]\n\t"
  "s_waitcnt lgkmcnt(3)\n\t"
  "v_mfma_f32_32x32x16_bf16 v[0:15], v[160:163], v[128:131], v[0:15]\n\t"
  "ds_read_b64_tr_b16 v[168:169], v178 offset:16384\n\t"
  "ds_read_b64_tr_b16 v[170:171], v178 offset:16640\n\t"
  "s_waitcnt lgkmcnt(3)\n\t"
  "v_mfma_f32_32x32x16_bf16 v[16:31], v[164:167], v[128:131], v[16:31]\n\t"
  "ds_read_b128 v[144:147], v176 offset:1024\n\t"
  "ds_read_b64_tr_b16 v[172:173], v178 offset:16896\n\t"
  "s_waitcnt lgkmcnt(4)\n\t"
  "v_mfma_f32_32x32x16_bf16 v[32:47], v[160:163], v[132:135], v[32:47]\n\t"
  "ds_read_b64_tr_b16 v[174:175], v178 offset:17152\n\t"
  "ds_read_b128 v[148:151], v176 offset:17408\n\t"
  "v_mfma_f32_32x32x16_bf16 v[48:63], v[164:167], v[132:135], v[48:63]\n\t"
  "s_waitcnt vmcnt(0) lgkmcnt(0)\n\t"
  "s_barrier\n\t"
  "v_add_u32 v176, 131072, v187\n\t"
  "v_add_u32 v178, 131072, v188\n\t"
  "v_mfma_f32_32x32x16_bf16 v[0:15], v[168:171], v[144:147], v[0:15]\n\t"
  "ds_read_b64_tr_b16 v[160:161], v178 offset:0\n\t"
  "ds_read_b64_tr_b16 v[162:163], v178 offset:256\n\t"
  "v_mfma_f32_32x32x16_bf16 v[16:31], v[172:175], v[144:147], v[16:31]\n\t"
  "ds_read_b128 v[128:131], v176 offset:0\n\t"
  "ds_read_b64_tr_b16 v[164:165], v178 offset:512\n\t"
  "v_mfma_f32_32x32x16_bf16 v[32:47], v[168:171], v[148:151], v[32:47]\n\t"
  "ds_read_b64_tr_b16 v[166:167], v178 offset:768\n\t"
  "ds_read_b128 v[132:135], v176 offset:16384\n\t"
  "v_mfma_f32_32x32x16_bf16 v[48:63], v[172:175], v[148:151], v[48:63]\n\t"
  "s_cmp_lg_u32 s69, 0\n\t"
  "s_cbranch_scc1 LFkf_%=\n\t"
  "v_mov_b32 v236, s77\n\t"
  "v_mov_b32 v237, 0\n\t"
  "s_mov_b64 exec, 1\n\t"
  "buffer_store_dword v236, v237, s[60:63], s81 offen offset:0 sc1\n\t"
  "s_mov_b64 exec, -1\n\t"
  "LFkf_%=:\n\t"
  "s_waitcnt lgkmcnt(3)\n\t"
  "v_mfma_f32_32x32x16_bf16 v[0:15], v[160:163], v[128:131], v[0:15]\n\t"
  "ds_read_b64_tr_b16 v[168:169], v178 offset:16384\n\t"
  "ds_read_b64_tr_b16 v[170:171], v178 offset:16640\n\t"
  "s_waitcnt lgkmcnt(3)\n\t"
  "v_mfma_f32_32x32x16_bf16 v[16:31], v[164:167], v[128:131], v[16:31]\n\t"
  "ds_read_b128 v[144:147], v176 offset:1024\n\t"
  "ds_read_b64_tr_b16 v[172:173], v178 offset:16896\n\t"
  "s_waitcnt lgkmcnt(4)\n\t"
  "v_mfma_f32_32x32x16_bf16 v[32:47], v[160:163], v[132:135], v[32:47]\n\t"
  "ds_read_b64_tr_b16 v[174:175], v178 offset:17152\n\t"
  "ds_read_b128 v[148:151], v176 offset:17408\n\t"
  "v_mfma_f32_32x32x16_bf16 v[48:63], v[164:167], v[132:135], v[48:63]\n\t"
  "s_waitcnt lgkmcnt(0)\n\t"
  "s_barrier\n\t"
  "v_mfma_f32_32x32x16_bf16 v[0:15], v[168:171], v[144:147], v[0:15]\n\t"
  "v_mfma_f32_32x32x16_bf16 v[16:31], v[172:175], v[144:147], v[16:31]\n\t"
  "v_mfma_f32_32x32x16_bf16 v[32:47], v[168:171], v[148:151], v[32:47]\n\t"
  "v_mfma_f32_32x32x16_bf16 v[48:63], v[172:175], v[148:151], v[48:63]\n\t"
  "s_and_b32 s83, s81, 0xffffff80\n\t"
  "v_lshrrev_b32 v240, 2, v186\n\t"
  "v_and_b32 v240, 28, v240\n\t"
  "buffer_load_dword v241, v240, s[60:63], s83 offen sc1\n\t"
  "s_branch LFq_%=\n\t"
  "LFrolev_%=:\n\t"
  "s_waitcnt vmcnt(2) lgkmcnt(0)\n\t"
  "s_barrier\n\t"
  "s_mov_b32 s68, 0\n\t"
  "v_add_u32 v178, 131072, v181\n\t"
  "v_xad_u32 v176, v180, 0, s68\n\t"
  "v_xor_b32 v177, 32, v176\n\t"
  "ds_read_b64_tr_b16 v[160:161], v178 offset:0\n\t"
  "ds_read_b64_tr_b16 v[162:163], v178 offset:256\n\t"
  "ds_read_b128 v[128:131], v176 offset:0\n\t"
  "ds_read_b64_tr_b16 v[164:165], v178 offset:512\n\t"
  "ds_read_b64_tr_b16 v[166:167], v178 offset:768\n\t"
  "ds_read_b128 v[132:135], v176 offset:4096\n\t"
  "ds_read_b128 v[136:139], v176 offset:8192\n\t"
  "ds_read_b128 v[140:143], v176 offset:12288\n\t"
  "s_waitcnt lgkmcnt(0)\n\t"
  "v_mfma_f32_32x32x16_bf16 v[0:15], v[128:131], v[160:163], 0\n\t"
  "ds_read_b64_tr_b16 v[168:169], v178 offset:16384\n\t"
  "ds_read_b64_tr_b16 v[170:171], v178 offset:16640\n\t"
  "v_mfma_f32_32x32x16_bf16 v[16:31], v[128:131], v[164:167], 0\n\t"
  "ds_read_b128 v[144:147], v177 offset:0\n\t"
  "ds_read_b64_tr_b16 v[172:173], v178 offset:16896\n\t"
  "v_mfma_f32_32x32x16_bf16 v[32:47], v[132:135], v[160:163], 0\n\t"
  "ds_read_b64_tr_b16 v[174:175], v178 offset:17152\n\t"
  "ds_read_b128 v[148:151], v177 offset:4096\n\t"
  "v_mfma_f32_32x32x16_bf16 v[48:63], v[132:135], v[164:167], 0\n\t"
  "ds_read_b128 v[152:155], v177 offset:8192\n\t"
  "ds_read_b128 v[156:159], v177 offset:12288\n\t"
  "v_mfma_f32_32x32x16_bf16 v[64:79], v[136:139], v[160:163], 0\n\t"
  "s_add_u32 m0, s66, 32768\n\t"
  "s_add_u32 s73, s64, 128\n\t"
  "buffer_load_dwordx4 v182, s[40:43], s73 offen lds\n\t"
  "v_mfma_f32_32x32x16_bf16 v[80:95], v[136:139], v[164:167], 0\n\t"
  "buffer_load_dwordx4 v183, s[40:43], s73 offen offset:1024 lds\n\t"
  "v_mfma_f32_32x32x16_bf16 v[96:111], v[140:143], v[160:163], 0\n\t"
  "s_add_u32 m0, s66, 65536\n\t"
  "s_add_u32 s73, s64, 16512\n\t"
  "buffer_load_dwordx4 v182, s[40:43], s73 offen lds\n\t"
  "v_mfma_f32_32x32x16_bf16 v[112:127], v[140:143], v[164:167], 0\n\t"
  "buffer_load_dwordx4 v183, s[40:43], s73 offen offset:1024 lds\n\t"
  "s_waitcnt vmcnt(4) lgkmcnt(0)\n\t"
  "s_barrier\n\t"
  "s_mov_b32 s68, 0\n\t"
  "v_add_u32 v178, 0, v181\n\t"
  "v_xad_u32 v176, v180, 64, s68\n\t"
  "v_xor_b32 v177, 32, v176\n\t"
  "v_mfma_f32_32x32x16_bf16 v[0:15], v[144:147], v[168:171], v[0:15]\n\t"
  "ds_read_b64_tr_b16 v[160:161], v178 offset:0\n\t"
  "ds_read_b64_tr_b16 v[162:163], v178 offset:256\n\t"
  "v_mfma_f32_32x32x16_bf16 v[16:31], v[144:147], v[172:175], v[16:31]\n\t"
  "ds_read_b128 v[128:131], v176 offset:0\n\t"
  "ds_read_b64_tr_b16 v[164:165], v178 offset:512\n\t"
  "v_mfma_f32_32x32x16_bf16 v[32:47], v[148:151], v[168:171], v[32:47]\n\t"
  "ds_read_b64_tr_b16 v[166:167], v178 offset:768\n\t"
  "ds_read_b128 v[132:135], v176 offset:4096\n\t"
  "v_mfma_f32_32x32x16_bf16 v[48:63], v[148:151], v[172:175], v[48:63]\n\t"
  "ds_read_b128 v[136:139], v176 offset:8192\n\t"
  "ds_read_b128 v[140:143], v176 offset:12288\n\t"
  "v_mfma_f32_32x32x16_bf16 v[64:79], v[152:155], v[168:171], v[64:79]\n\t"
  "s_add_u32 m0, s66, 32768\n\t"
  "s_nop 0\n\t"
  "buffer_load_dwordx4 v184, s[44:47], s65 offen offset:2048 lds\n\t"
  "v_mfma_f32_32x32x16_bf16 v[80:95], v[152:155], v[172:175], v[80:95]\n\t"
  "buffer_load_dwordx4 v185, s[44:47], s65 offen offset:3072 lds\n\t"
  "s_add_u32 s65, s65, 262144\n\t"
  "v_mfma_f32_32x32x16_bf16 v[96:111], v[156:159], v[168:171], v[96:111]\n\t"
  "s_add_u32 m0, s66, 65536\n\t"
  "s_nop 0\n\t"
  "buffer_load_dwordx4 v184, s[44:47], s65 offen offset:2048 lds\n\t"
  "v_mfma_f32_32x32x16_bf16 v[112:127], v[156:159], v[172:175], v[112:127]\n\t"
  "buffer_load_dwordx4 v185, s[44:47], s65 offen offset:3072 lds\n\t"
  "s_add_u32 s65, s65, 262144\n\t"
  "s_waitcnt lgkmcnt(0)\n\t"
  "v_mfma_f32_32x32x16_bf16 v[0:15], v[128:131], v[160:163], v[0:15]\n\t"
  "ds_read_b64_tr_b16 v[168:169], v178 offset:16384\n\t"
  "ds_read_b64_tr_b16 v[170:171], v178 offset:16640\n\t"
  "v_mfma_f32_32x32x16_bf16 v[16:31], v[128:131], v[164:167], v[16:31]\n\t"
  "ds_read_b128 v[144:147], v177 offset:0\n\t"
  "ds_read_b64_tr_b16 v[172:173], v178 offset:16896\n\t"
  "v_mfma_f32_32x32x16_bf16 v[32:47], v[132:135], v[160:163], v[32:47]\n\t"
  "ds_read_b64_tr_b16 v[174:175], v178 offset:17152\n\t"
  "ds_read_b128 v[148:151], v177 offset:4096\n\t"
  "v_mfma_f32_32x32x16_bf16 v[48:63], v[132:135], v[164:167], v[48:63]\n\t"
  "ds_read_b128 v[152:155], v177 offset:8192\n\t"
  "ds_read_b128 v[156:159], v177 offset:12288\n\t"
  "v_mfma_f32_32x32x16_bf16 v[64:79], v[136:139], v[160:163], v[64:79]\n\t"
  "s_add_u32 m0, s66, 98304\n\t"
  "s_add_u32 s73, s64, 256\n\t"
  "buffer_load_dwordx4 v182, s[40:43], s73 offen lds\n\t"
  "v_mfma_f32_32x32x16_bf16 v[80:95], v[136:139], v[164:167], v[80:95]\n\t"
  "buffer_load_dwordx4 v183, s[40:43], s73 offen offset:1024 lds\n\t"
  "v_mfma_f32_32x32x16_bf16 v[96:111], v[140:143], v[160:163], v[96:111]\n\t"
  "s_add_u32 m0, s66, 98304\n\t"
  "s_nop 0\n\t"
  "buffer_load_dwordx4 v184, s[44:47], s65 offen offset:2048 lds\n\t"
  "v_mfma_f32_32x32x16_bf16 v[112:127], v[140:143], v[164:167], v[112:127]\n\t"
  "buffer_load_dwordx4 v185, s[44:47], s65 offen offset:3072 lds\n\t"
  "s_add_u32 s65, s65, 262144\n\t"
  "s_waitcnt vmcnt(6) lgkmcnt(0)\n\t"
  "s_barrier\n\t"
  "s_mov_b32 s68, 32768\n\t"
  "v_add_u32 v178, 32768, v181\n\t"
  "v_xad_u32 v176, v179, 0, s68\n\t"
  "v_xor_b32 v177, 32, v176\n\t"
  "v_mfma_f32_32x32x16_bf16 v[0:15], v[144:147], v[168:171], v[0:15]\n\t"
  "ds_read_b64_tr_b16 v[160:161], v178 offset:0\n\t"
  "ds_read_b64_tr_b16 v[162:163], v178 offset:256\n\t"
  "v_mfma_f32_32x32x16_bf16 v[16:31], v[144:147], v[172:175], v[16:31]\n\t"
  "ds_read_b128 v[128:131], v176 offset:0\n\t"
  "ds_read_b64_tr_b16 v[164:165], v178 offset:512\n\t"
  "v_mfma_f32_32x32x16_bf16 v[32:47], v[148:151], v[168:171], v[32:47]\n\t"
  "ds_read_b64_tr_b16 v[166:167], v178 offset:768\n\t"
  "ds_read_b128 v[132:135], v176 offset:4096\n\t"
  "v_mfma_f32_32x32x16_bf16 v[48:63], v[148:151], v[172:175], v[48:63]\n\t"
  "ds_read_b128 v[136:139], v176 offset:8192\n\t"
  "ds_read_b128 v[140:143], v176 offset:12288\n\t"
  "v_mfma_f32_32x32x16_bf16 v[64:79], v[152:155], v[168:171], v[64:79]\n\t"
  "s_add_u32 m0, s66, 131072\n\t"
  "s_add_u32 s73, s64, 16640\n\t"
  "buffer_load_dwordx4 v182, s[40:43], s73 offen lds\n\t"
  "v_mfma_f32_32x32x16_bf16 v[80:95], v[152:155], v[172:175], v[80:95]\n\t"
  "buffer_load_dwordx4 v183, s[40:43], s73 offen offset:1024 lds\n\t"
  "v_mfma_f32_32x32x16_bf16 v[96:111], v[156:159], v[168:171], v[96:111]\n\t"
  "s_add_u32 m0, s66, 131072\n\t"
  "s_nop 0\n\t"
  "buffer_load_dwordx4 v184, s[44:47], s65 offen offset:2048 lds\n\t"
  "v_mfma_f32_32x32x16_bf16 v[112:127], v[156:159], v[172:175], v[112:127]\n\t"
  "buffer_load_dwordx4 v185, s[44:47], s65 offen offset:3072 lds\n\t"
  "s_add_u32 s65, s65, 262144\n\t"
  "s_waitcnt lgkmcnt(0)\n\t"
  "v_mfma_f32_32x32x16_bf16 v[0:15], v[128:131], v[160:163], v[0:15]\n\t"
  "ds_read_b64_tr_b16 v[168:169], v178 offset:16384\n\t"
  "ds_read_b64_tr_b16 v[170:171], v178 offset:16640\n\t"
  "v_mfma_f32_32x32x16_bf16 v[16:31], v[128:131], v[164:167], v[16:31]\n\t"
  "ds_read_b128 v[144:147], v177 offset:0\n\t"
  "ds_read_b64_tr_b16 v[172:173], v178 offset:16896\n\t"
  "v_mfma_f32_32x32x16_bf16 v[32:47], v[132:135], v[160:163], v[32:47]\n\t"
  "ds_read_b64_tr_b16 v[174:175], v178 offset:17152\n\t"
  "ds_read_b128 v[148:151], v177 offset:4096\n\t"
  "s_add_u32 m0, s66, 0\n\t"
  "s_add_u32 s73, s64, 384\n\t"
  "buffer_load_dwordx4 v182, s[40:43], s73 offen lds\n\t"
  "v_mfma_f32_32x32x16_bf16 v[48:63], v[132:135], v[164:167], v[48:63]\n\t"
  "ds_read_b128 v[152:155], v177 offset:8192\n\t"
  "ds_read_b128 v[156:159], v177 offset:12288\n\t"
  "buffer_load_dwordx4 v183, s[40:43], s73 offen offset:1024 lds\n\t"
  "v_mfma_f32_32x32x16_bf16 v[64:79], v[136:139], v[160:163], v[64:79]\n\t"
  "s_add_u32 m0, s66, 0\n\t"
  "s_nop 0\n\t"
  "buffer_load_dwordx4 v184, s[44:47], s65 offen offset:2048 lds\n\t"
  "v_mfma_f32_32x32x16_bf16 v[80:95], v[136:139], v[164:167], v[80:95]\n\t"
  "buffer_load_dwordx4 v185, s[44:47], s65 offen offset:3072 lds\n\t"
  "s_add_u32 s65, s65, 262144\n\t"
  "v_mfma_f32_32x32x16_bf16 v[96:111], v[140:143], v[160:163], v[96:111]\n\t"
  "v_lshlrev_b32 v250, 8, v186\n\t"
  "v_lshl_or_b32 v250, v186, 3, v250\n\t"
  "v_and_b32 v250, 0x3e080, v250\n\t"
  "s_add_u32 s83, s39, 0\n\t"
  "s_mov_b64 exec, 0x3ff\n\t"
  "buffer_load_dword v250, v250, s[56:59], s83 offen\n\t"
  "s_mov_b64 exec, -1\n\t"
  "v_mfma_f32_32x32x16_bf16 v[112:127], v[140:143], v[164:167], v[112:127]\n\t"
  "buffer_load_dwordx4 v[244:247], v190, s[60:63], 0 offen sc1\n\t"
  "s_waitcnt vmcnt(14) lgkmcnt(0)\n\t"
  "s_barrier\n\t"
  "s_mov_b32 s68, 32768\n\t"
  "v_add_u32 v178, 65536, v181\n\t"
  "v_xad_u32 v176, v179, 64, s68\n\t"
  "v_xor_b32 v177, 32, v176\n\t"
  "v_mfma_f32_32x32x16_bf16 v[0:15], v[144:147], v[168:171], v[0:15]\n\t"
  "ds_read_b64_tr_b16 v[160:161], v178 offset:0\n\t"
  "ds_read_b64_tr_b16 v[162:163], v178 offset:256\n\t"
  "v_mfma_f32_32x32x16_bf16 v[16:31], v[144:147], v[172:175], v[16:31]\n\t"
  "ds_read_b128 v[128:131], v176 offset:0\n\t"
  "ds_read_b64_tr_b16 v[164:165], v178 offset:512\n\t"
  "v_mfma_f32_32x32x16_bf16 v[32:47], v[148:151], v[168:171], v[32:47]\n\t"
  "ds_read_b64_tr_b16 v[166:167], v178 offset:768\n\t"
  "ds_read_b128 v[132:135], v176 offset:4096\n\t"
  "v_mfma_f32_32x32x16_bf16 v[48:63], v[148:151], v[172:175], v[48:63]\n\t"
  "ds_read_b128 v[136:139], v176 offset:8192\n\t"
  "ds_read_b128 v[140:143], v176 offset:12288\n\t"
  "v_mfma_f32_32x32x16_bf16 v[64:79], v[152:155], v[168:171], v[64:79]\n\t"
  "v_mfma_f32_32x32x16_bf16 v[80:95], v[152:155], v[172:175], v[80:95]\n\t"
  "v_mfma_f32_32x32x16_bf16 v[96:111], v[156:159], v[168:171], v[96:111]\n\t"
  "s_add_u32 m0, s66, 32768\n\t"
  "s_nop 0\n\t"
  "buffer_load_dwordx4 v184, s[44:47], s65 offen offset:2048 lds\n\t"
  "v_mfma_f32_32x32x16_bf16 v[112:127], v[156:159], v[172:175], v[112:127]\n\t"
  "buffer_load_dwordx4 v185, s[44:47], s65 offen offset:3072 lds\n\t"
  "s_add_u32 s65, s65, 262144\n\t"
  "s_waitcnt lgkmcnt(0)\n\t"
  "v_mfma_f32_32x32x16_bf16 v[0:15], v[128:131], v[160:163], v[0:15]\n\t"
  "ds_read_b64_tr_b16 v[168:169], v178 offset:16384\n\t"
  "ds_read_b64_tr_b16 v[170:171], v178 offset:16640\n\t"
  "v_mfma_f32_32x32x16_bf16 v[16:31], v[128:131], v[164:167], v[16:31]\n\t"
  "ds_read_b128 v[144:147], v177 offset:0\n\t"
  "ds_read_b64_tr_b16 v[172:173], v178 offset:16896\n\t"
  "v_mfma_f32_32x32x16_bf16 v[32:47], v[132:135], v[160:163], v[32:47]\n\t"
  "ds_read_b64_tr_b16 v[174:175], v178 offset:17152\n\t"
  "ds_read_b128 v[148:151], v177 offset:4096\n\t"
  "v_mfma_f32_32x32x16_bf16 v[48:63], v[132:135], v[164:167], v[48:63]\n\t"
  "ds_read_b128 v[152:155], v177 offset:8192\n\t"
  "ds_read_b128 v[156:159], v177 offset:12288\n\t"
  "v_mfma_f32_32x32x16_bf16 v[64:79], v[136:139], v[160:163], v[64:79]\n\t"
  "v_mfma_f32_32x32x16_bf16 v[80:95], v[136:139], v[164:167], v[80:95]\n\t"
  "v_mfma_f32_32x32x16_bf16 v[96:111], v[140:143], v[160:163], v[96:111]\n\t"
  "v_mfma_f32_32x32x16_bf16 v[112:127], v[140:143], v[164:167], v[112:127]\n\t"
  "s_waitcnt vmcnt(10) lgkmcnt(0)\n\t"
  "s_barrier\n\t"
  "s_mov_b32 s68, 98304\n\t"
  "v_add_u32 v178, 98304, v181\n\t"
  "v_xad_u32 v176, v179, 0, s68\n\t"
  "v_xor_b32 v177, 32, v176\n\t"
  "v_mfma_f32_32x32x16_bf16 v[0:15], v[144:147], v[168:171], v[0:15]\n\t"
  "ds_read_b64_tr_b16 v[160:161], v178 offset:0\n\t"
  "ds_read_b64_tr_b16 v[162:163], v178 offset:256\n\t"
  "v_mfma_f32_32x32x16_bf16 v[16:31], v[144:147], v[172:175], v[16:31]\n\t"
  "ds_read_b128 v[128:131], v176 offset:0\n\t"
  "ds_read_b64_tr_b16 v[164:165], v178 offset:512\n\t"
  "v_mfma_f32_32x32x16_bf16 v[32:47], v[148:151], v[168:171], v[32:47]\n\t"
  "ds_read_b64_tr_b16 v[166:167], v178 offset:768\n\t"
  "ds_read_b128 v[132:135], v176 offset:4096\n\t"
  "v_mfma_f32_32x32x16_bf16 v[48:63], v[148:151], v[172:175], v[48:63]\n\t"
  "ds_read_b128 v[136:139], v176 offset:8192\n\t"
  "ds_read_b128 v[140:143], v176 offset:12288\n\t"
  "v_mfma_f32_32x32x16_bf16 v[64:79], v[152:155], v[168:171], v[64:79]\n\t"
  "s_add_u32 m0, s66, 32768\n\t"
  "s_add_u32 s73, s64, 16768\n\t"
  "buffer_load_dwordx4 v182, s[40:43], s73 offen lds\n\t"
  "v_mfma_f32_32x32x16_bf16 v[80:95], v[152:155], v[172:175], v[80:95]\n\t"
  "buffer_load_dwordx4 v183, s[40:43], s73 offen offset:1024 lds\n\t"
  "v_mfma_f32_32x32x16_bf16 v[96:111], v[156:159], v[168:171], v[96:111]\n\t"
  "s_add_u32 m0, s66, 65536\n\t"
  "s_add_u32 s73, s64, 512\n\t"
  "buffer_load_dwordx4 v182, s[40:43], s73 offen lds\n\t"
  "v_mfma_f32_32x32x16_bf16 v[112:127], v[156:159], v[172:175], v[112:127]\n\t"
  "buffer_load_dwordx4 v183, s[40:43], s73 offen offset:1024 lds\n\t"
  "s_waitcnt lgkmcnt(0)\n\t"
  "v_mfma_f32_32x32x16_bf16 v[0:15], v[128:131], v[160:163], v[0:15]\n\t"
  "ds_read_b64_tr_b16 v[168:169], v178 offset:16384\n\t"
  "ds_read_b64_tr_b16 v[170:171], v178 offset:16640\n\t"
  "s_waitcnt vmcnt(6)\n\t"
  "LFgcv_%=:\n\t"
  "v_xor_b32 v232, s77, v245\n\t"
  "v_xor_b32 v233, s77, v247\n\t"
  "v_or_b32 v232, v232, v233\n\t"
  "v_cmp_ne_u32 vcc, 0, v232\n\t"
  "s_cbranch_vccnz LFgsv_%=\n\t"
  "LFgov_%=:\n\t"
  "ds_swizzle_b32 v192, v244 offset:0x18\n\t"
  "ds_swizzle_b32 v194, v246 offset:0x18\n\t"
  "ds_swizzle_b32 v196, v244 offset:0x38\n\t"
  "v_mfma_f32_32x32x16_bf16 v[16:31], v[128:131], v[164:167], v[16:31]\n\t"
  "ds_read_b128 v[144:147], v177 offset:0\n\t"
  "ds_read_b64_tr_b16 v[172:173], v178 offset:16896\n\t"
  "ds_swizzle_b32 v198, v246 offset:0x38\n\t"
  "ds_swizzle_b32 v200, v244 offset:0x58\n\t"
  "ds_swizzle_b32 v202, v246 offset:0x58\n\t"
  "ds_swizzle_b32 v204, v244 offset:0x78\n\t"
  "v_mfma_f32_32x32x16_bf16 v[32:47], v[132:135], v[160:163], v[32:47]\n\t"
  "ds_read_b64_tr_b16 v[174:175], v178 offset:17152\n\t"
  "ds_read_b128 v[148:151], v177 offset:4096\n\t"
  "ds_swizzle_b32 v206, v246 offset:0x78\n\t"
  "ds_swizzle_b32 v208, v244 offset:0x98\n\t"
  "ds_swizzle_b32 v210, v246 offset:0x98\n\t"
  "ds_swizzle_b32 v212, v244 offset:0xb8\n\t"
  "v_mfma_f32_32x32x16_bf16 v[48:63], v[132:135], v[164:167], v[48:63]\n\t"
  "ds_read_b128 v[152:155], v177 offset:8192\n\t"
  "ds_read_b128 v[156:159], v177 offset:12288\n\t"
  "ds_swizzle_b32 v214, v246 offset:0xb8\n\t"
  "ds_swizzle_b32 v216, v244 offset:0xd8\n\t"
  "ds_swizzle_b32 v218, v246 offset:0xd8\n\t"
  "ds_swizzle_b32 v220, v244 offset:0xf8\n\t"
  "v_mfma_f32_32x32x16_bf16 v[64:79], v[136:139], v[160:163], v[64:79]\n\t"
  "ds_swizzle_b32 v222, v246 offset:0xf8\n\t"
  "v_mfma_f32_32x32x16_bf16 v[80:95], v[136:139], v[164:167], v[80:95]\n\t"
  "s_add_u32 m0, s66, 65536\n\t"
  "s_nop 0\n\t"
  "buffer_load_dwordx4 v184, s[44:47], s65 offen offset:2048 lds\n\t"
  "v_mfma_f32_32x32x16_bf16 v[96:111], v[140:143], v[160:163], v[96:111]\n\t"
  "buffer_load_dwordx4 v185, s[44:47], s65 offen offset:3072 lds\n\t"
  "s_add_u32 s65, s65, 262144\n\t"
  "v_mfma_f32_32x32x16_bf16 v[112:127], v[140:143], v[164:167], v[112:127]\n\t"
  "v_lshlrev_b32 v251, 8, v186\n\t"
  "v_lshl_or_b32 v251, v186, 3, v251\n\t"
  "v_and_b32 v251, 0x3e080, v251\n\t"
  "s_add_u32 s83, s39, 1310720\n\t"
  "s_mov_b64 exec, 0x3ff\n\t"
  "buffer_load_dword v251, v251, s[56:59], s83 offen\n\t"
  "s_mov_b64 exec, -1\n\t"
  "s_waitcnt lgkmcnt(0)\n\t"
  "s_barrier\n\t"
  "s_mov_b32 s68, 98304\n\t"
  "v_add_u32 v178, 131072, v181\n\t"
  "v_xad_u32 v176, v179, 64, s68\n\t"
  "v_xor_b32 v177, 32, v176\n\t"
  "v_mfma_f32_32x32x16_bf16 v[0:15], v[144:147], v[168:171], v[0:15]\n\t"
  "ds_read_b64_tr_b16 v[160:161], v178 offset:0\n\t"
  "ds_read_b64_tr_b16 v[162:163], v178 offset:256\n\t"
  "v_lshlrev_b32 v232, 16, v224\n\t"
  "v_and_b32 v233, 0xffff0000, v224\n\t"
  "v_lshlrev_b32 v234, 16, v200\n\t"
  "v_and_b32 v235, 0xffff0000, v200\n\t"
  "v_mfma_f32_32x32x16_bf16 v[16:31], v[144:147], v[172:175], v[16:31]\n\t"
  "ds_read_b128 v[128:131], v176 offset:0\n\t"
  "ds_read_b64_tr_b16 v[164:165], v178 offset:512\n\t"
  "v_lshlrev_b32 v236, 16, v225\n\t"
  "v_and_b32 v237, 0xffff0000, v225\n\t"
  "v_lshlrev_b32 v238, 16, v202\n\t"
  "v_and_b32 v239, 0xffff0000, v202\n\t"
  "v_mfma_f32_32x32x16_bf16 v[32:47], v[148:151], v[168:171], v[32:47]\n\t"
  "ds_read_b64_tr_b16 v[166:167], v178 offset:768\n\t"
  "ds_read_b128 v[132:135], v176 offset:4096\n\t"
  "v_lshlrev_b32 v240, 16, v226\n\t"
  "v_and_b32 v241, 0xffff0000, v226\n\t"
  "v_lshlrev_b32 v242, 16, v204\n\t"
  "v_and_b32 v243, 0xffff0000, v204\n\t"
  "v_mfma_f32_32x32x16_bf16 v[48:63], v[148:151], v[172:175], v[48:63]\n\t"
  "ds_read_b128 v[136:139], v176 offset:8192\n\t"
  "ds_read_b128 v[140:143], v176 offset:12288\n\t"
  "v_lshlrev_b32 v244, 16, v227\n\t"
  "v_and_b32 v245, 0xffff0000, v227\n\t"
  "v_lshlrev_b32 v246, 16, v206\n\t"
  "v_and_b32 v247, 0xffff0000, v206\n\t"
  "v_mfma_f32_32x32x16_bf16 v[64:79], v[152:155], v[168:171], v[64:79]\n\t"
  "v_mul_f32 v232, v232, v234\n\t"
  "v_mul_f32 v233, v233, v235\n\t"
  "v_lshlrev_b32 v234, 16, v192\n\t"
  "v_and_b32 v235, 0xffff0000, v192\n\t"
  "v_mfma_f32_32x32x16_bf16 v[80:95], v[152:155], v[172:175], v[80:95]\n\t"
  "v_cvt_pk_bf16_f32 v232, 0, v232\n\t"
  "v_cvt_pk_bf16_f32 v233, 0, v233\n\t"
  "v_mul_f32 v236, v236, v238\n\t"
  "v_mul_f32 v237, v237, v239\n\t"
  "v_mfma_f32_32x32x16_bf16 v[96:111], v[156:159], v[168:171], v[96:111]\n\t"
  "s_add_u32 m0, s66, 98304\n\t"
  "s_nop 0\n\t"
  "buffer_load_dwordx4 v184, s[44:47], s65 offen offset:2048 lds\n\t"
  "v_lshlrev_b32 v238, 16, v194\n\t"
  "v_and_b32 v239, 0xffff0000, v194\n\t"
  "v_cvt_pk_bf16_f32 v236, 0, v236\n\t"
  "v_cvt_pk_bf16_f32 v237, 0, v237\n\t"
  "v_mfma_f32_32x32x16_bf16 v[112:127], v[156:159], v[172:175], v[112:127]\n\t"
  "buffer_load_dwordx4 v185, s[44:47], s65 offen offset:3072 lds\n\t"
  "s_add_u32 s65, s65, 262144\n\t"
  "v_mul_f32 v240, v240, v242\n\t"
  "v_mul_f32 v241, v241, v243\n\t"
  "v_lshlrev_b32 v242, 16, v196\n\t"
  "v_and_b32 v243, 0xffff0000, v196\n\t"
  "s_waitcnt lgkmcnt(0)\n\t"
  "v_mfma_f32_32x32x16_bf16 v[0:15], v[128:131], v[160:163], v[0:15]\n\t"
  "ds_read_b64_tr_b16 v[168:169], v178 offset:16384\n\t"
  "ds_read_b64_tr_b16 v[170:171], v178 offset:16640\n\t"
  "v_cvt_pk_bf16_f32 v240, 0, v240\n\t"
  "v_cvt_pk_bf16_f32 v241, 0, v241\n\t"
  "v_mul_f32 v244, v244, v246\n\t"
  "v_mul_f32 v245, v245, v247\n\t"
  "v_mfma_f32_32x32x16_bf16 v[16:31], v[128:131], v[164:167], v[16:31]\n\t"
  "ds_read_b128 v[144:147], v177 offset:0\n\t"
  "ds_read_b64_tr_b16 v[172:173], v178 offset:16896\n\t"
  "v_lshlrev_b32 v246, 16, v198\n\t"
  "v_and_b32 v247, 0xffff0000, v198\n\t"
  "v_cvt_pk_bf16_f32 v244, 0, v244\n\t"
  "v_cvt_pk_bf16_f32 v245, 0, v245\n\t"
  "v_mfma_f32_32x32x16_bf16 v[32:47], v[132:135], v[160:163], v[32:47]\n\t"
  "ds_read_b64_tr_b16 v[174:175], v178 offset:17152\n\t"
  "ds_read_b128 v[148:151], v177 offset:4096\n\t"
  "v_add_f32 v232, v232, v234\n\t"
  "v_add_f32 v233, v233, v235\n\t"
  "v_add_f32 v236, v236, v238\n\t"
  "v_add_f32 v237, v237, v239\n\t"
  "v_mfma_f32_32x32x16_bf16 v[48:63], v[132:135], v[164:167], v[48:63]\n\t"
  "ds_read_b128 v[152:155], v177 offset:8192\n\t"
  "ds_read_b128 v[156:159], v177 offset:12288\n\t"
  "v_add_f32 v240, v240, v242\n\t"
  "v_add_f32 v241, v241, v243\n\t"
  "v_add_f32 v244, v244, v246\n\t"
  "v_add_f32 v245, v245, v247\n\t"
  "v_mfma_f32_32x32x16_bf16 v[64:79], v[136:139], v[160:163], v[64:79]\n\t"
  "v_cvt_pk_bf16_f32 v224, v232, v233\n\t"
  "v_cvt_pk_bf16_f32 v225, v236, v237\n\t"
  "v_cvt_pk_bf16_f32 v226, v240, v241\n\t"
  "v_cvt_pk_bf16_f32 v227, v244, v245\n\t"
  "v_mfma_f32_32x32x16_bf16 v[80:95], v[136:139], v[164:167], v[80:95]\n\t"
  "buffer_store_dwordx4 v[224:227], v191, s[52:55], 0 offen sc1\n\t"
  "v_lshlrev_b32 v232, 16, v228\n\t"
  "v_and_b32 v233, 0xffff0000, v228\n\t"
  "v_lshlrev_b32 v234, 16, v216\n\t"
  "v_mfma_f32_32x32x16_bf16 v[96:111], v[140:143], v[160:163], v[96:111]\n\t"
  "v_and_b32 v235, 0xffff0000, v216\n\t"
  "v_lshlrev_b32 v236, 16, v229\n\t"
  "v_and_b32 v237, 0xffff0000, v229\n\t"
  "v_lshlrev_b32 v238, 16, v218\n\t"
  "v_mfma_f32_32x32x16_bf16 v[112:127], v[140:143], v[164:167], v[112:127]\n\t"
  "v_and_b32 v239, 0xffff0000, v218\n\t"
  "v_lshlrev_b32 v240, 16, v230\n\t"
  "v_and_b32 v241, 0xffff0000, v230\n\t"
  "v_lshlrev_b32 v242, 16, v220\n\t"
  "s_waitcnt vmcnt(8) lgkmcnt(0)\n\t"
  "s_barrier\n\t"
  "s_mov_b32 s68, 0\n\t"
  "v_add_u32 v178, 0, v181\n\t"
  "v_xad_u32 v176, v179, 0, s68\n\t"
  "v_xor_b32 v177, 32, v176\n\t"
  "v_mfma_f32_32x32x16_bf16 v[0:15], v[144:147], v[168:171], v[0:15]\n\t"
  "ds_read_b64_tr_b16 v[160:161], v178 offset:0\n\t"
  "ds_read_b64_tr_b16 v[162:163], v178 offset:256\n\t"
  "v_and_b32 v243, 0xffff0000, v220\n\t"
  "v_lshlrev_b32 v244, 16, v231\n\t"
  "v_and_b32 v245, 0xffff0000, v231\n\t"
  "v_lshlrev_b32 v246, 16, v222\n\t"
  "v_mfma_f32_32x32x16_bf16 v[16:31], v[144:147], v[172:175], v[16:31]\n\t"
  "ds_read_b128 v[128:131], v176 offset:0\n\t"
  "ds_read_b64_tr_b16 v[164:165], v178 offset:512\n\t"
  "v_and_b32 v247, 0xffff0000, v222\n\t"
  "v_mul_f32 v232, v232, v234\n\t"
  "v_mul_f32 v233, v233, v235\n\t"
  "v_lshlrev_b32 v234, 16, v208\n\t"
  "v_mfma_f32_32x32x16_bf16 v[32:47], v[148:151], v[168:171], v[32:47]\n\t"
  "ds_read_b64_tr_b16 v[166:167], v178 offset:768\n\t"
  "ds_read_b128 v[132:135], v176 offset:4096\n\t"
  "v_and_b32 v235, 0xffff0000, v208\n\t"
  "v_cvt_pk_bf16_f32 v232, 0, v232\n\t"
  "v_cvt_pk_bf16_f32 v233, 0, v233\n\t"
  "v_mul_f32 v236, v236, v238\n\t"
  "v_mfma_f32_32x32x16_bf16 v[48:63], v[148:151], v[172:175], v[48:63]\n\t"
  "ds_read_b128 v[136:139], v176 offset:8192\n\t"
  "ds_read_b128 v[140:143], v176 offset:12288\n\t"
  "v_mul_f32 v237, v237, v239\n\t"
  "v_lshlrev_b32 v238, 16, v210\n\t"
  "v_and_b32 v239, 0xffff0000, v210\n\t"
  "v_cvt_pk_bf16_f32 v236, 0, v236\n\t"
  "v_mfma_f32_32x32x16_bf16 v[64:79], v[152:155], v[168:171], v[64:79]\n\t"
  "s_add_u32 m0, s66, 98304\n\t"
  "s_add_u32 s73, s64, 16896\n\t"
  "buffer_load_dwordx4 v182, s[40:43], s73 offen lds\n\t"
  "v_cvt_pk_bf16_f32 v237, 0, v237\n\t"
  "v_mul_f32 v240, v240, v242\n\t"
  "v_mul_f32 v241, v241, v243\n\t"
  "v_lshlrev_b32 v242, 16, v212\n\t"
  "v_mfma_f32_32x32x16_bf16 v[80:95], v[152:155], v[172:175], v[80:95]\n\t"
  "buffer_load_dwordx4 v183, s[40:43], s73 offen offset:1024 lds\n\t"
  "v_and_b32 v243, 0xffff0000, v212\n\t"
  "v_cvt_pk_bf16_f32 v240, 0, v240\n\t"
  "v_cvt_pk_bf16_f32 v241, 0, v241\n\t"
  "v_mul_f32 v244, v244, v246\n\t"
  "v_mfma_f32_32x32x16_bf16 v[96:111], v[156:159], v[168:171], v[96:111]\n\t"
  "s_add_u32 m0, s66, 131072\n\t"
  "s_add_u32 s73, s64, 640\n\t"
  "buffer_load_dwordx4 v182, s[40:43], s73 offen lds\n\t"
  "v_mul_f32 v245, v245, v247\n\t"
  "v_lshlrev_b32 v246, 16, v214\n\t"
  "v_and_b32 v247, 0xffff0000, v214\n\t"
  "v_cvt_pk_bf16_f32 v244, 0, v244\n\t"
  "v_mfma_f32_32x32x16_bf16 v[112:127], v[156:159], v[172:175], v[112:127]\n\t"
  "buffer_load_dwordx4 v183, s[40:43], s73 offen offset:1024 lds\n\t"
  "v_cvt_pk_bf16_f32 v245, 0, v245\n\t"
  "v_add_f32 v232, v232, v234\n\t"
  "v_add_f32 v233, v233, v235\n\t"
  "v_add_f32 v236, v236, v238\n\t"
  "s_waitcnt lgkmcnt(0)\n\t"
  "v_mfma_f32_32x32x16_bf16 v[0:15], v[128:131], v[160:163], v[0:15]\n\t"
  "ds_read_b64_tr_b16 v[168:169], v178 offset:16384\n\t"
  "ds_read_b64_tr_b16 v[170:171], v178 offset:16640\n\t"
  "v_add_f32 v237, v237, v239\n\t"
  "v_add_f32 v240, v240, v242\n\t"
  "v_add_f32 v241, v241, v243\n\t"
  "v_add_f32 v244, v244, v246\n\t"
  "v_mfma_f32_32x32x16_bf16 v[16:31], v[128:131], v[164:167], v[16:31]\n\t"
  "ds_read_b128 v[144:147], v177 offset:0\n\t"
  "ds_read_b64_tr_b16 v[172:173], v178 offset:16896\n\t"
  "v_add_f32 v245, v245, v247\n\t"
  "v_cvt_pk_bf16_f32 v228, v232, v233\n\t"
  "v_cvt_pk_bf16_f32 v229, v236, v237\n\t"
  "v_cvt_pk_bf16_f32 v230, v240, v241\n\t"
  "v_mfma_f32_32x32x16_bf16 v[32:47], v[132:135], v[160:163], v[32:47]\n\t"
  "ds_read_b64_tr_b16 v[174:175], v178 offset:17152\n\t"
  "ds_read_b128 v[148:151], v177 offset:4096\n\t"
  "v_cvt_pk_bf16_f32 v231, v244, v245\n\t"
  "buffer_store_dwordx4 v[228:231], v249, s[52:55], 0 offen sc1\n\t"
  "v_mfma_f32_32x32x16_bf16 v[48:63], v[132:135], v[164:167], v[48:63]\n\t"
  "ds_read_b128 v[152:155], v177 offset:8192\n\t"
  "ds_read_b128 v[156:159], v177 offset:12288\n\t"
  "v_mfma_f32_32x32x16_bf16 v[64:79], v[136:139], v[160:163], v[64:79]\n\t"
  "v_mfma_f32_32x32x16_bf16 v[80:95], v[136:139], v[164:167], v[80:95]\n\t"
  "s_add_u32 m0, s66, 131072\n\t"
  "s_nop 0\n\t"
  "buffer_load_dwordx4 v184, s[44:47], s65 offen offset:2048 lds\n\t"
  "v_mfma_f32_32x32x16_bf16 v[96:111], v[140:143], v[160:163], v[96:111]\n\t"
  "buffer_load_dwordx4 v185, s[44:47], s65 offen offset:3072 lds\n\t"
  "s_add_u32 s65, s65, 262144\n\t"
  "v_mfma_f32_32x32x16_bf16 v[112:127], v[140:143], v[164:167], v[112:127]\n\t"
  "v_lshlrev_b32 v250, 8, v186\n\t"
  "v_lshl_or_b32 v250, v186, 3, v250\n\t"
  "v_and_b32 v250, 0x3e080, v250\n\t"
  "s_add_u32 s83, s39, 2621440\n\t"
  "s_mov_b64 exec, 0x3ff\n\t"
  "buffer_load_dword v250, v250, s[56:59], s83 offen\n\t"
  "s_mov_b64 exec, -1\n\t"
  "s_waitcnt lgkmcnt(0)\n\t"
  "s_barrier\n\t"
  "s_mov_b32 s68, 0\n\t"
  "v_add_u32 v178, 32768, v181\n\t"
  "v_xad_u32 v176, v179, 64, s68\n\t"
  "v_xor_b32 v177, 32, v176\n\t"
  "v_mfma_f32_32x32x16_bf16 v[0:15], v[144:147], v[168:171], v[0:15]\n\t"
  "ds_read_b64_tr_b16 v[160:161], v178 offset:0\n\t"
  "ds_read_b64_tr_b16 v[162:163], v178 offset:256\n\t"
  "v_mfma_f32_32x32x16_bf16 v[16:31], v[144:147], v[172:175], v[16:31]\n\t"
  "ds_read_b128 v[128:131], v176 offset:0\n\t"
  "ds_read_b64_tr_b16 v[164:165], v178 offset:512\n\t"
  "v_mfma_f32_32x32x16_bf16 v[32:47], v[148:151], v[168:171], v[32:47]\n\t"
  "ds_read_b64_tr_b16 v[166:167], v178 offset:768\n\t"
  "ds_read_b128 v[132:135], v176 offset:4096\n\t"
  "v_mfma_f32_32x32x16_bf16 v[48:63], v[148:151], v[172:175], v[48:63]\n\t"
  "ds_read_b128 v[136:139], v176 offset:8192\n\t"
  "ds_read_b128 v[140:143], v176 offset:12288\n\t"
  "v_mfma_f32_32x32x16_bf16 v[64:79], v[152:155], v[168:171], v[64:79]\n\t"
  "v_mfma_f32_32x32x16_bf16 v[80:95], v[152:155], v[172:175], v[80:95]\n\t"
  "v_mfma_f32_32x32x16_bf16 v[96:111], v[156:159], v[168:171], v[96:111]\n\t"
  "s_add_u32 m0, s66, 0\n\t"
  "s_nop 0\n\t"
  "buffer_load_dwordx4 v184, s[44:47], s65 offen offset:2048 lds\n\t"
  "v_mfma_f32_32x32x16_bf16 v[112:127], v[156:159], v[172:175], v[112:127]\n\t"
  "buffer_load_dwordx4 v185, s[44:47], s65 offen offset:3072 lds\n\t"
  "s_add_u32 s65, s65, 262144\n\t"
  "s_waitcnt lgkmcnt(0)\n\t"
  "v_mfma_f32_32x32x16_bf16 v[0:15], v[128:131], v[160:163], v[0:15]\n\t"
  "ds_read_b64_tr_b16 v[168:169], v178 offset:16384\n\t"
  "ds_read_b64_tr_b16 v[170:171], v178 offset:16640\n\t"
  "v_mfma_f32_32x32x16_bf16 v[16:31], v[128:131], v[164:167], v[16:31]\n\t"
  "ds_read_b128 v[144:147], v177 offset:0\n\t"
  "ds_read_b64_tr_b16 v[172:173], v178 offset:16896\n\t"
  "v_mfma_f32_32x32x16_bf16 v[32:47], v[132:135], v[160:163], v[32:47]\n\t"
  "ds_read_b64_tr_b16 v[174:175], v178 offset:17152\n\t"
  "ds_read_b128 v[148:151], v177 offset:4096\n\t"
  "v_mfma_f32_32x32x16_bf16 v[48:63], v[132:135], v[164:167], v[48:63]\n\t"
  "ds_read_b128 v[152:155], v177 offset:8192\n\t"
  "ds_read_b128 v[156:159], v177 offset:12288\n\t"
  "v_mfma_f32_32x32x16_bf16 v[64:79], v[136:139], v[160:163], v[64:79]\n\t"
  "v_mfma_f32_32x32x16_bf16 v[80:95], v[136:139], v[164:167], v[80:95]\n\t"
  "v_mfma_f32_32x32x16_bf16 v[96:111], v[140:143], v[160:163], v[96:111]\n\t"
  "v_mfma_f32_32x32x16_bf16 v[112:127], v[140:143], v[164:167], v[112:127]\n\t"
  "s_waitcnt vmcnt(5)\n\t"
  "s_waitcnt lgkmcnt(0)\n\t"
  "s_barrier\n\t"
  "s_mov_b32 s68, 65536\n\t"
  "v_add_u32 v178, 65536, v181\n\t"
  "v_xad_u32 v176, v179, 0, s68\n\t"
  "v_xor_b32 v177, 32, v176\n\t"
  "v_mfma_f32_32x32x16_bf16 v[0:15], v[144:147], v[168:171], v[0:15]\n\t"
  "ds_read_b64_tr_b16 v[160:161], v178 offset:0\n\t"
  "ds_read_b64_tr_b16 v[162:163], v178 offset:256\n\t"
  "v_mfma_f32_32x32x16_bf16 v[16:31], v[144:147], v[172:175], v[16:31]\n\t"
  "ds_read_b128 v[128:131], v176 offset:0\n\t"
  "ds_read_b64_tr_b16 v[164:165], v178 offset:512\n\t"
  "v_mfma_f32_32x32x16_bf16 v[32:47], v[148:151], v[168:171], v[32:47]\n\t"
  "ds_read_b64_tr_b16 v[166:167], v178 offset:768\n\t"
  "ds_read_b128 v[132:135], v176 offset:4096\n\t"
  "v_mfma_f32_32x32x16_bf16 v[48:63], v[148:151], v[172:175], v[48:63]\n\t"
  "ds_read_b128 v[136:139], v176 offset:8192\n\t"
  "ds_read_b128 v[140:143], v176 offset:12288\n\t"
  "v_mfma_f32_32x32x16_bf16 v[64:79], v[152:155], v[168:171], v[64:79]\n\t"
  "s_add_u32 m0, s66, 0\n\t"
  "s_add_u32 s73, s64, 17024\n\t"
  "buffer_load_dwordx4 v182, s[40:43], s73 offen lds\n\t"
  "v_mfma_f32_32x32x16_bf16 v[80:95], v[152:155], v[172:175], v[80:95]\n\t"
  "buffer_load_dwordx4 v183, s[40:43], s73 offen offset:1024 lds\n\t"
  "v_mfma_f32_32x32x16_bf16 v[96:111], v[156:159], v[168:171], v[96:111]\n\t"
  "s_add_u32 m0, s66, 32768\n\t"
  "s_add_u32 s73, s64, 768\n\t"
  "buffer_load_dwordx4 v182, s[40:43], s73 offen lds\n\t"
  "v_mfma_f32_32x32x16_bf16 v[112:127], v[156:159], v[172:175], v[112:127]\n\t"
  "buffer_load_dwordx4 v183, s[40:43], s73 offen offset:1024 lds\n\t"
  "s_waitcnt lgkmcnt(0)\n\t"
  "v_mfma_f32_32x32x16_bf16 v[0:15], v[128:131], v[160:163], v[0:15]\n\t"
  "ds_read_b64_tr_b16 v[168:169], v178 offset:16384\n\t"
  "ds_read_b64_tr_b16 v[170:171], v178 offset:16640\n\t"
  "v_mfma_f32_32x32x16_bf16 v[16:31], v[128:131], v[164:167], v[16:31]\n\t"
  "ds_read_b128 v[144:147], v177 offset:0\n\t"
  "ds_read_b64_tr_b16 v[172:173], v178 offset:16896\n\t"
  "v_mfma_f32_32x32x16_bf16 v[32:47], v[132:135], v[160:163], v[32:47]\n\t"
  "ds_read_b64_tr_b16 v[174:175], v178 offset:17152\n\t"
  "ds_read_b128 v[148:151], v177 offset:4096\n\t"
  "v_mfma_f32_32x32x16_bf16 v[48:63], v[132:135], v[164:167], v[48:63]\n\t"
  "ds_read_b128 v[152:155], v177 offset:8192\n\t"
  "ds_read_b128 v[156:159], v177 offset:12288\n\t"
  "v_mfma_f32_32x32x16_bf16 v[64:79], v[136:139], v[160:163], v[64:79]\n\t"
  "v_mfma_f32_32x32x16_bf16 v[80:95], v[136:139], v[164:167], v[80:95]\n\t"
  "s_add_u32 m0, s66, 32768\n\t"
  "s_nop 0\n\t"
  "buffer_load_dwordx4 v184, s[44:47], s65 offen offset:2048 lds\n\t"
  "v_mfma_f32_32x32x16_bf16 v[96:111], v[140:143], v[160:163], v[96:111]\n\t"
  "buffer_load_dwordx4 v185, s[44:47], s65 offen offset:3072 lds\n\t"
  "s_add_u32 s65, s65, 262144\n\t"
  "v_mfma_f32_32x32x16_bf16 v[112:127], v[140:143], v[164:167], v[112:127]\n\t"
  "v_lshlrev_b32 v251, 8, v186\n\t"
  "v_lshl_or_b32 v251, v186, 3, v251\n\t"
  "v_and_b32 v251, 0x3e080, v251\n\t"
  "s_add_u32 s83, s39, 3932160\n\t"
  "s_mov_b64 exec, 0x3ff\n\t"
  "buffer_load_dword v251, v251, s[56:59], s83 offen\n\t"
  "s_mov_b64 exec, -1\n\t"
  "s_waitcnt lgkmcnt(0)\n\t"
  "s_barrier\n\t"
  "s_mov_b32 s68, 65536\n\t"
  "v_add_u32 v178, 98304, v181\n\t"
  "v_xad_u32 v176, v179, 64, s68\n\t"
  "v_xor_b32 v177, 32, v176\n\t"
  "v_mfma_f32_32x32x16_bf16 v[0:15], v[144:147], v[168:171], v[0:15]\n\t"
  "ds_read_b64_tr_b16 v[160:161], v178 offset:0\n\t"
  "ds_read_b64_tr_b16 v[162:163], v178 offset:256\n\t"
  "v_mfma_f32_32x32x16_bf16 v[16:31], v[144:147], v[172:175], v[16:31]\n\t"
  "ds_read_b128 v[128:131], v176 offset:0\n\t"
  "ds_read_b64_tr_b16 v[164:165], v178 offset:512\n\t"
  "v_mfma_f32_32x32x16_bf16 v[32:47], v[148:151], v[168:171], v[32:47]\n\t"
  "ds_read_b64_tr_b16 v[166:167], v178 offset:768\n\t"
  "ds_read_b128 v[132:135], v176 offset:4096\n\t"
  "v_mfma_f32_32x32x16_bf16 v[48:63], v[148:151], v[172:175], v[48:63]\n\t"
  "ds_read_b128 v[136:139], v176 offset:8192\n\t"
  "ds_read_b128 v[140:143], v176 offset:12288\n\t"
  "v_mfma_f32_32x32x16_bf16 v[64:79], v[152:155], v[168:171], v[64:79]\n\t"
  "v_mfma_f32_32x32x16_bf16 v[80:95], v[152:155], v[172:175], v[80:95]\n\t"
  "v_mfma_f32_32x32x16_bf16 v[96:111], v[156:159], v[168:171], v[96:111]\n\t"
  "s_add_u32 m0, s66, 65536\n\t"
  "s_nop 0\n\t"
  "buffer_load_dwordx4 v184, s[44:47], s65 offen offset:2048 lds\n\t"
  "v_mfma_f32_32x32x16_bf16 v[112:127], v[156:159], v[172:175], v[112:127]\n\t"
  "buffer_load_dwordx4 v185, s[44:47], s65 offen offset:3072 lds\n\t"
  "s_add_u32 s65, s65, 262144\n\t"
  "s_waitcnt lgkmcnt(0)\n\t"
  "v_mfma_f32_32x32x16_bf16 v[0:15], v[128:131], v[160:163], v[0:15]\n\t"
  "ds_read_b64_tr_b16 v[168:169], v178 offset:16384\n\t"
  "ds_read_b64_tr_b16 v[170:171], v178 offset:16640\n\t"
  "v_mfma_f32_32x32x16_bf16 v[16:31], v[128:131], v[164:167], v[16:31]\n\t"
  "ds_read_b128 v[144:147], v177 offset:0\n\t"
  "ds_read_b64_tr_b16 v[172:173], v178 offset:16896\n\t"
  "v_mfma_f32_32x32x16_bf16 v[32:47], v[132:135], v[160:163], v[32:47]\n\t"
  "ds_read_b64_tr_b16 v[174:175], v178 offset:17152\n\t"
  "ds_read_b128 v[148:151], v177 offset:4096\n\t"
  "v_mfma_f32_32x32x16_bf16 v[48:63], v[132:135], v[164:167], v[48:63]\n\t"
  "ds_read_b128 v[152:155], v177 offset:8192\n\t"
  "ds_read_b128 v[156:159], v177 offset:12288\n\t"
  "v_mfma_f32_32x32x16_bf16 v[64:79], v[136:139], v[160:163], v[64:79]\n\t"
  "v_mfma_f32_32x32x16_bf16 v[80:95], v[136:139], v[164:167], v[80:95]\n\t"
  "v_mfma_f32_32x32x16_bf16 v[96:111], v[140:143], v[160:163], v[96:111]\n\t"
  "v_mfma_f32_32x32x16_bf16 v[112:127], v[140:143], v[164:167], v[112:127]\n\t"
  "s_and_b32 s83, s80, 0xffffff80\n\t"
  "v_lshrrev_b32 v240, 2, v186\n\t"
  "v_and_b32 v240, 124, v240\n\t"
  "buffer_load_dword v192, v240, s[60:63], s83 offen sc1\n\t"
  "s_waitcnt vmcnt(8) lgkmcnt(0)\n\t"
  "s_barrier\n\t"
  "s_mov_b32 s68, 0\n\t"
  "v_add_u32 v178, 131072, v181\n\t"
  "v_xad_u32 v176, v180, 0, s68\n\t"
  "v_xor_b32 v177, 32, v176\n\t"
  "v_mfma_f32_32x32x16_bf16 v[0:15], v[144:147], v[168:171], v[0:15]\n\t"
  "ds_read_b64_tr_b16 v[160:161], v178 offset:0\n\t"
  "ds_read_b64_tr_b16 v[162:163], v178 offset:256\n\t"
  "v_mfma_f32_32x32x16_bf16 v[16:31], v[144:147], v[172:175], v[16:31]\n\t"
  "ds_read_b128 v[128:131], v176 offset:0\n\t"
  "ds_read_b64_tr_b16 v[164:165], v178 offset:512\n\t"
  "v_mfma_f32_32x32x16_bf16 v[32:47], v[148:151], v[168:171], v[32:47]\n\t"
  "ds_read_b64_tr_b16 v[166:167], v178 offset:768\n\t"
  "ds_read_b128 v[132:135], v176 offset:4096\n\t"
  "v_mfma_f32_32x32x16_bf16 v[48:63], v[148:151], v[172:175], v[48:63]\n\t"
  "ds_read_b128 v[136:139], v176 offset:8192\n\t"
  "ds_read_b128 v[140:143], v176 offset:12288\n\t"
  "v_mfma_f32_32x32x16_bf16 v[64:79], v[152:155], v[168:171], v[64:79]\n\t"
  "s_add_u32 m0, s66, 65536\n\t"
  "s_add_u32 s73, s64, 17152\n\t"
  "buffer_load_dwordx4 v182, s[40:43], s73 offen lds\n\t"
  "v_mfma_f32_32x32x16_bf16 v[80:95], v[152:155], v[172:175], v[80:95]\n\t"
  "buffer_load_dwordx4 v183, s[40:43], s73 offen offset:1024 lds\n\t"
  "v_mfma_f32_32x32x16_bf16 v[96:111], v[156:159], v[168:171], v[96:111]\n\t"
  "s_add_u32 m0, s66, 98304\n\t"
  "s_add_u32 s73, s64, 896\n\t"
  "buffer_load_dwordx4 v182, s[40:43], s73 offen lds\n\t"
  "v_mfma_f32_32x32x16_bf16 v[112:127], v[156:159], v[172:175], v[112:127]\n\t"
  "buffer_load_dwordx4 v183, s[40:43], s73 offen offset:1024 lds\n\t"
  "s_waitcnt lgkmcnt(0)\n\t"
  "v_mfma_f32_32x32x16_bf16 v[0:15], v[128:131], v[160:163], v[0:15]\n\t"
  "ds_read_b64_tr_b16 v[168:169], v178 offset:16384\n\t"
  "ds_read_b64_tr_b16 v[170:171], v178 offset:16640\n\t"
  "v_mfma_f32_32x32x16_bf16 v[16:31], v[128:131], v[164:167], v[16:31]\n\t"
  "ds_read_b128 v[144:147], v177 offset:0\n\t"
  "ds_read_b64_tr_b16 v[172:173], v178 offset:16896\n\t"
  "v_mfma_f32_32x32x16_bf16 v[32:47], v[132:135], v[160:163], v[32:47]\n\t"
  "ds_read_b64_tr_b16 v[174:175], v178 offset:17152\n\t"
  "ds_read_b128 v[148:151], v177 offset:4096\n\t"
  "v_mfma_f32_32x32x16_bf16 v[48:63], v[132:135], v[164:167], v[48:63]\n\t"
  "ds_read_b128 v[152:155], v177 offset:8192\n\t"
  "ds_read_b128 v[156:159], v177 offset:12288\n\t"
  "v_mfma_f32_32x32x16_bf16 v[64:79], v[136:139], v[160:163], v[64:79]\n\t"
  "v_mfma_f32_32x32x16_bf16 v[80:95], v[136:139], v[164:167], v[80:95]\n\t"
  "v_mfma_f32_32x32x16_bf16 v[96:111], v[140:143], v[160:163], v[96:111]\n\t"
  "s_add_u32 m0, s66, 98304\n\t"
  "s_nop 0\n\t"
  "buffer_load_dwordx4 v184, s[44:47], s65 offen offset:2048 lds\n\t"
  "v_mfma_f32_32x32x16_bf16 v[112:127], v[140:143], v[164:167], v[112:127]\n\t"
  "buffer_load_dwordx4 v185, s[44:47], s65 offen offset:3072 lds\n\t"
  "s_add_u32 s65, s65, 262144\n\t"
  "s_waitcnt lgkmcnt(0)\n\t"
  "s_barrier\n\t"
  "s_mov_b32 s68, 0\n\t"
  "v_add_u32 v178, 0, v181\n\t"
  "v_xad_u32 v176, v180, 64, s68\n\t"
  "v_xor_b32 v177, 32, v176\n\t"
  "v_mfma_f32_32x32x16_bf16 v[0:15], v[144:147], v[168:171], v[0:15]\n\t"
  "ds_read_b64_tr_b16 v[160:161], v178 offset:0\n\t"
  "ds_read_b64_tr_b16 v[162:163], v178 offset:256\n\t"
  "v_mfma_f32_32x32x16_bf16 v[16:31], v[144:147], v[172:175], v[16:31]\n\t"
  "ds_read_b128 v[128:131], v176 offset:0\n\t"
  "ds_read_b64_tr_b16 v[164:165], v178 offset:512\n\t"
  "v_mfma_f32_32x32x16_bf16 v[32:47], v[148:151], v[168:171], v[32:47]\n\t"
  "ds_read_b64_tr_b16 v[166:167], v178 offset:768\n\t"
  "ds_read_b128 v[132:135], v176 offset:4096\n\t"
  "v_mfma_f32_32x32x16_bf16 v[48:63], v[148:151], v[172:175], v[48:63]\n\t"
  "ds_read_b128 v[136:139], v176 offset:8192\n\t"
  "ds_read_b128 v[140:143], v176 offset:12288\n\t"
  "v_mfma_f32_32x32x16_bf16 v[64:79], v[152:155], v[168:171], v[64:79]\n\t"
  "v_mfma_f32_32x32x16_bf16 v[80:95], v[152:155], v[172:175], v[80:95]\n\t"
  "v_mfma_f32_32x32x16_bf16 v[96:111], v[156:159], v[168:171], v[96:111]\n\t"
  "s_add_u32 m0, s66, 131072\n\t"
  "s_nop 0\n\t"
  "buffer_load_dwordx4 v184, s[44:47], s65 offen offset:2048 lds\n\t"
  "v_mfma_f32_32x32x16_bf16 v[112:127], v[156:159], v[172:175], v[112:127]\n\t"
  "buffer_load_dwordx4 v185, s[44:47], s65 offen offset:3072 lds\n\t"
  "s_add_u32 s65, s65, 262144\n\t"
  "s_waitcnt lgkmcnt(0)\n\t"
  "v_mfma_f32_32x32x16_bf16 v[0:15], v[128:131], v[160:163], v[0:15]\n\t"
  "ds_read_b64_tr_b16 v[168:169], v178 offset:16384\n\t"
  "ds_read_b64_tr_b16 v[170:171], v178 offset:16640\n\t"
  "v_mfma_f32_32x32x16_bf16 v[16:31], v[128:131], v[164:167], v[16:31]\n\t"
  "ds_read_b128 v[144:147], v177 offset:0\n\t"
  "ds_read_b64_tr_b16 v[172:173], v178 offset:16896\n\t"
  "v_mfma_f32_32x32x16_bf16 v[32:47], v[132:135], v[160:163], v[32:47]\n\t"
  "ds_read_b64_tr_b16 v[174:175], v178 offset:17152\n\t"
  "ds_read_b128 v[148:151], v177 offset:4096\n\t"
  "v_mfma_f32_32x32x16_bf16 v[48:63], v[132:135], v[164:167], v[48:63]\n\t"
  "ds_read_b128 v[152:155], v177 offset:8192\n\t"
  "ds_read_b128 v[156:159], v177 offset:12288\n\t"
  "v_mfma_f32_32x32x16_bf16 v[64:79], v[136:139], v[160:163], v[64:79]\n\t"
  "v_mfma_f32_32x32x16_bf16 v[80:95], v[136:139], v[164:167], v[80:95]\n\t"
  "v_mfma_f32_32x32x16_bf16 v[96:111], v[140:143], v[160:163], v[96:111]\n\t"
  "v_mfma_f32_32x32x16_bf16 v[112:127], v[140:143], v[164:167], v[112:127]\n\t"
  "s_waitcnt vmcnt(8)\n\t"
  "LFhcv_%=:\n\t"
  "v_cmp_ne_u32 vcc, s77, v192\n\t"
  "s_cbranch_vccnz LFhsv_%=\n\t"
  "LFhov_%=:\n\t"
  "s_waitcnt vmcnt(6) lgkmcnt(0)\n\t"
  "s_barrier\n\t"
  "s_mov_b32 s68, 32768\n\t"
  "v_add_u32 v178, 32768, v181\n\t"
  "v_xad_u32 v176, v179, 0, s68\n\t"
  "v_xor_b32 v177, 32, v176\n\t"
  "v_mfma_f32_32x32x16_bf16 v[0:15], v[144:147], v[168:171], v[0:15]\n\t"
  "ds_read_b64_tr_b16 v[160:161], v178 offset:0\n\t"
  "ds_read_b64_tr_b16 v[162:163], v178 offset:256\n\t"
  "v_mfma_f32_32x32x16_bf16 v[16:31], v[144:147], v[172:175], v[16:31]\n\t"
  "ds_read_b128 v[128:131], v176 offset:0\n\t"
  "ds_read_b64_tr_b16 v[164:165], v178 offset:512\n\t"
  "v_mfma_f32_32x32x16_bf16 v[32:47], v[148:151], v[168:171], v[32:47]\n\t"
  "ds_read_b64_tr_b16 v[166:167], v178 offset:768\n\t"
  "ds_read_b128 v[132:135], v176 offset:4096\n\t"
  "v_mfma_f32_32x32x16_bf16 v[48:63], v[148:151], v[172:175], v[48:63]\n\t"
  "ds_read_b128 v[136:139], v176 offset:8192\n\t"
  "ds_read_b128 v[140:143], v176 offset:12288\n\t"
  "s_add_u32 m0, s66, 131072\n\t"
  "s_add_u32 s73, s64, 17280\n\t"
  "buffer_load_dwordx4 v182, s[40:43], s73 offen lds\n\t"
  "v_mfma_f32_32x32x16_bf16 v[64:79], v[152:155], v[168:171], v[64:79]\n\t"
  "buffer_load_dwordx4 v183, s[40:43], s73 offen offset:1024 lds\n\t"
  "v_mfma_f32_32x32x16_bf16 v[80:95], v[152:155], v[172:175], v[80:95]\n\t"
  "s_add_u32 m0, s66, 0\n\t"
  "s_add_u32 s73, s71, 0\n\t"
  "buffer_load_dwordx4 v186, s[52:55], s73 offen sc1 lds\n\t"
  "v_mfma_f32_32x32x16_bf16 v[96:111], v[156:159], v[168:171], v[96:111]\n\t"
  "buffer_load_dwordx4 v186, s[52:55], s73 offen offset:1024 sc1 lds\n\t"
  "s_add_u32 s73, s72, 0\n\t"
  "v_mfma_f32_32x32x16_bf16 v[112:127], v[156:159], v[172:175], v[112:127]\n\t"
  "buffer_load_dwordx4 v189, s[56:59], s73 offen offset:2048 lds\n\t"
  "s_waitcnt lgkmcnt(0)\n\t"
  "v_mfma_f32_32x32x16_bf16 v[0:15], v[128:131], v[160:163], v[0:15]\n\t"
  "ds_read_b64_tr_b16 v[168:169], v178 offset:16384\n\t"
  "ds_read_b64_tr_b16 v[170:171], v178 offset:16640\n\t"
  "v_mfma_f32_32x32x16_bf16 v[16:31], v[128:131], v[164:167], v[16:31]\n\t"
  "ds_read_b128 v[144:147], v177 offset:0\n\t"
  "ds_read_b64_tr_b16 v[172:173], v178 offset:16896\n\t"
  "v_mfma_f32_32x32x16_bf16 v[32:47], v[132:135], v[160:163], v[32:47]\n\t"
  "ds_read_b64_tr_b16 v[174:175], v178 offset:17152\n\t"
  "ds_read_b128 v[148:151], v177 offset:4096\n\t"
  "v_mfma_f32_32x32x16_bf16 v[48:63], v[132:135], v[164:167], v[48:63]\n\t"
  "ds_read_b128 v[152:155], v177 offset:8192\n\t"
  "ds_read_b128 v[156:159], v177 offset:12288\n\t"
  "v_mfma_f32_32x32x16_bf16 v[64:79], v[136:139], v[160:163], v[64:79]\n\t"
  "v_mfma_f32_32x32x16_bf16 v[80:95], v[136:139], v[164:167], v[80:95]\n\t"
  "v_mfma_f32_32x32x16_bf16 v[96:111], v[140:143], v[160:163], v[96:111]\n\t"
  "v_mfma_f32_32x32x16_bf16 v[112:127], v[140:143], v[164:167], v[112:127]\n\t"
  "s_waitcnt lgkmcnt(0)\n\t"
  "s_barrier\n\t"
  "s_mov_b32 s68, 32768\n\t"
  "v_add_u32 v178, 65536, v181\n\t"
  "v_xad_u32 v176, v179, 64, s68\n\t"
  "v_xor_b32 v177, 32, v176\n\t"
  "v_mfma_f32_32x32x16_bf16 v[0:15], v[144:147], v[168:171], v[0:15]\n\t"
  "ds_read_b64_tr_b16 v[160:161], v178 offset:0\n\t"
  "ds_read_b64_tr_b16 v[162:163], v178 offset:256\n\t"
  "v_mfma_f32_32x32x16_bf16 v[16:31], v[144:147], v[172:175], v[16:31]\n\t"
  "ds_read_b128 v[128:131], v176 offset:0\n\t"
  "ds_read_b64_tr_b16 v[164:165], v178 offset:512\n\t"
  "v_mfma_f32_32x32x16_bf16 v[32:47], v[148:151], v[168:171], v[32:47]\n\t"
  "ds_read_b64_tr_b16 v[166:167], v178 offset:768\n\t"
  "ds_read_b128 v[132:135], v176 offset:4096\n\t"
  "v_mfma_f32_32x32x16_bf16 v[48:63], v[148:151], v[172:175], v[48:63]\n\t"
  "ds_read_b128 v[136:139], v176 offset:8192\n\t"
  "ds_read_b128 v[140:143], v176 offset:12288\n\t"
  "v_mfma_f32_32x32x16_bf16 v[64:79], v[152:155], v[168:171], v[64:79]\n\t"
  "v_mfma_f32_32x32x16_bf16 v[80:95], v[152:155], v[172:175], v[80:95]\n\t"
  "v_mfma_f32_32x32x16_bf16 v[96:111], v[156:159], v[168:171], v[96:111]\n\t"
  "v_mfma_f32_32x32x16_bf16 v[112:127], v[156:159], v[172:175], v[112:127]\n\t"
  "s_waitcnt lgkmcnt(0)\n\t"
  "v_mfma_f32_32x32x16_bf16 v[0:15], v[128:131], v[160:163], v[0:15]\n\t"
  "ds_read_b64_tr_b16 v[168:169], v178 offset:16384\n\t"
  "ds_read_b64_tr_b16 v[170:171], v178 offset:16640\n\t"
  "v_mfma_f32_32x32x16_bf16 v[16:31], v[128:131], v[164:167], v[16:31]\n\t"
  "ds_read_b128 v[144:147], v177 offset:0\n\t"
  "ds_read_b64_tr_b16 v[172:173], v178 offset:16896\n\t"
  "v_mfma_f32_32x32x16_bf16 v[32:47], v[132:135], v[160:163], v[32:47]\n\t"
  "ds_read_b64_tr_b16 v[174:175], v178 offset:17152\n\t"
  "ds_read_b128 v[148:151], v177 offset:4096\n\t"
  "v_mfma_f32_32x32x16_bf16 v[48:63], v[132:135], v[164:167], v[48:63]\n\t"
  "ds_read_b128 v[152:155], v177 offset:8192\n\t"
  "ds_read_b128 v[156:159], v177 offset:12288\n\t"
  "v_mfma_f32_32x32x16_bf16 v[64:79], v[136:139], v[160:163], v[64:79]\n\t"
  "v_mfma_f32_32x32x16_bf16 v[80:95], v[136:139], v[164:167], v[80:95]\n\t"
  "v_mfma_f32_32x32x16_bf16 v[96:111], v[140:143], v[160:163], v[96:111]\n\t"
  "v_mfma_f32_32x32x16_bf16 v[112:127], v[140:143], v[164:167], v[112:127]\n\t"
  "s_waitcnt vmcnt(3) lgkmcnt(0)\n\t"
  "s_barrier\n\t"
  "s_mov_b32 s68, 98304\n\t"
  "v_add_u32 v178, 98304, v181\n\t"
  "v_xad_u32 v176, v179, 0, s68\n\t"
  "v_xor_b32 v177, 32, v176\n\t"
  "v_mfma_f32_32x32x16_bf16 v[0:15], v[144:147], v[168:171], v[0:15]\n\t"
  "ds_read_b64_tr_b16 v[160:161], v178 offset:0\n\t"
  "ds_read_b64_tr_b16 v[162:163], v178 offset:256\n\t"
  "v_mfma_f32_32x32x16_bf16 v[16:31], v[144:147], v[172:175], v[16:31]\n\t"
  "ds_read_b128 v[128:131], v176 offset:0\n\t"
  "ds_read_b64_tr_b16 v[164:165], v178 offset:512\n\t"
  "v_mfma_f32_32x32x16_bf16 v[32:47], v[148:151], v[168:171], v[32:47]\n\t"
  "ds_read_b64_tr_b16 v[166:167], v178 offset:768\n\t"
  "ds_read_b128 v[132:135], v176 offset:4096\n\t"
  "s_add_u32 m0, s66, 32768\n\t"
  "s_add_u32 s73, s71, 2048\n\t"
  "buffer_load_dwordx4 v186, s[52:55], s73 offen sc1 lds\n\t"
  "v_mfma_f32_32x32x16_bf16 v[48:63], v[148:151], v[172:175], v[48:63]\n\t"
  "ds_read_b128 v[136:139], v176 offset:8192\n\t"
  "ds_read_b128 v[140:143], v176 offset:12288\n\t"
  "buffer_load_dwordx4 v186, s[52:55], s73 offen offset:1024 sc1 lds\n\t"
  "s_add_u32 s73, s72, 262144\n\t"
  "v_mfma_f32_32x32x16_bf16 v[64:79], v[152:155], v[168:171], v[64:79]\n\t"
  "buffer_load_dwordx4 v189, s[56:59], s73 offen offset:2048 lds\n\t"
  "v_mfma_f32_32x32x16_bf16 v[80:95], v[152:155], v[172:175], v[80:95]\n\t"
  "s_add_u32 m0, s66, 65536\n\t"
  "s_add_u32 s73, s71, 4096\n\t"
  "buffer_load_dwordx4 v186, s[52:55], s73 offen sc1 lds\n\t"
  "v_mfma_f32_32x32x16_bf16 v[96:111], v[156:159], v[168:171], v[96:111]\n\t"
  "buffer_load_dwordx4 v186, s[52:55], s73 offen offset:1024 sc1 lds\n\t"
  "s_add_u32 s73, s72, 524288\n\t"
  "v_mfma_f32_32x32x16_bf16 v[112:127], v[156:159], v[172:175], v[112:127]\n\t"
  "buffer_load_dwordx4 v189, s[56:59], s73 offen offset:2048 lds\n\t"
  "s_waitcnt lgkmcnt(0)\n\t"
  "v_mfma_f32_32x32x16_bf16 v[0:15], v[128:131], v[160:163], v[0:15]\n\t"
  "ds_read_b64_tr_b16 v[168:169], v178 offset:16384\n\t"
  "ds_read_b64_tr_b16 v[170:171], v178 offset:16640\n\t"
  "v_mfma_f32_32x32x16_bf16 v[16:31], v[128:131], v[164:167], v[16:31]\n\t"
  "ds_read_b128 v[144:147], v177 offset:0\n\t"
  "ds_read_b64_tr_b16 v[172:173], v178 offset:16896\n\t"
  "v_mfma_f32_32x32x16_bf16 v[32:47], v[132:135], v[160:163], v[32:47]\n\t"
  "ds_read_b64_tr_b16 v[174:175], v178 offset:17152\n\t"
  "ds_read_b128 v[148:151], v177 offset:4096\n\t"
  "v_mfma_f32_32x32x16_bf16 v[48:63], v[132:135], v[164:167], v[48:63]\n\t"
  "ds_read_b128 v[152:155], v177 offset:8192\n\t"
  "ds_read_b128 v[156:159], v177 offset:12288\n\t"
  "v_mfma_f32_32x32x16_bf16 v[64:79], v[136:139], v[160:163], v[64:79]\n\t"
  "v_mfma_f32_32x32x16_bf16 v[80:95], v[136:139], v[164:167], v[80:95]\n\t"
  "v_mfma_f32_32x32x16_bf16 v[96:111], v[140:143], v[160:163], v[96:111]\n\t"
  "v_mfma_f32_32x32x16_bf16 v[112:127], v[140:143], v[164:167], v[112:127]\n\t"
  "s_waitcnt lgkmcnt(0)\n\t"
  "s_barrier\n\t"
  "s_mov_b32 s68, 98304\n\t"
  "v_add_u32 v178, 131072, v181\n\t"
  "v_xad_u32 v176, v179, 64, s68\n\t"
  "v_xor_b32 v177, 32, v176\n\t"
  "v_mfma_f32_32x32x16_bf16 v[0:15], v[144:147], v[168:171], v[0:15]\n\t"
  "ds_read_b64_tr_b16 v[160:161], v178 offset:0\n\t"
  "ds_read_b64_tr_b16 v[162:163], v178 offset:256\n\t"
  "v_mfma_f32_32x32x16_bf16 v[16:31], v[144:147], v[172:175], v[16:31]\n\t"
  "ds_read_b128 v[128:131], v176 offset:0\n\t"
  "ds_read_b64_tr_b16 v[164:165], v178 offset:512\n\t"
  "v_mfma_f32_32x32x16_bf16 v[32:47], v[148:151], v[168:171], v[32:47]\n\t"
  "ds_read_b64_tr_b16 v[166:167], v178 offset:768\n\t"
  "ds_read_b128 v[132:135], v176 offset:4096\n\t"
  "v_mfma_f32_32x32x16_bf16 v[48:63], v[148:151], v[172:175], v[48:63]\n\t"
  "ds_read_b128 v[136:139], v176 offset:8192\n\t"
  "ds_read_b128 v[140:143], v176 offset:12288\n\t"
  "v_mfma_f32_32x32x16_bf16 v[64:79], v[152:155], v[168:171], v[64:79]\n\t"
  "v_mfma_f32_32x32x16_bf16 v[80:95], v[152:155], v[172:175], v[80:95]\n\t"
  "v_mfma_f32_32x32x16_bf16 v[96:111], v[156:159], v[168:171], v[96:111]\n\t"
  "v_mfma_f32_32x32x16_bf16 v[112:127], v[156:159], v[172:175], v[112:127]\n\t"
  "s_waitcnt lgkmcnt(0)\n\t"
  "v_mfma_f32_32x32x16_bf16 v[0:15], v[128:131], v[160:163], v[0:15]\n\t"
  "ds_read_b64_tr_b16 v[168:169], v178 offset:16384\n\t"
  "ds_read_b64_tr_b16 v[170:171], v178 offset:16640\n\t"
  "v_mfma_f32_32x32x16_bf16 v[16:31], v[128:131], v[164:167], v[16:31]\n\t"
  "ds_read_b128 v[144:147], v177 offset:0\n\t"
  "ds_read_b64_tr_b16 v[172:173], v178 offset:16896\n\t"
  "v_mfma_f32_32x32x16_bf16 v[32:47], v[132:135], v[160:163], v[32:47]\n\t"
  "ds_read_b64_tr_b16 v[174:175], v178 offset:17152\n\t"
  "ds_read_b128 v[148:151], v177 offset:4096\n\t"
  "v_mfma_f32_32x32x16_bf16 v[48:63], v[132:135], v[164:167], v[48:63]\n\t"
  "ds_read_b128 v[152:155], v177 offset:8192\n\t"
  "ds_read_b128 v[156:159], v177 offset:12288\n\t"
  "v_mfma_f32_32x32x16_bf16 v[64:79], v[136:139], v[160:163], v[64:79]\n\t"
  "v_mfma_f32_32x32x16_bf16 v[80:95], v[136:139], v[164:167], v[80:95]\n\t"
  "v_mfma_f32_32x32x16_bf16 v[96:111], v[140:143], v[160:163], v[96:111]\n\t"
  "v_mfma_f32_32x32x16_bf16 v[112:127], v[140:143], v[164:167], v[112:127]\n\t"
  "s_waitcnt vmcnt(6) lgkmcnt(0)\n\t"
  "s_barrier\n\t"
  "v_add_u32 v176, 0, v187\n\t"
  "v_add_u32 v178, 0, v188\n\t"
  "v_mfma_f32_32x32x16_bf16 v[0:15], v[144:147], v[168:171], v[0:15]\n\t"
  "ds_read_b64_tr_b16 v[160:161], v178 offset:0\n\t"
  "ds_read_b64_tr_b16 v[162:163], v178 offset:256\n\t"
  "v_mfma_f32_32x32x16_bf16 v[16:31], v[144:147], v[172:175], v[16:31]\n\t"
  "ds_read_b128 v[128:131], v176 offset:0\n\t"
  "ds_read_b64_tr_b16 v[164:165], v178 offset:512\n\t"
  "v_mfma_f32_32x32x16_bf16 v[32:47], v[148:151], v[168:171], v[32:47]\n\t"
  "ds_read_b64_tr_b16 v[166:167], v178 offset:768\n\t"
  "ds_read_b128 v[132:135], v176 offset:16384\n\t"
  "s_add_u32 m0, s66, 98304\n\t"
  "s_add_u32 s73, s71, 6144\n\t"
  "buffer_load_dwordx4 v186, s[52:55], s73 offen sc1 lds\n\t"
  "v_mfma_f32_32x32x16_bf16 v[48:63], v[148:151], v[172:175], v[48:63]\n\t"
  "buffer_load_dwordx4 v186, s[52:55], s73 offen offset:1024 sc1 lds\n\t"
  "s_add_u32 s73, s72, 786432\n\t"
  "v_mfma_f32_32x32x16_bf16 v[64:79], v[152:155], v[168:171], v[64:79]\n\t"
  "buffer_load_dwordx4 v189, s[56:59], s73 offen offset:2048 lds\n\t"
  "v_mfma_f32_32x32x16_bf16 v[80:95], v[152:155], v[172:175], v[80:95]\n\t"
  "s_add_u32 m0, s66, 131072\n\t"
  "s_add_u32 s73, s71, 8192\n\t"
  "buffer_load_dwordx4 v186, s[52:55], s73 offen sc1 lds\n\t"
  "v_mfma_f32_32x32x16_bf16 v[96:111], v[156:159], v[168:171], v[96:111]\n\t"
  "buffer_load_dwordx4 v186, s[52:55], s73 offen offset:1024 sc1 lds\n\t"
  "s_add_u32 s73, s72, 1048576\n\t"
  "v_mfma_f32_32x32x16_bf16 v[112:127], v[156:159], v[172:175], v[112:127]\n\t"
  "buffer_load_dwordx4 v189, s[56:59], s73 offen offset:2048 lds\n\t"
  "v_add_u32 v233, 0x1000, v186\n\t"
  "v_add_u32 v234, 0x2000, v186\n\t"
  "v_add_u32 v235, 0x3000, v186\n\t"
  "v_mov_b32 v232, v186\n\t"
  "s_xor_b32 s68, s70, 0x800\n\t"
  "s_add_u32 s68, s68, 0x4000\n\t"
  "s_nop 3\n\t"
  "v_cvt_pk_bf16_f32 v64, v64, v65\n\t"
  "v_cvt_pk_bf16_f32 v65, v66, v67\n\t"
  "v_cvt_pk_bf16_f32 v66, v68, v69\n\t"
  "v_cvt_pk_bf16_f32 v67, v70, v71\n\t"
  "v_cvt_pk_bf16_f32 v68, v72, v73\n\t"
  "v_cvt_pk_bf16_f32 v69, v74, v75\n\t"
  "v_cvt_pk_bf16_f32 v70, v76, v77\n\t"
  "v_cvt_pk_bf16_f32 v71, v78, v79\n\t"
  "v_cvt_pk_bf16_f32 v72, v80, v81\n\t"
  "v_cvt_pk_bf16_f32 v73, v82, v83\n\t"
  "v_cvt_pk_bf16_f32 v74, v84, v85\n\t"
  "v_cvt_pk_bf16_f32 v75, v86, v87\n\t"
  "v_cvt_pk_bf16_f32 v76, v88, v89\n\t"
  "v_cvt_pk_bf16_f32 v77, v90, v91\n\t"
  "v_cvt_pk_bf16_f32 v78, v92, v93\n\t"
  "v_cvt_pk_bf16_f32 v79, v94, v95\n\t"
  "v_cvt_pk_bf16_f32 v80, v96, v97\n\t"
  "v_cvt_pk_bf16_f32 v81, v98, v99\n\t"
  "v_cvt_pk_bf16_f32 v82, v100, v101\n\t"
  "v_cvt_pk_bf16_f32 v83, v102, v103\n\t"
  "v_cvt_pk_bf16_f32 v84, v104, v105\n\t"
  "v_cvt_pk_bf16_f32 v85, v106, v107\n\t"
  "v_cvt_pk_bf16_f32 v86, v108, v109\n\t"
  "v_cvt_pk_bf16_f32 v87, v110, v111\n\t"
  "v_cvt_pk_bf16_f32 v88, v112, v113\n\t"
  "v_cvt_pk_bf16_f32 v89, v114, v115\n\t"
  "v_cvt_pk_bf16_f32 v90, v116, v117\n\t"
  "v_cvt_pk_bf16_f32 v91, v118, v119\n\t"
  "v_cvt_pk_bf16_f32 v92, v120, v121\n\t"
  "v_cvt_pk_bf16_f32 v93, v122, v123\n\t"
  "v_cvt_pk_bf16_f32 v94, v124, v125\n\t"
  "v_cvt_pk_bf16_f32 v95, v126, v127\n\t"
  "v_cvt_pk_bf16_f32 v96, v0, v1\n\t"
  "v_cvt_pk_bf16_f32 v97, v2, v3\n\t"
  "v_cvt_pk_bf16_f32 v98, v4, v5\n\t"
  "v_cvt_pk_bf16_f32 v99, v6, v7\n\t"
  "v_cvt_pk_bf16_f32 v100, v8, v9\n\t"
  "v_cvt_pk_bf16_f32 v101, v10, v11\n\t"
  "v_cvt_pk_bf16_f32 v102, v12, v13\n\t"
  "v_cvt_pk_bf16_f32 v103, v14, v15\n\t"
  "v_cvt_pk_bf16_f32 v104, v16, v17\n\t"
  "v_cvt_pk_bf16_f32 v105, v18, v19\n\t"
  "v_cvt_pk_bf16_f32 v106, v20, v21\n\t"
  "v_cvt_pk_bf16_f32 v107, v22, v23\n\t"
  "v_cvt_pk_bf16_f32 v108, v24, v25\n\t"
  "v_cvt_pk_bf16_f32 v109, v26, v27\n\t"
  "v_cvt_pk_bf16_f32 v110, v28, v29\n\t"
  "v_cvt_pk_bf16_f32 v111, v30, v31\n\t"
  "v_cvt_pk_bf16_f32 v112, v32, v33\n\t"
  "v_cvt_pk_bf16_f32 v113, v34, v35\n\t"
  "v_cvt_pk_bf16_f32 v114, v36, v37\n\t"
  "v_cvt_pk_bf16_f32 v115, v38, v39\n\t"
  "v_cvt_pk_bf16_f32 v116, v40, v41\n\t"
  "v_cvt_pk_bf16_f32 v117, v42, v43\n\t"
  "v_cvt_pk_bf16_f32 v118, v44, v45\n\t"
  "v_cvt_pk_bf16_f32 v119, v46, v47\n\t"
  "v_cvt_pk_bf16_f32 v120, v48, v49\n\t"
  "v_cvt_pk_bf16_f32 v121, v50, v51\n\t"
  "v_cvt_pk_bf16_f32 v122, v52, v53\n\t"
  "v_cvt_pk_bf16_f32 v123, v54, v55\n\t"
  "v_cvt_pk_bf16_f32 v124, v56, v57\n\t"
  "v_cvt_pk_bf16_f32 v125, v58, v59\n\t"
  "v_cvt_pk_bf16_f32 v126, v60, v61\n\t"
  "v_cvt_pk_bf16_f32 v127, v62, v63\n\t"
  "s_waitcnt lgkmcnt(3)\n\t"
  "v_mfma_f32_32x32x16_bf16 v[0:15], v[160:163], v[128:131], 0\n\t"
  "ds_read_b64_tr_b16 v[168:169], v178 offset:16384\n\t"
  "ds_read_b64_tr_b16 v[170:171], v178 offset:16640\n\t"
  "s_waitcnt lgkmcnt(3)\n\t"
  "v_mfma_f32_32x32x16_bf16 v[16:31], v[164:167], v[128:131], 0\n\t"
  "ds_read_b128 v[144:147], v176 offset:1024\n\t"
  "ds_read_b64_tr_b16 v[172:173], v178 offset:16896\n\t"
  "s_waitcnt lgkmcnt(4)\n\t"
  "v_mfma_f32_32x32x16_bf16 v[32:47], v[160:163], v[132:135], 0\n\t"
  "ds_read_b64_tr_b16 v[174:175], v178 offset:17152\n\t"
  "ds_read_b128 v[148:151], v176 offset:17408\n\t"
  "v_mfma_f32_32x32x16_bf16 v[48:63], v[164:167], v[132:135], 0\n\t"
  "s_waitcnt vmcnt(9) lgkmcnt(0)\n\t"
  "s_barrier\n\t"
  "v_add_u32 v176, 32768, v187\n\t"
  "v_add_u32 v178, 32768, v188\n\t"
  "v_mfma_f32_32x32x16_bf16 v[0:15], v[168:171], v[144:147], v[0:15]\n\t"
  "ds_read_b64_tr_b16 v[160:161], v178 offset:0\n\t"
  "ds_read_b64_tr_b16 v[162:163], v178 offset:256\n\t"
  "v_mfma_f32_32x32x16_bf16 v[16:31], v[172:175], v[144:147], v[16:31]\n\t"
  "ds_read_b128 v[128:131], v176 offset:0\n\t"
  "ds_read_b64_tr_b16 v[164:165], v178 offset:512\n\t"
  "s_add_u32 m0, s67, 0\n\t"
  "s_add_u32 s73, s75, 10240\n\t"
  "buffer_load_dwordx4 v186, s[52:55], s73 offen sc1 lds\n\t"
  "v_mfma_f32_32x32x16_bf16 v[32:47], v[168:171], v[148:151], v[32:47]\n\t"
  "ds_read_b64_tr_b16 v[166:167], v178 offset:768\n\t"
  "ds_read_b128 v[132:135], v176 offset:16384\n\t"
  "buffer_load_dwordx4 v186, s[52:55], s73 offen offset:1024 sc1 lds\n\t"
  "s_add_u32 s73, s76, 1310720\n\t"
  "v_mfma_f32_32x32x16_bf16 v[48:63], v[172:175], v[148:151], v[48:63]\n\t"
  "buffer_load_dwordx4 v189, s[56:59], s73 offen offset:2048 lds\n\t"
  "s_waitcnt lgkmcnt(3)\n\t"
  "v_mfma_f32_32x32x16_bf16 v[0:15], v[160:163], v[128:131], v[0:15]\n\t"
  "ds_read_b64_tr_b16 v[168:169], v178 offset:16384\n\t"
  "ds_read_b64_tr_b16 v[170:171], v178 offset:16640\n\t"
  "s_waitcnt lgkmcnt(3)\n\t"
  "v_mfma_f32_32x32x16_bf16 v[16:31], v[164:167], v[128:131], v[16:31]\n\t"
  "ds_read_b128 v[144:147], v176 offset:1024\n\t"
  "ds_read_b64_tr_b16 v[172:173], v178 offset:16896\n\t"
  "s_add_u32 m0, s67, 16384\n\t"
  "s_add_u32 s73, s78, 10240\n\t"
  "buffer_load_dwordx4 v186, s[52:55], s73 offen sc1 lds\n\t"
  "s_waitcnt lgkmcnt(4)\n\t"
  "v_mfma_f32_32x32x16_bf16 v[32:47], v[160:163], v[132:135], v[32:47]\n\t"
  "ds_read_b64_tr_b16 v[174:175], v178 offset:17152\n\t"
  "ds_read_b128 v[148:151], v176 offset:17408\n\t"
  "buffer_load_dwordx4 v186, s[52:55], s73 offen offset:1024 sc1 lds\n\t"
  "s_add_u32 s73, s79, 1310720\n\t"
  "v_mfma_f32_32x32x16_bf16 v[48:63], v[164:167], v[132:135], v[48:63]\n\t"
  "buffer_load_dwordx4 v189, s[56:59], s73 offen offset:2048 lds\n\t"
  "s_waitcnt vmcnt(12) lgkmcnt(0)\n\t"
  "s_barrier\n\t"
  "v_add_u32 v176, 65536, v187\n\t"
  "v_add_u32 v178, 65536, v188\n\t"
  "v_mfma_f32_32x32x16_bf16 v[0:15], v[168:171], v[144:147], v[0:15]\n\t"
  "ds_read_b64_tr_b16 v[160:161], v178 offset:0\n\t"
  "ds_read_b64_tr_b16 v[162:163], v178 offset:256\n\t"
  "v_mfma_f32_32x32x16_bf16 v[16:31], v[172:175], v[144:147], v[16:31]\n\t"
  "ds_read_b128 v[128:131], v176 offset:0\n\t"
  "ds_read_b64_tr_b16 v[164:165], v178 offset:512\n\t"
  "s_add_u32 m0, s67, 32768\n\t"
  "s_add_u32 s73, s75, 12288\n\t"
  "buffer_load_dwordx4 v186, s[52:55], s73 offen sc1 lds\n\t"
  "v_mfma_f32_32x32x16_bf16 v[32:47], v[168:171], v[148:151], v[32:47]\n\t"
  "ds_read_b64_tr_b16 v[166:167], v178 offset:768\n\t"
  "ds_read_b128 v[132:135], v176 offset:16384\n\t"
  "buffer_load_dwordx4 v186, s[52:55], s73 offen offset:1024 sc1 lds\n\t"
  "s_add_u32 s73, s76, 1572864\n\t"
  "v_mfma_f32_32x32x16_bf16 v[48:63], v[172:175], v[148:151], v[48:63]\n\t"
  "buffer_load_dwordx4 v189, s[56:59], s73 offen offset:2048 lds\n\t"
  "s_waitcnt lgkmcnt(3)\n\t"
  "v_mfma_f32_32x32x16_bf16 v[0:15], v[160:163], v[128:131], v[0:15]\n\t"
  "ds_read_b64_tr_b16 v[168:169], v178 offset:16384\n\t"
  "ds_read_b64_tr_b16 v[170:171], v178 offset:16640\n\t"
  "s_waitcnt lgkmcnt(3)\n\t"
  "v_mfma_f32_32x32x16_bf16 v[16:31], v[164:167], v[128:131], v[16:31]\n\t"
  "ds_read_b128 v[144:147], v176 offset:1024\n\t"
  "ds_read_b64_tr_b16 v[172:173], v178 offset:16896\n\t"
  "s_add_u32 m0, s67, 49152\n\t"
  "s_add_u32 s73, s78, 12288\n\t"
  "buffer_load_dwordx4 v186, s[52:55], s73 offen sc1 lds\n\t"
  "s_waitcnt lgkmcnt(4)\n\t"
  "v_mfma_f32_32x32x16_bf16 v[32:47], v[160:163], v[132:135], v[32:47]\n\t"
  "ds_read_b64_tr_b16 v[174:175], v178 offset:17152\n\t"
  "ds_read_b128 v[148:151], v176 offset:17408\n\t"
  "buffer_load_dwordx4 v186, s[52:55], s73 offen offset:1024 sc1 lds\n\t"
  "s_add_u32 s73, s79, 1572864\n\t"
  "v_mfma_f32_32x32x16_bf16 v[48:63], v[164:167], v[132:135], v[48:63]\n\t"
  "buffer_load_dwordx4 v189, s[56:59], s73 offen offset:2048 lds\n\t"
  "s_waitcnt vmcnt(15) lgkmcnt(0)\n\t"
  "s_barrier\n\t"
  "v_add_u32 v176, 98304, v187\n\t"
  "v_add_u32 v178, 98304, v188\n\t"
  "v_mfma_f32_32x32x16_bf16 v[0:15], v[168:171], v[144:147], v[0:15]\n\t"
  "ds_read_b64_tr_b16 v[160:161], v178 offset:0\n\t"
  "ds_read_b64_tr_b16 v[162:163], v178 offset:256\n\t"
  "v_mfma_f32_32x32x16_bf16 v[16:31], v[172:175], v[144:147], v[16:31]\n\t"
  "ds_read_b128 v[128:131], v176 offset:0\n\t"
  "ds_read_b64_tr_b16 v[164:165], v178 offset:512\n\t"
  "s_add_u32 m0, s67, 65536\n\t"
  "s_add_u32 s73, s75, 14336\n\t"
  "buffer_load_dwordx4 v186, s[52:55], s73 offen sc1 lds\n\t"
  "v_mfma_f32_32x32x16_bf16 v[32:47], v[168:171], v[148:151], v[32:47]\n\t"
  "ds_read_b64_tr_b16 v[166:167], v178 offset:768\n\t"
  "ds_read_b128 v[132:135], v176 offset:16384\n\t"
  "buffer_load_dwordx4 v186, s[52:55], s73 offen offset:1024 sc1 lds\n\t"
  "s_add_u32 s73, s76, 1835008\n\t"
  "v_mfma_f32_32x32x16_bf16 v[48:63], v[172:175], v[148:151], v[48:63]\n\t"
  "buffer_load_dwordx4 v189, s[56:59], s73 offen offset:2048 lds\n\t"
  "s_waitcnt lgkmcnt(3)\n\t"
  "v_mfma_f32_32x32x16_bf16 v[0:15], v[160:163], v[128:131], v[0:15]\n\t"
  "ds_read_b64_tr_b16 v[168:169], v178 offset:16384\n\t"
  "ds_read_b64_tr_b16 v[170:171], v178 offset:16640\n\t"
  "s_waitcnt lgkmcnt(3)\n\t"
  "v_mfma_f32_32x32x16_bf16 v[16:31], v[164:167], v[128:131], v[16:31]\n\t"
  "ds_read_b128 v[144:147], v176 offset:1024\n\t"
  "ds_read_b64_tr_b16 v[172:173], v178 offset:16896\n\t"
  "s_add_u32 m0, s67, 81920\n\t"
  "s_add_u32 s73, s78, 14336\n\t"
  "buffer_load_dwordx4 v186, s[52:55], s73 offen sc1 lds\n\t"
  "s_waitcnt lgkmcnt(4)\n\t"
  "v_mfma_f32_32x32x16_bf16 v[32:47], v[160:163], v[132:135], v[32:47]\n\t"
  "ds_read_b64_tr_b16 v[174:175], v178 offset:17152\n\t"
  "ds_read_b128 v[148:151], v176 offset:17408\n\t"
  "buffer_load_dwordx4 v186, s[52:55], s73 offen offset:1024 sc1 lds\n\t"
  "s_add_u32 s73, s79, 1835008\n\t"
  "v_mfma_f32_32x32x16_bf16 v[48:63], v[164:167], v[132:135], v[48:63]\n\t"
  "buffer_load_dwordx4 v189, s[56:59], s73 offen offset:2048 lds\n\t"
  "s_waitcnt vmcnt(18) lgkmcnt(0)\n\t"
  "s_barrier\n\t"
  "v_add_u32 v176, 131072, v187\n\t"
  "v_add_u32 v178, 131072, v188\n\t"
  "v_mfma_f32_32x32x16_bf16 v[0:15], v[168:171], v[144:147], v[0:15]\n\t"
  "ds_read_b64_tr_b16 v[160:161], v178 offset:0\n\t"
  "ds_read_b64_tr_b16 v[162:163], v178 offset:256\n\t"
  "v_mfma_f32_32x32x16_bf16 v[16:31], v[172:175], v[144:147], v[16:31]\n\t"
  "ds_read_b128 v[128:131], v176 offset:0\n\t"
  "ds_read_b64_tr_b16 v[164:165], v178 offset:512\n\t"
  "s_add_u32 m0, s67, 98304\n\t"
  "s_add_u32 s73, s75, 16384\n\t"
  "buffer_load_dwordx4 v186, s[52:55], s73 offen sc1 lds\n\t"
  "v_mfma_f32_32x32x16_bf16 v[32:47], v[168:171], v[148:151], v[32:47]\n\t"
  "ds_read_b64_tr_b16 v[166:167], v178 offset:768\n\t"
  "ds_read_b128 v[132:135], v176 offset:16384\n\t"
  "buffer_load_dwordx4 v186, s[52:55], s73 offen offset:1024 sc1 lds\n\t"
  "s_add_u32 s73, s76, 2097152\n\t"
  "v_mfma_f32_32x32x16_bf16 v[48:63], v[172:175], v[148:151], v[48:63]\n\t"
  "buffer_load_dwordx4 v189, s[56:59], s73 offen offset:2048 lds\n\t"
  "s_waitcnt lgkmcnt(3)\n\t"
  "v_mfma_f32_32x32x16_bf16 v[0:15], v[160:163], v[128:131], v[0:15]\n\t"
  "ds_read_b64_tr_b16 v[168:169], v178 offset:16384\n\t"
  "ds_read_b64_tr_b16 v[170:171], v178 offset:16640\n\t"
  "s_waitcnt lgkmcnt(3)\n\t"
  "v_mfma_f32_32x32x16_bf16 v[16:31], v[164:167], v[128:131], v[16:31]\n\t"
  "ds_read_b128 v[144:147], v176 offset:1024\n\t"
  "ds_read_b64_tr_b16 v[172:173], v178 offset:16896\n\t"
  "s_add_u32 m0, s67, 114688\n\t"
  "s_add_u32 s73, s78, 16384\n\t"
  "buffer_load_dwordx4 v186, s[52:55], s73 offen sc1 lds\n\t"
  "s_waitcnt lgkmcnt(4)\n\t"
  "v_mfma_f32_32x32x16_bf16 v[32:47], v[160:163], v[132:135], v[32:47]\n\t"
  "ds_read_b64_tr_b16 v[174:175], v178 offset:17152\n\t"
  "ds_read_b128 v[148:151], v176 offset:17408\n\t"
  "buffer_load_dwordx4 v186, s[52:55], s73 offen offset:1024 sc1 lds\n\t"
  "s_add_u32 s73, s79, 2097152\n\t"
  "v_mfma_f32_32x32x16_bf16 v[48:63], v[164:167], v[132:135], v[48:63]\n\t"
  "buffer_load_dwordx4 v189, s[56:59], s73 offen offset:2048 lds\n\t"
  "s_waitcnt vmcnt(18) lgkmcnt(0)\n\t"
  "s_barrier\n\t"
  "v_add_u32 v176, 0, v187\n\t"
  "v_add_u32 v178, 0, v188\n\t"
  "v_mfma_f32_32x32x16_bf16 v[0:15], v[168:171], v[144:147], v[0:15]\n\t"
  "ds_read_b64_tr_b16 v[160:161], v178 offset:0\n\t"
  "ds_read_b64_tr_b16 v[162:163], v178 offset:256\n\t"
  "v_mfma_f32_32x32x16_bf16 v[16:31], v[172:175], v[144:147], v[16:31]\n\t"
  "ds_read_b128 v[128:131], v176 offset:0\n\t"
  "ds_read_b64_tr_b16 v[164:165], v178 offset:512\n\t"
  "s_add_u32 m0, s67, 131072\n\t"
  "s_add_u32 s73, s75, 18432\n\t"
  "buffer_load_dwordx4 v186, s[52:55], s73 offen sc1 lds\n\t"
  "v_mfma_f32_32x32x16_bf16 v[32:47], v[168:171], v[148:151], v[32:47]\n\t"
  "ds_read_b64_tr_b16 v[166:167], v178 offset:768\n\t"
  "ds_read_b128 v[132:135], v176 offset:16384\n\t"
  "buffer_load_dwordx4 v186, s[52:55], s73 offen offset:1024 sc1 lds\n\t"
  "s_add_u32 s73, s76, 2359296\n\t"
  "v_mfma_f32_32x32x16_bf16 v[48:63], v[172:175], v[148:151], v[48:63]\n\t"
  "buffer_load_dwordx4 v189, s[56:59], s73 offen offset:2048 lds\n\t"
  "s_waitcnt lgkmcnt(3)\n\t"
  "v_mfma_f32_32x32x16_bf16 v[0:15], v[160:163], v[128:131], v[0:15]\n\t"
  "ds_read_b64_tr_b16 v[168:169], v178 offset:16384\n\t"
  "ds_read_b64_tr_b16 v[170:171], v178 offset:16640\n\t"
  "s_waitcnt lgkmcnt(3)\n\t"
  "v_mfma_f32_32x32x16_bf16 v[16:31], v[164:167], v[128:131], v[16:31]\n\t"
  "ds_read_b128 v[144:147], v176 offset:1024\n\t"
  "ds_read_b64_tr_b16 v[172:173], v178 offset:16896\n\t"
  "s_add_u32 m0, s67, 147456\n\t"
  "s_add_u32 s73, s78, 18432\n\t"
  "buffer_load_dwordx4 v186, s[52:55], s73 offen sc1 lds\n\t"
  "s_waitcnt lgkmcnt(4)\n\t"
  "v_mfma_f32_32x32x16_bf16 v[32:47], v[160:163], v[132:135], v[32:47]\n\t"
  "ds_read_b64_tr_b16 v[174:175], v178 offset:17152\n\t"
  "ds_read_b128 v[148:151], v176 offset:17408\n\t"
  "buffer_load_dwordx4 v186, s[52:55], s73 offen offset:1024 sc1 lds\n\t"
  "s_add_u32 s73, s79, 2359296\n\t"
  "v_mfma_f32_32x32x16_bf16 v[48:63], v[164:167], v[132:135], v[48:63]\n\t"
  "buffer_load_dwordx4 v189, s[56:59], s73 offen offset:2048 lds\n\t"
  "s_waitcnt vmcnt(18) lgkmcnt(0)\n\t"
  "s_barrier\n\t"
  "v_add_u32 v176, 32768, v187\n\t"
  "v_add_u32 v178, 32768, v188\n\t"
  "v_mfma_f32_32x32x16_bf16 v[0:15], v[168:171], v[144:147], v[0:15]\n\t"
  "ds_read_b64_tr_b16 v[160:161], v178 offset:0\n\t"
  "ds_read_b64_tr_b16 v[162:163], v178 offset:256\n\t"
  "v_mfma_f32_32x32x16_bf16 v[16:31], v[172:175], v[144:147], v[16:31]\n\t"
  "ds_read_b128 v[128:131], v176 offset:0\n\t"
  "ds_read_b64_tr_b16 v[164:165], v178 offset:512\n\t"
  "s_add_u32 m0, s67, 0\n\t"
  "s_add_u32 s73, s75, 20480\n\t"
  "buffer_load_dwordx4 v186, s[52:55], s73 offen sc1 lds\n\t"
  "v_mfma_f32_32x32x16_bf16 v[32:47], v[168:171], v[148:151], v[32:47]\n\t"
  "ds_read_b64_tr_b16 v[166:167], v178 offset:768\n\t"
  "ds_read_b128 v[132:135], v176 offset:16384\n\t"
  "buffer_load_dwordx4 v186, s[52:55], s73 offen offset:1024 sc1 lds\n\t"
  "s_add_u32 s73, s76, 2621440\n\t"
  "v_mfma_f32_32x32x16_bf16 v[48:63], v[172:175], v[148:151], v[48:63]\n\t"
  "buffer_load_dwordx4 v189, s[56:59], s73 offen offset:2048 lds\n\t"
  "s_waitcnt lgkmcnt(3)\n\t"
  "v_mfma_f32_32x32x16_bf16 v[0:15], v[160:163], v[128:131], v[0:15]\n\t"
  "ds_read_b64_tr_b16 v[168:169], v178 offset:16384\n\t"
  "ds_read_b64_tr_b16 v[170:171], v178 offset:16640\n\t"
  "s_waitcnt lgkmcnt(3)\n\t"
  "v_mfma_f32_32x32x16_bf16 v[16:31], v[164:167], v[128:131], v[16:31]\n\t"
  "ds_read_b128 v[144:147], v176 offset:1024\n\t"
  "ds_read_b64_tr_b16 v[172:173], v178 offset:16896\n\t"
  "s_add_u32 m0, s67, 16384\n\t"
  "s_add_u32 s73, s78, 20480\n\t"
  "buffer_load_dwordx4 v186, s[52:55], s73 offen sc1 lds\n\t"
  "s_waitcnt lgkmcnt(4)\n\t"
  "v_mfma_f32_32x32x16_bf16 v[32:47], v[160:163], v[132:135], v[32:47]\n\t"
  "ds_read_b64_tr_b16 v[174:175], v178 offset:17152\n\t"
  "ds_read_b128 v[148:151], v176 offset:17408\n\t"
  "buffer_load_dwordx4 v186, s[52:55], s73 offen offset:1024 sc1 lds\n\t"
  "s_add_u32 s73, s79, 2621440\n\t"
  "v_mfma_f32_32x32x16_bf16 v[48:63], v[164:167], v[132:135], v[48:63]\n\t"
  "buffer_load_dwordx4 v189, s[56:59], s73 offen offset:2048 lds\n\t"
  "s_waitcnt vmcnt(18) lgkmcnt(0)\n\t"
  "s_barrier\n\t"
  "v_add_u32 v176, 65536, v187\n\t"
  "v_add_u32 v178, 65536, v188\n\t"
  "v_mfma_f32_32x32x16_bf16 v[0:15], v[168:171], v[144:147], v[0:15]\n\t"
  "ds_read_b64_tr_b16 v[160:161], v178 offset:0\n\t"
  "ds_read_b64_tr_b16 v[162:163], v178 offset:256\n\t"
  "v_mfma_f32_32x32x16_bf16 v[16:31], v[172:175], v[144:147], v[16:31]\n\t"
  "ds_read_b128 v[128:131], v176 offset:0\n\t"
  "ds_read_b64_tr_b16 v[164:165], v178 offset:512\n\t"
  "s_add_u32 m0, s67, 32768\n\t"
  "s_add_u32 s73, s75, 22528\n\t"
  "buffer_load_dwordx4 v186, s[52:55], s73 offen sc1 lds\n\t"
  "v_mfma_f32_32x32x16_bf16 v[32:47], v[168:171], v[148:151], v[32:47]\n\t"
  "ds_read_b64_tr_b16 v[166:167], v178 offset:768\n\t"
  "ds_read_b128 v[132:135], v176 offset:16384\n\t"
  "buffer_load_dwordx4 v186, s[52:55], s73 offen offset:1024 sc1 lds\n\t"
  "s_add_u32 s73, s76, 2883584\n\t"
  "v_mfma_f32_32x32x16_bf16 v[48:63], v[172:175], v[148:151], v[48:63]\n\t"
  "buffer_load_dwordx4 v189, s[56:59], s73 offen offset:2048 lds\n\t"
  "s_waitcnt lgkmcnt(3)\n\t"
  "v_mfma_f32_32x32x16_bf16 v[0:15], v[160:163], v[128:131], v[0:15]\n\t"
  "ds_read_b64_tr_b16 v[168:169], v178 offset:16384\n\t"
  "ds_read_b64_tr_b16 v[170:171], v178 offset:16640\n\t"
  "s_waitcnt lgkmcnt(3)\n\t"
  "v_mfma_f32_32x32x16_bf16 v[16:31], v[164:167], v[128:131], v[16:31]\n\t"
  "ds_read_b128 v[144:147], v176 offset:1024\n\t"
  "ds_read_b64_tr_b16 v[172:173], v178 offset:16896\n\t"
  "s_add_u32 m0, s67, 49152\n\t"
  "s_add_u32 s73, s78, 22528\n\t"
  "buffer_load_dwordx4 v186, s[52:55], s73 offen sc1 lds\n\t"
  "s_waitcnt lgkmcnt(4)\n\t"
  "v_mfma_f32_32x32x16_bf16 v[32:47], v[160:163], v[132:135], v[32:47]\n\t"
  "ds_read_b64_tr_b16 v[174:175], v178 offset:17152\n\t"
  "ds_read_b128 v[148:151], v176 offset:17408\n\t"
  "buffer_load_dwordx4 v186, s[52:55], s73 offen offset:1024 sc1 lds\n\t"
  "s_add_u32 s73, s79, 2883584\n\t"
  "v_mfma_f32_32x32x16_bf16 v[48:63], v[164:167], v[132:135], v[48:63]\n\t"
  "buffer_load_dwordx4 v189, s[56:59], s73 offen offset:2048 lds\n\t"
  "s_waitcnt vmcnt(18) lgkmcnt(0)\n\t"
  "s_barrier\n\t"
  "v_add_u32 v176, 98304, v187\n\t"
  "v_add_u32 v178, 98304, v188\n\t"
  "v_mfma_f32_32x32x16_bf16 v[0:15], v[168:171], v[144:147], v[0:15]\n\t"
  "ds_read_b64_tr_b16 v[160:161], v178 offset:0\n\t"
  "ds_read_b64_tr_b16 v[162:163], v178 offset:256\n\t"
  "v_mfma_f32_32x32x16_bf16 v[16:31], v[172:175], v[144:147], v[16:31]\n\t"
  "ds_read_b128 v[128:131], v176 offset:0\n\t"
  "ds_read_b64_tr_b16 v[164:165], v178 offset:512\n\t"
  "s_add_u32 m0, s67, 65536\n\t"
  "s_add_u32 s73, s75, 24576\n\t"
  "buffer_load_dwordx4 v186, s[52:55], s73 offen sc1 lds\n\t"
  "v_mfma_f32_32x32x16_bf16 v[32:47], v[168:171], v[148:151], v[32:47]\n\t"
  "ds_read_b64_tr_b16 v[166:167], v178 offset:768\n\t"
  "ds_read_b128 v[132:135], v176 offset:16384\n\t"
  "buffer_load_dwordx4 v186, s[52:55], s73 offen offset:1024 sc1 lds\n\t"
  "s_add_u32 s73, s76, 3145728\n\t"
  "v_mfma_f32_32x32x16_bf16 v[48:63], v[172:175], v[148:151], v[48:63]\n\t"
  "buffer_load_dwordx4 v189, s[56:59], s73 offen offset:2048 lds\n\t"
  "s_waitcnt lgkmcnt(3)\n\t"
  "v_mfma_f32_32x32x16_bf16 v[0:15], v[160:163], v[128:131], v[0:15]\n\t"
  "ds_read_b64_tr_b16 v[168:169], v178 offset:16384\n\t"
  "ds_read_b64_tr_b16 v[170:171], v178 offset:16640\n\t"
  "s_waitcnt lgkmcnt(3)\n\t"
  "v_mfma_f32_32x32x16_bf16 v[16:31], v[164:167], v[128:131], v[16:31]\n\t"
  "ds_read_b128 v[144:147], v176 offset:1024\n\t"
  "ds_read_b64_tr_b16 v[172:173], v178 offset:16896\n\t"
  "s_add_u32 m0, s67, 81920\n\t"
  "s_add_u32 s73, s78, 24576\n\t"
  "buffer_load_dwordx4 v186, s[52:55], s73 offen sc1 lds\n\t"
  "s_waitcnt lgkmcnt(4)\n\t"
  "v_mfma_f32_32x32x16_bf16 v[32:47], v[160:163], v[132:135], v[32:47]\n\t"
  "ds_read_b64_tr_b16 v[174:175], v178 offset:17152\n\t"
  "ds_read_b128 v[148:151], v176 offset:17408\n\t"
  "buffer_load_dwordx4 v186, s[52:55], s73 offen offset:1024 sc1 lds\n\t"
  "s_add_u32 s73, s79, 3145728\n\t"
  "v_mfma_f32_32x32x16_bf16 v[48:63], v[164:167], v[132:135], v[48:63]\n\t"
  "buffer_load_dwordx4 v189, s[56:59], s73 offen offset:2048 lds\n\t"
  "s_waitcnt vmcnt(18) lgkmcnt(0)\n\t"
  "s_barrier\n\t"
  "v_add_u32 v176, 131072, v187\n\t"
  "v_add_u32 v178, 131072, v188\n\t"
  "v_mfma_f32_32x32x16_bf16 v[0:15], v[168:171], v[144:147], v[0:15]\n\t"
  "ds_read_b64_tr_b16 v[160:161], v178 offset:0\n\t"
  "ds_read_b64_tr_b16 v[162:163], v178 offset:256\n\t"
  "v_mfma_f32_32x32x16_bf16 v[16:31], v[172:175], v[144:147], v[16:31]\n\t"
  "ds_read_b128 v[128:131], v176 offset:0\n\t"
  "ds_read_b64_tr_b16 v[164:165], v178 offset:512\n\t"
  "v_mfma_f32_32x32x16_bf16 v[32:47], v[168:171], v[148:151], v[32:47]\n\t"
  "ds_read_b64_tr_b16 v[166:167], v178 offset:768\n\t"
  "ds_read_b128 v[132:135], v176 offset:16384\n\t"
  "v_mfma_f32_32x32x16_bf16 v[48:63], v[172:175], v[148:151], v[48:63]\n\t"
  "buffer_store_dwordx4 v[96:99], v232, s[48:51], s70 offen offset:0 sc1\n\t"
  "s_waitcnt lgkmcnt(3)\n\t"
  "v_mfma_f32_32x32x16_bf16 v[0:15], v[160:163], v[128:131], v[0:15]\n\t"
  "ds_read_b64_tr_b16 v[168:169], v178 offset:16384\n\t"
  "ds_read_b64_tr_b16 v[170:171], v178 offset:16640\n\t"
  "s_waitcnt lgkmcnt(3)\n\t"
  "v_mfma_f32_32x32x16_bf16 v[16:31], v[164:167], v[128:131], v[16:31]\n\t"
  "ds_read_b128 v[144:147], v176 offset:1024\n\t"
  "ds_read_b64_tr_b16 v[172:173], v178 offset:16896\n\t"
  "s_waitcnt lgkmcnt(4)\n\t"
  "v_mfma_f32_32x32x16_bf16 v[32:47], v[160:163], v[132:135], v[32:47]\n\t"
  "ds_read_b64_tr_b16 v[174:175], v178 offset:17152\n\t"
  "ds_read_b128 v[148:151], v176 offset:17408\n\t"
  "v_mfma_f32_32x32x16_bf16 v[48:63], v[164:167], v[132:135], v[48:63]\n\t"
  "buffer_store_dwordx4 v[100:103], v233, s[48:51], s70 offen offset:0 sc1\n\t"
  "s_waitcnt vmcnt(14) lgkmcnt(0)\n\t"
  "s_barrier\n\t"
  "v_add_u32 v176, 0, v187\n\t"
  "v_add_u32 v178, 0, v188\n\t"
  "v_mfma_f32_32x32x16_bf16 v[0:15], v[168:171], v[144:147], v[0:15]\n\t"
  "ds_read_b64_tr_b16 v[160:161], v178 offset:0\n\t"
  "ds_read_b64_tr_b16 v[162:163], v178 offset:256\n\t"
  "v_mfma_f32_32x32x16_bf16 v[16:31], v[172:175], v[144:147], v[16:31]\n\t"
  "ds_read_b128 v[128:131], v176 offset:0\n\t"
  "ds_read_b64_tr_b16 v[164:165], v178 offset:512\n\t"
  "v_mfma_f32_32x32x16_bf16 v[32:47], v[168:171], v[148:151], v[32:47]\n\t"
  "ds_read_b64_tr_b16 v[166:167], v178 offset:768\n\t"
  "ds_read_b128 v[132:135], v176 offset:16384\n\t"
  "v_mfma_f32_32x32x16_bf16 v[48:63], v[172:175], v[148:151], v[48:63]\n\t"
  "buffer_store_dwordx4 v[104:107], v232, s[48:51], s70 offen offset:1024 sc1\n\t"
  "s_waitcnt lgkmcnt(3)\n\t"
  "v_mfma_f32_32x32x16_bf16 v[0:15], v[160:163], v[128:131], v[0:15]\n\t"
  "ds_read_b64_tr_b16 v[168:169], v178 offset:16384\n\t"
  "ds_read_b64_tr_b16 v[170:171], v178 offset:16640\n\t"
  "s_waitcnt lgkmcnt(3)\n\t"
  "v_mfma_f32_32x32x16_bf16 v[16:31], v[164:167], v[128:131], v[16:31]\n\t"
  "ds_read_b128 v[144:147], v176 offset:1024\n\t"
  "ds_read_b64_tr_b16 v[172:173], v178 offset:16896\n\t"
  "s_waitcnt lgkmcnt(4)\n\t"
  "v_mfma_f32_32x32x16_bf16 v[32:47], v[160:163], v[132:135], v[32:47]\n\t"
  "ds_read_b64_tr_b16 v[174:175], v178 offset:17152\n\t"
  "ds_read_b128 v[148:151], v176 offset:17408\n\t"
  "v_mfma_f32_32x32x16_bf16 v[48:63], v[164:167], v[132:135], v[48:63]\n\t"
  "buffer_store_dwordx4 v[108:111], v233, s[48:51], s70 offen offset:1024 sc1\n\t"
  "s_waitcnt vmcnt(10) lgkmcnt(0)\n\t"
  "s_barrier\n\t"
  "v_add_u32 v176, 32768, v187\n\t"
  "v_add_u32 v178, 32768, v188\n\t"
  "v_mfma_f32_32x32x16_bf16 v[0:15], v[168:171], v[144:147], v[0:15]\n\t"
  "ds_read_b64_tr_b16 v[160:161], v178 offset:0\n\t"
  "ds_read_b64_tr_b16 v[162:163], v178 offset:256\n\t"
  "v_mfma_f32_32x32x16_bf16 v[16:31], v[172:175], v[144:147], v[16:31]\n\t"
  "ds_read_b128 v[128:131], v176 offset:0\n\t"
  "ds_read_b64_tr_b16 v[164:165], v178 offset:512\n\t"
  "v_mfma_f32_32x32x16_bf16 v[32:47], v[168:171], v[148:151], v[32:47]\n\t"
  "ds_read_b64_tr_b16 v[166:167], v178 offset:768\n\t"
  "ds_read_b128 v[132:135], v176 offset:16384\n\t"
  "v_mfma_f32_32x32x16_bf16 v[48:63], v[172:175], v[148:151], v[48:63]\n\t"
  "buffer_store_dwordx4 v[112:115], v234, s[48:51], s70 offen offset:0 sc1\n\t"
  "s_waitcnt lgkmcnt(3)\n\t"
  "v_mfma_f32_32x32x16_bf16 v[0:15], v[160:163], v[128:131], v[0:15]\n\t"
  "ds_read_b64_tr_b16 v[168:169], v178 offset:16384\n\t"
  "ds_read_b64_tr_b16 v[170:171], v178 offset:16640\n\t"
  "s_waitcnt lgkmcnt(3)\n\t"
  "v_mfma_f32_32x32x16_bf16 v[16:31], v[164:167], v[128:131], v[16:31]\n\t"
  "ds_read_b128 v[144:147], v176 offset:1024\n\t"
  "ds_read_b64_tr_b16 v[172:173], v178 offset:16896\n\t"
  "s_waitcnt lgkmcnt(4)\n\t"
  "v_mfma_f32_32x32x16_bf16 v[32:47], v[160:163], v[132:135], v[32:47]\n\t"
  "ds_read_b64_tr_b16 v[174:175], v178 offset:17152\n\t"
  "ds_read_b128 v[148:151], v176 offset:17408\n\t"
  "v_mfma_f32_32x32x16_bf16 v[48:63], v[164:167], v[132:135], v[48:63]\n\t"
  "buffer_store_dwordx4 v[116:119], v235, s[48:51], s70 offen offset:0 sc1\n\t"
  "s_waitcnt vmcnt(6) lgkmcnt(0)\n\t"
  "s_barrier\n\t"
  "v_add_u32 v176, 65536, v187\n\t"
  "v_add_u32 v178, 65536, v188\n\t"
  "v_mfma_f32_32x32x16_bf16 v[0:15], v[168:171], v[144:147], v[0:15]\n\t"
  "ds_read_b64_tr_b16 v[160:161], v178 offset:0\n\t"
  "ds_read_b64_tr_b16 v[162:163], v178 offset:256\n\t"
  "v_mfma_f32_32x32x16_bf16 v[16:31], v[172:175], v[144:147], v[16:31]\n\t"
  "ds_read_b128 v[128:131], v176 offset:0\n\t"
  "ds_read_b64_tr_b16 v[164:165], v178 offset:512\n\t"
  "v_mfma_f32_32x32x16_bf16 v[32:47], v[168:171], v[148:151], v[32:47]\n\t"
  "ds_read_b64_tr_b16 v[166:167], v178 offset:768\n\t"
  "ds_read_b128 v[132:135], v176 offset:16384\n\t"
  "v_mfma_f32_32x32x16_bf16 v[48:63], v[172:175], v[148:151], v[48:63]\n\t"
  "buffer_store_dwordx4 v[120:123], v234, s[48:51], s70 offen offset:1024 sc1\n\t"
  "s_waitcnt lgkmcnt(3)\n\t"
  "v_mfma_f32_32x32x16_bf16 v[0:15], v[160:163], v[128:131], v[0:15]\n\t"
  "ds_read_b64_tr_b16 v[168:169], v178 offset:16384\n\t"
  "ds_read_b64_tr_b16 v[170:171], v178 offset:16640\n\t"
  "s_waitcnt lgkmcnt(3)\n\t"
  "v_mfma_f32_32x32x16_bf16 v[16:31], v[164:167], v[128:131], v[16:31]\n\t"
  "ds_read_b128 v[144:147], v176 offset:1024\n\t"
  "ds_read_b64_tr_b16 v[172:173], v178 offset:16896\n\t"
  "s_waitcnt lgkmcnt(4)\n\t"
  "v_mfma_f32_32x32x16_bf16 v[32:47], v[160:163], v[132:135], v[32:47]\n\t"
  "ds_read_b64_tr_b16 v[174:175], v178 offset:17152\n\t"
  "ds_read_b128 v[148:151], v176 offset:17408\n\t"
  "v_mfma_f32_32x32x16_bf16 v[48:63], v[164:167], v[132:135], v[48:63]\n\t"
  "buffer_store_dwordx4 v[124:127], v235, s[48:51], s70 offen offset:1024 sc1\n\t"
  "s_waitcnt lgkmcnt(0)\n\t"
  "s_barrier\n\t"
  "v_add_u32 v176, 98304, v187\n\t"
  "v_add_u32 v178, 98304, v188\n\t"
  "v_mfma_f32_32x32x16_bf16 v[0:15], v[168:171], v[144:147], v[0:15]\n\t"
  "ds_read_b64_tr_b16 v[160:161], v178 offset:0\n\t"
  "ds_read_b64_tr_b16 v[162:163], v178 offset:256\n\t"
  "v_mfma_f32_32x32x16_bf16 v[16:31], v[172:175], v[144:147], v[16:31]\n\t"
  "ds_read_b128 v[128:131], v176 offset:0\n\t"
  "ds_read_b64_tr_b16 v[164:165], v178 offset:512\n\t"
  "v_mfma_f32_32x32x16_bf16 v[32:47], v[168:171], v[148:151], v[32:47]\n\t"
  "ds_read_b64_tr_b16 v[166:167], v178 offset:768\n\t"
  "ds_read_b128 v[132:135], v176 offset:16384\n\t"
  "v_mfma_f32_32x32x16_bf16 v[48:63], v[172:175], v[148:151], v[48:63]\n\t"
  "buffer_store_dwordx4 v[64:67], v232, s[48:51], s68 offen offset:0 sc1\n\t"
  "s_waitcnt lgkmcnt(3)\n\t"
  "v_mfma_f32_32x32x16_bf16 v[0:15], v[160:163], v[128:131], v[0:15]\n\t"
  "ds_read_b64_tr_b16 v[168:169], v178 offset:16384\n\t"
  "ds_read_b64_tr_b16 v[170:171], v178 offset:16640\n\t"
  "s_waitcnt lgkmcnt(3)\n\t"
  "v_mfma_f32_32x32x16_bf16 v[16:31], v[164:167], v[128:131], v[16:31]\n\t"
  "ds_read_b128 v[144:147], v176 offset:1024\n\t"
  "ds_read_b64_tr_b16 v[172:173], v178 offset:16896\n\t"
  "s_waitcnt lgkmcnt(4)\n\t"
  "v_mfma_f32_32x32x16_bf16 v[32:47], v[160:163], v[132:135], v[32:47]\n\t"
  "ds_read_b64_tr_b16 v[174:175], v178 offset:17152\n\t"
  "ds_read_b128 v[148:151], v176 offset:17408\n\t"
  "v_mfma_f32_32x32x16_bf16 v[48:63], v[164:167], v[132:135], v[48:63]\n\t"
  "buffer_store_dwordx4 v[68:71], v233, s[48:51], s68 offen offset:0 sc1\n\t"
  "s_waitcnt lgkmcnt(0)\n\t"
  "s_barrier\n\t"
  "v_add_u32 v176, 131072, v187\n\t"
  "v_add_u32 v178, 131072, v188\n\t"
  "v_mfma_f32_32x32x16_bf16 v[0:15], v[168:171], v[144:147], v[0:15]\n\t"
  "ds_read_b64_tr_b16 v[160:161], v178 offset:0\n\t"
  "ds_read_b64_tr_b16 v[162:163], v178 offset:256\n\t"
  "v_mfma_f32_32x32x16_bf16 v[16:31], v[172:175], v[144:147], v[16:31]\n\t"
  "ds_read_b128 v[128:131], v176 offset:0\n\t"
  "ds_read_b64_tr_b16 v[164:165], v178 offset:512\n\t"
  "v_mfma_f32_32x32x16_bf16 v[32:47], v[168:171], v[148:151], v[32:47]\n\t"
  "ds_read_b64_tr_b16 v[166:167], v178 offset:768\n\t"
  "ds_read_b128 v[132:135], v176 offset:16384\n\t"
  "v_mfma_f32_32x32x16_bf16 v[48:63], v[172:175], v[148:151], v[48:63]\n\t"
  "buffer_store_dwordx4 v[72:75], v232, s[48:51], s68 offen offset:1024 sc1\n\t"
  "s_waitcnt lgkmcnt(3)\n\t"
  "v_mfma_f32_32x32x16_bf16 v[0:15], v[160:163], v[128:131], v[0:15]\n\t"
  "ds_read_b64_tr_b16 v[168:169], v178 offset:16384\n\t"
  "ds_read_b64_tr_b16 v[170:171], v178 offset:16640\n\t"
  "s_waitcnt lgkmcnt(3)\n\t"
  "v_mfma_f32_32x32x16_bf16 v[16:31], v[164:167], v[128:131], v[16:31]\n\t"
  "ds_read_b128 v[144:147], v176 offset:1024\n\t"
  "ds_read_b64_tr_b16 v[172:173], v178 offset:16896\n\t"
  "s_waitcnt lgkmcnt(4)\n\t"
  "v_mfma_f32_32x32x16_bf16 v[32:47], v[160:163], v[132:135], v[32:47]\n\t"
  "ds_read_b64_tr_b16 v[174:175], v178 offset:17152\n\t"
  "ds_read_b128 v[148:151], v176 offset:17408\n\t"
  "v_mfma_f32_32x32x16_bf16 v[48:63], v[164:167], v[132:135], v[48:63]\n\t"
  "buffer_store_dwordx4 v[76:79], v233, s[48:51], s68 offen offset:1024 sc1\n\t"
  "s_waitcnt lgkmcnt(0)\n\t"
  "s_barrier\n\t"
  "v_add_u32 v176, 0, v187\n\t"
  "v_add_u32 v178, 0, v188\n\t"
  "v_mfma_f32_32x32x16_bf16 v[0:15], v[168:171], v[144:147], v[0:15]\n\t"
  "ds_read_b64_tr_b16 v[160:161], v178 offset:0\n\t"
  "ds_read_b64_tr_b16 v[162:163], v178 offset:256\n\t"
  "v_mfma_f32_32x32x16_bf16 v[16:31], v[172:175], v[144:147], v[16:31]\n\t"
  "ds_read_b128 v[128:131], v176 offset:0\n\t"
  "ds_read_b64_tr_b16 v[164:165], v178 offset:512\n\t"
  "v_mfma_f32_32x32x16_bf16 v[32:47], v[168:171], v[148:151], v[32:47]\n\t"
  "ds_read_b64_tr_b16 v[166:167], v178 offset:768\n\t"
  "ds_read_b128 v[132:135], v176 offset:16384\n\t"
  "v_mfma_f32_32x32x16_bf16 v[48:63], v[172:175], v[148:151], v[48:63]\n\t"
  "buffer_store_dwordx4 v[80:83], v234, s[48:51], s68 offen offset:0 sc1\n\t"
  "s_waitcnt lgkmcnt(3)\n\t"
  "v_mfma_f32_32x32x16_bf16 v[0:15], v[160:163], v[128:131], v[0:15]\n\t"
  "ds_read_b64_tr_b16 v[168:169], v178 offset:16384\n\t"
  "ds_read_b64_tr_b16 v[170:171], v178 offset:16640\n\t"
  "s_waitcnt lgkmcnt(3)\n\t"
  "v_mfma_f32_32x32x16_bf16 v[16:31], v[164:167], v[128:131], v[16:31]\n\t"
  "ds_read_b128 v[144:147], v176 offset:1024\n\t"
  "ds_read_b64_tr_b16 v[172:173], v178 offset:16896\n\t"
  "s_waitcnt lgkmcnt(4)\n\t"
  "v_mfma_f32_32x32x16_bf16 v[32:47], v[160:163], v[132:135], v[32:47]\n\t"
  "ds_read_b64_tr_b16 v[174:175], v178 offset:17152\n\t"
  "ds_read_b128 v[148:151], v176 offset:17408\n\t"
  "v_mfma_f32_32x32x16_bf16 v[48:63], v[164:167], v[132:135], v[48:63]\n\t"
  "buffer_store_dwordx4 v[84:87], v235, s[48:51], s68 offen offset:0 sc1\n\t"
  "s_waitcnt lgkmcnt(0)\n\t"
  "s_barrier\n\t"
  "v_add_u32 v176, 32768, v187\n\t"
  "v_add_u32 v178, 32768, v188\n\t"
  "v_mfma_f32_32x32x16_bf16 v[0:15], v[168:171], v[144:147], v[0:15]\n\t"
  "ds_read_b64_tr_b16 v[160:161], v178 offset:0\n\t"
  "ds_read_b64_tr_b16 v[162:163], v178 offset:256\n\t"
  "v_mfma_f32_32x32x16_bf16 v[16:31], v[172:175], v[144:147], v[16:31]\n\t"
  "ds_read_b128 v[128:131], v176 offset:0\n\t"
  "ds_read_b64_tr_b16 v[164:165], v178 offset:512\n\t"
  "v_mfma_f32_32x32x16_bf16 v[32:47], v[168:171], v[148:151], v[32:47]\n\t"
  "ds_read_b64_tr_b16 v[166:167], v178 offset:768\n\t"
  "ds_read_b128 v[132:135], v176 offset:16384\n\t"
  "v_mfma_f32_32x32x16_bf16 v[48:63], v[172:175], v[148:151], v[48:63]\n\t"
  "buffer_store_dwordx4 v[88:91], v234, s[48:51], s68 offen offset:1024 sc1\n\t"
  "s_waitcnt lgkmcnt(3)\n\t"
  "v_mfma_f32_32x32x16_bf16 v[0:15], v[160:163], v[128:131], v[0:15]\n\t"
  "ds_read_b64_tr_b16 v[168:169], v178 offset:16384\n\t"
  "ds_read_b64_tr_b16 v[170:171], v178 offset:16640\n\t"
  "s_waitcnt lgkmcnt(3)\n\t"
  "v_mfma_f32_32x32x16_bf16 v[16:31], v[164:167], v[128:131], v[16:31]\n\t"
  "ds_read_b128 v[144:147], v176 offset:1024\n\t"
  "ds_read_b64_tr_b16 v[172:173], v178 offset:16896\n\t"
  "s_waitcnt lgkmcnt(4)\n\t"
  "v_mfma_f32_32x32x16_bf16 v[32:47], v[160:163], v[132:135], v[32:47]\n\t"
  "ds_read_b64_tr_b16 v[174:175], v178 offset:17152\n\t"
  "ds_read_b128 v[148:151], v176 offset:17408\n\t"
  "v_mfma_f32_32x32x16_bf16 v[48:63], v[164:167], v[132:135], v[48:63]\n\t"
  "buffer_store_dwordx4 v[92:95], v235, s[48:51], s68 offen offset:1024 sc1\n\t"
  "s_waitcnt lgkmcnt(0)\n\t"
  "s_barrier\n\t"
  "v_add_u32 v176, 65536, v187\n\t"
  "v_add_u32 v178, 65536, v188\n\t"
  "v_mfma_f32_32x32x16_bf16 v[0:15], v[168:171], v[144:147], v[0:15]\n\t"
  "ds_read_b64_tr_b16 v[160:161], v178 offset:0\n\t"
  "ds_read_b64_tr_b16 v[162:163], v178 offset:256\n\t"
  "v_mfma_f32_32x32x16_bf16 v[16:31], v[172:175], v[144:147], v[16:31]\n\t"
  "ds_read_b128 v[128:131], v176 offset:0\n\t"
  "ds_read_b64_tr_b16 v[164:165], v178 offset:512\n\t"
  "v_mfma_f32_32x32x16_bf16 v[32:47], v[168:171], v[148:151], v[32:47]\n\t"
  "ds_read_b64_tr_b16 v[166:167], v178 offset:768\n\t"
  "ds_read_b128 v[132:135], v176 offset:16384\n\t"
  "v_mfma_f32_32x32x16_bf16 v[48:63], v[172:175], v[148:151], v[48:63]\n\t"
  "s_waitcnt lgkmcnt(3)\n\t"
  "v_mfma_f32_32x32x16_bf16 v[0:15], v[160:163], v[128:131], v[0:15]\n\t"
  "ds_read_b64_tr_b16 v[168:169], v178 offset:16384\n\t"
  "ds_read_b64_tr_b16 v[170:171], v178 offset:16640\n\t"
  "s_waitcnt lgkmcnt(3)\n\t"
  "v_mfma_f32_32x32x16_bf16 v[16:31], v[164:167], v[128:131], v[16:31]\n\t"
  "ds_read_b128 v[144:147], v176 offset:1024\n\t"
  "ds_read_b64_tr_b16 v[172:173], v178 offset:16896\n\t"
  "s_waitcnt lgkmcnt(4)\n\t"
  "v_mfma_f32_32x32x16_bf16 v[32:47], v[160:163], v[132:135], v[32:47]\n\t"
  "ds_read_b64_tr_b16 v[174:175], v178 offset:17152\n\t"
  "ds_read_b128 v[148:151], v176 offset:17408\n\t"
  "v_mfma_f32_32x32x16_bf16 v[48:63], v[164:167], v[132:135], v[48:63]\n\t"
  "s_waitcnt lgkmcnt(0)\n\t"
  "s_barrier\n\t"
  "v_add_u32 v176, 98304, v187\n\t"
  "v_add_u32 v178, 98304, v188\n\t"
  "v_mfma_f32_32x32x16_bf16 v[0:15], v[168:171], v[144:147], v[0:15]\n\t"
  "ds_read_b64_tr_b16 v[160:161], v178 offset:0\n\t"
  "ds_read_b64_tr_b16 v[162:163], v178 offset:256\n\t"
  "v_mfma_f32_32x32x16_bf16 v[16:31], v[172:175], v[144:147], v[16:31]\n\t"
  "ds_read_b128 v[128:131], v176 offset:0\n\t"
  "ds_read_b64_tr_b16 v[164:165], v178 offset:512\n\t"
  "v_mfma_f32_32x32x16_bf16 v[32:47], v[168:171], v[148:151], v[32:47]\n\t"
  "ds_read_b64_tr_b16 v[166:167], v178 offset:768\n\t"
  "ds_read_b128 v[132:135], v176 offset:16384\n\t"
  "v_mfma_f32_32x32x16_bf16 v[48:63], v[172:175], v[148:151], v[48:63]\n\t"
  "s_waitcnt lgkmcnt(3)\n\t"
  "v_mfma_f32_32x32x16_bf16 v[0:15], v[160:163], v[128:131], v[0:15]\n\t"
  "ds_read_b64_tr_b16 v[168:169], v178 offset:16384\n\t"
  "ds_read_b64_tr_b16 v[170:171], v178 offset:16640\n\t"
  "s_waitcnt lgkmcnt(3)\n\t"
  "v_mfma_f32_32x32x16_bf16 v[16:31], v[164:167], v[128:131], v[16:31]\n\t"
  "ds_read_b128 v[144:147], v176 offset:1024\n\t"
  "ds_read_b64_tr_b16 v[172:173], v178 offset:16896\n\t"
  "s_waitcnt lgkmcnt(4)\n\t"
  "v_mfma_f32_32x32x16_bf16 v[32:47], v[160:163], v[132:135], v[32:47]\n\t"
  "ds_read_b64_tr_b16 v[174:175], v178 offset:17152\n\t"
  "ds_read_b128 v[148:151], v176 offset:17408\n\t"
  "v_mfma_f32_32x32x16_bf16 v[48:63], v[164:167], v[132:135], v[48:63]\n\t"
  "s_waitcnt vmcnt(0)\n\t"
  "s_waitcnt lgkmcnt(0)\n\t"
  "s_barrier\n\t"
  "v_add_u32 v176, 131072, v187\n\t"
  "v_add_u32 v178, 131072, v188\n\t"
  "v_mfma_f32_32x32x16_bf16 v[0:15], v[168:171], v[144:147], v[0:15]\n\t"
  "ds_read_b64_tr_b16 v[160:161], v178 offset:0\n\t"
  "ds_read_b64_tr_b16 v[162:163], v178 offset:256\n\t"
  "v_mfma_f32_32x32x16_bf16 v[16:31], v[172:175], v[144:147], v[16:31]\n\t"
  "ds_read_b128 v[128:131], v176 offset:0\n\t"
  "ds_read_b64_tr_b16 v[164:165], v178 offset:512\n\t"
  "v_mfma_f32_32x32x16_bf16 v[32:47], v[168:171], v[148:151], v[32:47]\n\t"
  "ds_read_b64_tr_b16 v[166:167], v178 offset:768\n\t"
  "ds_read_b128 v[132:135], v176 offset:16384\n\t"
  "v_mfma_f32_32x32x16_bf16 v[48:63], v[172:175], v[148:151], v[48:63]\n\t"
  "s_waitcnt lgkmcnt(3)\n\t"
  "v_mfma_f32_32x32x16_bf16 v[0:15], v[160:163], v[128:131], v[0:15]\n\t"
  "ds_read_b64_tr_b16 v[168:169], v178 offset:16384\n\t"
  "ds_read_b64_tr_b16 v[170:171], v178 offset:16640\n\t"
  "s_waitcnt lgkmcnt(3)\n\t"
  "v_mfma_f32_32x32x16_bf16 v[16:31], v[164:167], v[128:131], v[16:31]\n\t"
  "ds_read_b128 v[144:147], v176 offset:1024\n\t"
  "ds_read_b64_tr_b16 v[172:173], v178 offset:16896\n\t"
  "s_waitcnt lgkmcnt(4)\n\t"
  "v_mfma_f32_32x32x16_bf16 v[32:47], v[160:163], v[132:135], v[32:47]\n\t"
  "ds_read_b64_tr_b16 v[174:175], v178 offset:17152\n\t"
  "ds_read_b128 v[148:151], v176 offset:17408\n\t"
  "v_mfma_f32_32x32x16_bf16 v[48:63], v[164:167], v[132:135], v[48:63]\n\t"
  "s_waitcnt lgkmcnt(0)\n\t"
  "s_barrier\n\t"
  "v_mfma_f32_32x32x16_bf16 v[0:15], v[168:171], v[144:147], v[0:15]\n\t"
  "v_mfma_f32_32x32x16_bf16 v[16:31], v[172:175], v[144:147], v[16:31]\n\t"
  "v_mfma_f32_32x32x16_bf16 v[32:47], v[168:171], v[148:151], v[32:47]\n\t"
  "v_mfma_f32_32x32x16_bf16 v[48:63], v[172:175], v[148:151], v[48:63]\n\t"
  "s_and_b32 s83, s81, 0xffffff80\n\t"
  "v_lshrrev_b32 v240, 2, v186\n\t"
  "v_and_b32 v240, 28, v240\n\t"
  "buffer_load_dword v241, v240, s[60:63], s83 offen sc1\n\t"
  "LFq_%=:\n\t"
  "v_add_u32 v176, s74, v186\n\t"
  "s_nop 7\n\t"
  "v_cvt_pk_bf16_f32 v192, v0, v1\n\t"
  "v_cvt_pk_bf16_f32 v193, v2, v3\n\t"
  "v_cvt_pk_bf16_f32 v194, v4, v5\n\t"
  "v_cvt_pk_bf16_f32 v195, v6, v7\n\t"
  "ds_write_b128 v176, v[192:195] offset:0\n\t"
  "v_cvt_pk_bf16_f32 v196, v8, v9\n\t"
  "v_cvt_pk_bf16_f32 v197, v10, v11\n\t"
  "v_cvt_pk_bf16_f32 v198, v12, v13\n\t"
  "v_cvt_pk_bf16_f32 v199, v14, v15\n\t"
  "ds_write_b128 v176, v[196:199] offset:1024\n\t"
  "v_cvt_pk_bf16_f32 v200, v16, v17\n\t"
  "v_cvt_pk_bf16_f32 v201, v18, v19\n\t"
  "v_cvt_pk_bf16_f32 v202, v20, v21\n\t"
  "v_cvt_pk_bf16_f32 v203, v22, v23\n\t"
  "ds_write_b128 v176, v[200:203] offset:2048\n\t"
  "v_cvt_pk_bf16_f32 v204, v24, v25\n\t"
  "v_cvt_pk_bf16_f32 v205, v26, v27\n\t"
  "v_cvt_pk_bf16_f32 v206, v28, v29\n\t"
  "v_cvt_pk_bf16_f32 v207, v30, v31\n\t"
  "ds_write_b128 v176, v[204:207] offset:3072\n\t"
  "v_cvt_pk_bf16_f32 v208, v32, v33\n\t"
  "v_cvt_pk_bf16_f32 v209, v34, v35\n\t"
  "v_cvt_pk_bf16_f32 v210, v36, v37\n\t"
  "v_cvt_pk_bf16_f32 v211, v38, v39\n\t"
  "ds_write_b128 v176, v[208:211] offset:32768\n\t"
  "v_cvt_pk_bf16_f32 v212, v40, v41\n\t"
  "v_cvt_pk_bf16_f32 v213, v42, v43\n\t"
  "v_cvt_pk_bf16_f32 v214, v44, v45\n\t"
  "v_cvt_pk_bf16_f32 v215, v46, v47\n\t"
  "ds_write_b128 v176, v[212:215] offset:33792\n\t"
  "v_cvt_pk_bf16_f32 v216, v48, v49\n\t"
  "v_cvt_pk_bf16_f32 v217, v50, v51\n\t"
  "v_cvt_pk_bf16_f32 v218, v52, v53\n\t"
  "v_cvt_pk_bf16_f32 v219, v54, v55\n\t"
  "ds_write_b128 v176, v[216:219] offset:34816\n\t"
  "v_cvt_pk_bf16_f32 v220, v56, v57\n\t"
  "v_cvt_pk_bf16_f32 v221, v58, v59\n\t"
  "v_cvt_pk_bf16_f32 v222, v60, v61\n\t"
  "v_cvt_pk_bf16_f32 v223, v62, v63\n\t"
  "ds_write_b128 v176, v[220:223] offset:35840\n\t"
  "s_waitcnt vmcnt(0) lgkmcnt(0)\n\t"
  "s_barrier\n\t"
  "LFkvc_%=:\n\t"
  "v_cmp_ne_u32 vcc, s77, v241\n\t"
  "s_cbranch_vccnz LFkvs_%=\n\t"
  "LFkvo_%=:\n\t"
  "s_add_u32 m0, s66, 0\n\t"
  "s_add_u32 s73, s38, 0x1000000\n\t"
  "buffer_load_dwordx4 v186, s[48:51], s38 offen offset:0 sc1 lds\n\t"
  "buffer_load_dwordx4 v186, s[48:51], s38 offen offset:1024 sc1 lds\n\t"
  "buffer_load_dwordx4 v186, s[48:51], s38 offen offset:2048 sc1 lds\n\t"
  "buffer_load_dwordx4 v186, s[48:51], s38 offen offset:3072 sc1 lds\n\t"
  "s_branch LFend_%=\n\t"
  "LFkvs_%=:\n\t"
  "buffer_load_dword v241, v240, s[60:63], s83 offen sc1\n\t"
  "s_waitcnt vmcnt(0)\n\t"
  "s_sleep 2\n\t"
  "s_add_u32 s82, s82, 1\n\t"
  "s_cmp_lt_u32 s82, 131072\n\t"
  "s_cbranch_scc1 LFkvc_%=\n\t"
  "s_branch LFkvo_%=\n\t"
  "LFgsk_%=:\n\t"
  "buffer_load_dwordx4 v[244:247], v190, s[60:63], 0 offen sc1\n\t"
  "s_waitcnt vmcnt(0)\n\t"
  "s_sleep 2\n\t"
  "s_add_u32 s82, s82, 1\n\t"
  "s_cmp_lt_u32 s82, 131072\n\t"
  "s_cbranch_scc1 LFgck_%=\n\t"
  "s_branch LFgok_%=\n\t"
  "LFhsk_%=:\n\t"
  "buffer_load_dword v192, v240, s[60:63], s83 offen sc1\n\t"
  "s_waitcnt vmcnt(0)\n\t"
  "s_sleep 2\n\t"
  "s_add_u32 s82, s82, 1\n\t"
  "s_cmp_lt_u32 s82, 131072\n\t"
  "s_cbranch_scc1 LFhck_%=\n\t"
  "s_branch LFhok_%=\n\t"
  "LFgsv_%=:\n\t"
  "buffer_load_dwordx4 v[244:247], v190, s[60:63], 0 offen sc1\n\t"
  "s_waitcnt vmcnt(0)\n\t"
  "s_sleep 2\n\t"
  "s_add_u32 s82, s82, 1\n\t"
  "s_cmp_lt_u32 s82, 131072\n\t"
  "s_cbranch_scc1 LFgcv_%=\n\t"
  "s_branch LFgov_%=\n\t"
  "LFhsv_%=:\n\t"
  "buffer_load_dword v192, v240, s[60:63], s83 offen sc1\n\t"
  "s_waitcnt vmcnt(0)\n\t"
  "s_sleep 2\n\t"
  "s_add_u32 s82, s82, 1\n\t"
  "s_cmp_lt_u32 s82, 131072\n\t"
  "s_cbranch_scc1 LFhcv_%=\n\t"
  "s_branch LFhov_%=\n\t"
  "LFend_%=:\n\t"
  : "+{s64}"(coff), "+{s65}"(woff), "+{v[224:231]}"(y)
  : "{v179}"(b0), "{v180}"(b1), "{v181}"(bw), "{v182}"(vc0), "{v183}"(vc1), "{v184}"(vw0), "{v185}"(vw1), "{v186}"(doff), "{v187}"(qb0), "{v188}"(qbw),
    "{v189}"(vqw), "{v190}"(vg), "{v191}"(vh), "{v249}"(vh1),
    "{s[40:43]}"(sck), "{s[44:47]}"(sw), "{s[48:51]}"(so), "{s[52:55]}"(sh), "{s[56:59]}"(swq), "{s[60:63]}"(sf), "{s66}"(sbase), "{s69}"(uw),
    "{s70}"(ooff), "{s71}"(hoff), "{s72}"(qoff), "{s74}"(qdst), "{s75}"(hofl), "{s77}"(tag), "{s78}"(hoff2), "{s79}"(qoff2), "{s80}"(hfl),
    "{s81}"(kfl), "{s38}"(ak0), "{s37}"(ak1), "{s39}"(pfb)
  : "v0", "v1", "v2", "v3", "v4", "v5", "v6", "v7", "v8", "v9", "v10", "v11", "v12", "v13", "v14", "v15", "v16", "v17", "v18", "v19", "v20", "v21", "v22", "v23", "v24", "v25", "v26", "v27", "v28", "v29", "v30", "v31", "v32", "v33", "v34", "v35", "v36", "v37", "v38", "v39", "v40", "v41", "v42", "v43", "v44", "v45", "v46", "v47", "v48", "v49", "v50", "v51", "v52", "v53", "v54", "v55", "v56", "v57", "v58", "v59", "v60", "v61", "v62", "v63", "v64", "v65", "v66", "v67", "v68", "v69", "v70", "v71", "v72", "v73", "v74", "v75", "v76", "v77", "v78", "v79", "v80", "v81", "v82", "v83", "v84", "v85", "v86", "v87", "v88", "v89", "v90", "v91", "v92", "v93", "v94", "v95", "v96", "v97", "v98", "v99", "v100", "v101", "v102", "v103", "v104", "v105", "v106", "v107", "v108", "v109", "v110", "v111", "v112", "v113", "v114", "v115", "v116", "v117", "v118", "v119", "v120", "v121", "v122", "v123", "v124", "v125", "v126", "v127", "v128", "v129", "v130", "v131", "v132", "v133", "v134", "v135", "v136", "v137", "v138", "v139", "v140", "v141", "v142", "v143", "v144", "v145", "v146", "v147", "v148", "v149", "v150", "v151", "v152", "v153", "v154", "v155", "v156", "v157", "v158", "v159", "v160", "v161", "v162", "v163", "v164", "v165", "v166", "v167", "v168", "v169", "v170", "v171", "v172", "v173", "v174", "v175", "v176", "v177", "v178", "v192", "v193", "v194", "v195", "v196", "v197", "v198", "v199", "v200", "v201", "v202", "v203", "v204", "v205", "v206", "v207", "v208", "v209", "v210", "v211", "v212", "v213", "v214", "v215", "v216", "v217", "v218", "v219", "v220", "v221", "v222", "v223", "v232", "v233", "v234", "v235", "v236", "v237", "v238", "v239", "v240", "v241", "v242", "v243", "v244", "v245", "v246", "v247", "v248", "v250", "v251", "s67", "s68", "s73", "s76", "s82", "s83", "vcc", "scc", "memory");
}

DI void attn_phase(const M1Args& A, unsigned tag, int xcd, int nt, int xq, int wid, int lane) {
  char* ws = A.ws;
  const int h = xcd * 4 + nt, bid = blockIdx.x;
  const int hh = lane >> 5, l32 = lane & 31;
  const int sp = wid >> 2, g = sp ? 7 - wid : wid;
  u32x16 q0, q1;
#pragma unroll
  for (int d = 0; d < 4; ++d) {
    const u32x4 a = *(const LAS u32x4*)(uintptr_t)(98304 + g * 8192 + d * 1024 + lane * 16);
    const u32x4 b = *(const LAS u32x4*)(uintptr_t)(98304 + g * 8192 + (d + 4) * 1024 + lane * 16);
#pragma unroll
    for (int e = 0; e < 4; ++e) { q0[4 * d + e] = a[e]; q1[4 * d + e] = b[e]; }
  }
  unsigned kaddr = sp * 16384 + lane * 16, vaddr = kaddr + 65536, qbaddr = 131072 + g * 8192 + lane * 16;
  unsigned doff = lane * 16, ones = (((lane >> 2) & 1) == ((lane >> 4) & 1)) ? 0x3f803f80u : 0u;
  int lim = 32 * g + l32 - 64 * sp - 4 * hh;
  u32x4 sk = mk_srd4(ws + WS_KF, NH * SEQ * HD * 4), sc = mk_srd4(ws + WS_CTX, SEQ * 4096 * 2);
  unsigned na = xq + 1, sbase = wid * 4096, odst = sp * 2 * 26624 + (4 + g) * 2048, kb = h * 524288 + wid * 4096;
  unsigned ca = ((xq * 4 + g) * 256 + h * 8 + sp * 4) * 1024, cb = (((15 - xq) * 4 + g) * 256 + h * 8 + sp * 4) * 1024;
  u32x2 pfa;
  {
    const int m = wid < 6 ? wid - 3 : min(bid / 14, 2);
    const char* fb_ = (const char*)(m == 0 ? A.wg : (m == 1 ? A.wu : A.wd));
    const char* base = wid < 3 ? (const char*)A.wo : fb_;
    const int l0 = wid < 3 ? (bid & 7) * 5120 + (bid >> 3) * 160 + wid * 64 : (wid < 6 ? bid * 64 : 16384 + (bid % 14) * 64);
    const char* pp = base + (size_t)(l0 + (wid == 2 ? l32 : (wid == 7 ? 0 : lane))) * 128;
    pfa = u32x2{(unsigned)(uintptr_t)pp, (unsigned)((uintptr_t)pp >> 32)};
  }
  const int wm = wid >> 1, wn = wid & 1, g4 = lane >> 4, q16 = lane & 15;
  unsigned ba = (2 * wm) * 2048 + (g4 >> 1) * 1024 + ((g4 & 1) * 32 + q16) * 16;
  const unsigned bw = 16384 + (g4 >> 1) * 5120 + (4 * (g4 & 1) + (q16 >> 2)) * 64 + (lane & 3) * 8;
  unsigned bw0 = bw + wn * 2048 + (((wn ^ g4) & 1) << 5), bw1 = bw + wn * 3072 + (((wn ^ g4 ^ 1) & 1) << 5);
  auto wsrc = [](int i) { const int pc = i >> 6, r = (i >> 2) & 15, k16 = pc >= 5; return (unsigned)((k16 * 16 + r) * 1280 + (pc - 5 * k16) * 64 + (((i & 3) ^ ((r >> 1) & 2)) << 4)); };
  unsigned vw0 = wsrc(wid * 80 + lane) + 4096, vw1 = wsrc(wid * 80 + 64 + q16) + 4096 - 1024;
  unsigned po = (q16 >> 2) * 81920 + g4 * 64 + (q16 & 3) * 16;
  u32x4 sw = mk_srd4((const char*)A.wo - 4096, 4096 * DM * 2 + 4096);
  u32x4 spt = mk_srd4(ws + WS_PART, 8 * SEQ * DM * 2), sf = mk_srd4(ws + WS_FLG, FL_BYTES);
  const int rbw = ((wid < 4) ? xq : 15 - xq) * 4 + (wid & 3);
  const int rbm = ((wm < 2) ? xq : 15 - xq) * 4 + (wm & 1) * 2;
  const unsigned a0 = (rbw * 256 + xcd * 32) * 1024, w0 = xcd * 512 * 1280 + nt * 320;
  unsigned aoff = a0 + nt * 8192, woff = w0 + nt * 128 * 1280, aend = a0 + 32768, wend = w0 + 512 * 1280;
  unsigned poff = rbm * 4 * 81920 + xcd * 10240 + (nt * 10 + wn * 5) * 256;
  unsigned cfl = FL_C + (xcd * 8 + xq) * 128 + nt * 4, uw = wid;
  const bool up = bid < 54, down = bid >= 54 && bid < 74;
  const unsigned rowb = up ? FF * 2 : DM * 2;
  u32x4 sfw = mk_srd4(up ? (wid < 4 ? A.wg : A.wu) : A.wd, DM * FF * 2);
  unsigned vf = (lane >> 2) * rowb + (lane & 3) * 16;
  unsigned foff = (up || down) ? (up ? (wid & 3) * 160 : wid * 224) * rowb + (up ? bid : bid - 54) * 64 : 0x7f000000u, fj = 16 * rowb - 1024;
  unsigned fi = 48 * rowb;
  asm volatile(
  "s_nop 4\n\t"
  "s_mov_b32 s66, 0x3db504f3\n\t"
  "s_mov_b32 s67, 0x3fb8aa3b\n\t"
  "s_mov_b32 s69, 0xbfb8aa3b\n\t"
  "s_mov_b32 s68, 0x41a00000\n\t"
  "s_mov_b32 s58, 0\n\t"
  "s_mov_b32 s64, 0\n\t"
  "s_mov_b32 s53, 0\n\t"
  "s_add_u32 s45, s44, 0x1000000\n\t"
  "s_mov_b32 s54, s44\n\t"
  "s_mov_b32 s55, s45\n\t"
  "s_lshl_b32 s62, s52, 15\n\t"
  "s_add_u32 s63, s62, s45\n\t"
  "s_add_u32 s62, s62, s44\n\t"
  "s_mov_b32 s56, s59\n\t"
  "s_add_u32 s57, s59, 0x10000\n\t"
  "s_lshr_b32 s65, s59, 3\n\t"
  "s_add_u32 s65, s65, 0x20000\n\t"
  "s_cmp_eq_u32 s52, 1\n\t"
  "s_cselect_b32 s70, 1, 0\n\t"
  "s_mov_b32 m0, s56\n\t"
  "s_xor_b32 s56, s56, 0x8000\n\t"
  "s_add_u32 s54, s54, 0x8000\n\t"
  "s_cmp_eq_u32 s54, s62\n\t"
  "s_cselect_b32 s54, s44, s54\n\t"
  "s_cselect_b32 s62, -1, s62\n\t"
  "s_mov_b32 m0, s56\n\t"
  "s_xor_b32 s56, s56, 0x8000\n\t"
  "buffer_load_dwordx4 v223, s[40:43], s54 offen offset:0 sc1 lds\n\t"
  "buffer_load_dwordx4 v223, s[40:43], s54 offen offset:1024 sc1 lds\n\t"
  "buffer_load_dwordx4 v223, s[40:43], s54 offen offset:2048 sc1 lds\n\t"
  "buffer_load_dwordx4 v223, s[40:43], s54 offen offset:3072 sc1 lds\n\t"
  "s_add_u32 s54, s54, 0x8000\n\t"
  "s_cmp_eq_u32 s54, s62\n\t"
  "s_cselect_b32 s54, s44, s54\n\t"
  "s_cselect_b32 s62, -1, s62\n\t"
  "s_mov_b32 m0, s57\n\t"
  "s_xor_b32 s57, s57, 0x8000\n\t"
  "buffer_load_dwordx4 v223, s[40:43], s55 offen offset:0 sc1 lds\n\t"
  "buffer_load_dwordx4 v223, s[40:43], s55 offen offset:1024 sc1 lds\n\t"
  "buffer_load_dwordx4 v223, s[40:43], s55 offen offset:2048 sc1 lds\n\t"
  "buffer_load_dwordx4 v223, s[40:43], s55 offen offset:3072 sc1 lds\n\t"
  "s_add_u32 s55, s55, 0x8000\n\t"
  "s_cmp_eq_u32 s55, s63\n\t"
  "s_cselect_b32 s55, s45, s55\n\t"
  "s_cselect_b32 s63, -1, s63\n\t"
  "v_mov_b32 v0, 0\n\t"
  "v_mov_b32 v1, 0\n\t"
  "v_mov_b32 v2, 0\n\t"
  "v_mov_b32 v3, 0\n\t"
  "v_mov_b32 v4, 0\n\t"
  "v_mov_b32 v5, 0\n\t"
  "v_mov_b32 v6, 0\n\t"
  "v_mov_b32 v7, 0\n\t"
  "v_mov_b32 v8, 0\n\t"
  "v_mov_b32 v9, 0\n\t"
  "v_mov_b32 v10, 0\n\t"
  "v_mov_b32 v11, 0\n\t"
  "v_mov_b32 v12, 0\n\t"
  "v_mov_b32 v13, 0\n\t"
  "v_mov_b32 v14, 0\n\t"
  "v_mov_b32 v15, 0\n\t"
  "v_mov_b32 v16, 0\n\t"
  "v_mov_b32 v17, 0\n\t"
  "v_mov_b32 v18, 0\n\t"
  "v_mov_b32 v19, 0\n\t"
  "v_mov_b32 v20, 0\n\t"
  "v_mov_b32 v21, 0\n\t"
  "v_mov_b32 v22, 0\n\t"
  "v_mov_b32 v23, 0\n\t"
  "v_mov_b32 v24, 0\n\t"
  "v_mov_b32 v25, 0\n\t"
  "v_mov_b32 v26, 0\n\t"
  "v_mov_b32 v27, 0\n\t"
  "v_mov_b32 v28, 0\n\t"
  "v_mov_b32 v29, 0\n\t"
  "v_mov_b32 v30, 0\n\t"
  "v_mov_b32 v31, 0\n\t"
  "v_mov_b32 v32, 0\n\t"
  "v_mov_b32 v33, 0\n\t"
  "v_mov_b32 v34, 0\n\t"
  "v_mov_b32 v35, 0\n\t"
  "v_mov_b32 v36, 0\n\t"
  "v_mov_b32 v37, 0\n\t"
  "v_mov_b32 v38, 0\n\t"
  "v_mov_b32 v39, 0\n\t"
  "v_mov_b32 v40, 0\n\t"
  "v_mov_b32 v41, 0\n\t"
  "v_mov_b32 v42, 0\n\t"
  "v_mov_b32 v43, 0\n\t"
  "v_mov_b32 v44, 0\n\t"
  "v_mov_b32 v45, 0\n\t"
  "v_mov_b32 v46, 0\n\t"
  "v_mov_b32 v47, 0\n\t"
  "v_mov_b32 v48, 0\n\t"
  "v_mov_b32 v49, 0\n\t"
  "v_mov_b32 v50, 0\n\t"
  "v_mov_b32 v51, 0\n\t"
  "v_mov_b32 v52, 0\n\t"
  "v_mov_b32 v53, 0\n\t"
  "v_mov_b32 v54, 0\n\t"
  "v_mov_b32 v55, 0\n\t"
  "v_mov_b32 v56, 0\n\t"
  "v_mov_b32 v57, 0\n\t"
  "v_mov_b32 v58, 0\n\t"
  "v_mov_b32 v59, 0\n\t"
  "v_mov_b32 v60, 0\n\t"
  "v_mov_b32 v61, 0\n\t"
  "v_mov_b32 v62, 0\n\t"
  "v_mov_b32 v63, 0\n\t"
  "v_mov_b32 v64, 0\n\t"
  "v_mov_b32 v65, 0\n\t"
  "v_mov_b32 v66, 0\n\t"
  "v_mov_b32 v67, 0\n\t"
  "v_mov_b32 v69, v68\n\t"
  "v_mov_b32 v70, v68\n\t"
  "v_mov_b32 v71, v68\n\t"
  "v_mov_b32 v218, 0x7149f2ca\n\t"
  "v_mov_b32 v224, 0xff800000\n\t"
  "s_waitcnt vmcnt(8) lgkmcnt(0)\n\t"
  "s_barrier\n\t"
  "ds_read_b128 v[152:155], v216 offset:0\n\t"
  "ds_read_b128 v[156:159], v216 offset:1024\n\t"
  "ds_read_b128 v[160:163], v216 offset:2048\n\t"
  "ds_read_b128 v[164:167], v216 offset:3072\n\t"
  "ds_read_b128 v[168:171], v216 offset:4096\n\t"
  "ds_read_b128 v[172:175], v216 offset:5120\n\t"
  "ds_read_b128 v[176:179], v216 offset:6144\n\t"
  "ds_read_b128 v[180:183], v216 offset:7168\n\t"
  "ds_read_b128 v[188:191], v216 offset:8192\n\t"
  "ds_read_b128 v[192:195], v216 offset:9216\n\t"
  "ds_read_b128 v[196:199], v216 offset:10240\n\t"
  "ds_read_b128 v[200:203], v216 offset:11264\n\t"
  "s_waitcnt lgkmcnt(8)\n\t"
  "v_mfma_f32_32x32x16_bf16 v[104:119], v[152:155], v[72:75], 0\n\t"
  "ds_read_b128 v[204:207], v216 offset:12288\n\t"
  "v_mfma_f32_32x32x16_bf16 v[104:119], v[156:159], v[76:79], v[104:119]\n\t"
  "ds_read_b128 v[208:211], v216 offset:13312\n\t"
  "v_mfma_f32_32x32x16_bf16 v[104:119], v[160:163], v[80:83], v[104:119]\n\t"
  "ds_read_b128 v[212:215], v216 offset:14336\n\t"
  "v_mfma_f32_32x32x16_bf16 v[104:119], v[164:167], v[84:87], v[104:119]\n\t"
  "ds_read_b128 v[152:155], v216 offset:15360\n\t"
  "s_waitcnt lgkmcnt(8)\n\t"
  "v_mfma_f32_32x32x16_bf16 v[104:119], v[168:171], v[88:91], v[104:119]\n\t"
  "v_mfma_f32_32x32x16_bf16 v[104:119], v[172:175], v[92:95], v[104:119]\n\t"
  "v_mfma_f32_32x32x16_bf16 v[104:119], v[176:179], v[96:99], v[104:119]\n\t"
  "v_mfma_f32_32x32x16_bf16 v[104:119], v[180:183], v[100:103], v[104:119]\n\t"
  "s_nop 7\n\t"
  "s_nop 1\n\t"
  "s_waitcnt lgkmcnt(4)\n\t"
  "v_mfma_f32_32x32x16_bf16 v[120:135], v[188:191], v[72:75], 0\n\t"
  "s_cmp_lg_u32 s70, 0\n\t"
  "s_cbranch_scc1 LATmaskA_f_%=\n\t"
  "LATmaskA_ret_f_%=:\n\t"
  "v_cvt_pk_bf16_f32 v104, 0, v104\n\t"
  "v_cvt_pk_bf16_f32 v105, 0, v105\n\t"
  "v_cvt_pk_bf16_f32 v106, 0, v106\n\t"
  "v_cvt_pk_bf16_f32 v107, 0, v107\n\t"
  "v_mul_f32 v104, s66, v104\n\t"
  "v_mul_f32 v105, s66, v105\n\t"
  "v_mfma_f32_32x32x16_bf16 v[120:135], v[192:195], v[76:79], v[120:135]\n\t"
  "v_mul_f32 v106, s66, v106\n\t"
  "v_mul_f32 v107, s66, v107\n\t"
  "v_cvt_pk_bf16_f32 v104, 0, v104\n\t"
  "v_cvt_pk_bf16_f32 v105, 0, v105\n\t"
  "v_cvt_pk_bf16_f32 v106, 0, v106\n\t"
  "v_mfma_f32_32x32x16_bf16 v[120:135], v[196:199], v[80:83], v[120:135]\n\t"
  "v_cvt_pk_bf16_f32 v107, 0, v107\n\t"
  "v_cvt_pk_bf16_f32 v108, 0, v108\n\t"
  "v_cvt_pk_bf16_f32 v109, 0, v109\n\t"
  "v_cvt_pk_bf16_f32 v110, 0, v110\n\t"
  "v_cvt_pk_bf16_f32 v111, 0, v111\n\t"
  "v_max_f32 v219, v104, v105\n\t"
  "v_mfma_f32_32x32x16_bf16 v[120:135], v[200:203], v[84:87], v[120:135]\n\t"
  "v_max_f32 v220, v106, v107\n\t"
  "v_mul_f32 v108, s66, v108\n\t"
  "v_mul_f32 v109, s66, v109\n\t"
  "v_mul_f32 v110, s66, v110\n\t"
  "v_mul_f32 v111, s66, v111\n\t"
  "s_waitcnt lgkmcnt(3)\n\t"
  "v_mfma_f32_32x32x16_bf16 v[120:135], v[204:207], v[88:91], v[120:135]\n\t"
  "v_cvt_pk_bf16_f32 v108, 0, v108\n\t"
  "v_cvt_pk_bf16_f32 v109, 0, v109\n\t"
  "v_cvt_pk_bf16_f32 v110, 0, v110\n\t"
  "v_cvt_pk_bf16_f32 v111, 0, v111\n\t"
  "v_cvt_pk_bf16_f32 v112, 0, v112\n\t"
  "v_cvt_pk_bf16_f32 v113, 0, v113\n\t"
  "s_waitcnt lgkmcnt(2)\n\t"
  "v_mfma_f32_32x32x16_bf16 v[120:135], v[208:211], v[92:95], v[120:135]\n\t"
  "v_cvt_pk_bf16_f32 v114, 0, v114\n\t"
  "v_cvt_pk_bf16_f32 v115, 0, v115\n\t"
  "v_max3_f32 v219, v108, v109, v219\n\t"
  "v_max3_f32 v220, v110, v111, v220\n\t"
  "v_mul_f32 v112, s66, v112\n\t"
  "s_waitcnt lgkmcnt(1)\n\t"
  "v_mfma_f32_32x32x16_bf16 v[120:135], v[212:215], v[96:99], v[120:135]\n\t"
  "v_mul_f32 v113, s66, v113\n\t"
  "v_mul_f32 v114, s66, v114\n\t"
  "v_mul_f32 v115, s66, v115\n\t"
  "v_cvt_pk_bf16_f32 v112, 0, v112\n\t"
  "v_cvt_pk_bf16_f32 v113, 0, v113\n\t"
  "v_cvt_pk_bf16_f32 v114, 0, v114\n\t"
  "s_waitcnt lgkmcnt(0)\n\t"
  "v_mfma_f32_32x32x16_bf16 v[120:135], v[152:155], v[100:103], v[120:135]\n\t"
  "s_add_u32 s71, s53, 1\n\t"
  "s_cmp_eq_u32 s71, s52\n\t"
  "s_cbranch_scc1 LATqb_f_%=\n\t"
  "LATqb_ret_f_%=:\n\t"
  "v_cvt_pk_bf16_f32 v115, 0, v115\n\t"
  "v_cvt_pk_bf16_f32 v116, 0, v116\n\t"
  "v_cvt_pk_bf16_f32 v117, 0, v117\n\t"
  "v_cvt_pk_bf16_f32 v118, 0, v118\n\t"
  "v_cvt_pk_bf16_f32 v119, 0, v119\n\t"
  "v_max3_f32 v219, v112, v113, v219\n\t"
  "v_max3_f32 v220, v114, v115, v220\n\t"
  "v_mul_f32 v116, s66, v116\n\t"
  "v_mul_f32 v117, s66, v117\n\t"
  "v_mul_f32 v118, s66, v118\n\t"
  "v_mul_f32 v119, s66, v119\n\t"
  "v_cvt_pk_bf16_f32 v116, 0, v116\n\t"
  "v_cvt_pk_bf16_f32 v117, 0, v117\n\t"
  "v_cvt_pk_bf16_f32 v118, 0, v118\n\t"
  "v_cvt_pk_bf16_f32 v119, 0, v119\n\t"
  "v_max3_f32 v219, v116, v117, v219\n\t"
  "v_max3_f32 v220, v118, v119, v220\n\t"
  "s_cmp_lg_u32 s70, 0\n\t"
  "s_cbranch_scc1 LATmaskB_f_%=\n\t"
  "LATmaskB_ret_f_%=:\n\t"
  "v_cvt_pk_bf16_f32 v120, 0, v120\n\t"
  "v_cvt_pk_bf16_f32 v121, 0, v121\n\t"
  "v_cvt_pk_bf16_f32 v122, 0, v122\n\t"
  "v_cvt_pk_bf16_f32 v123, 0, v123\n\t"
  "v_mul_f32 v120, s66, v120\n\t"
  "v_mul_f32 v121, s66, v121\n\t"
  "v_mul_f32 v122, s66, v122\n\t"
  "v_mul_f32 v123, s66, v123\n\t"
  "v_cvt_pk_bf16_f32 v120, 0, v120\n\t"
  "v_cvt_pk_bf16_f32 v121, 0, v121\n\t"
  "v_cvt_pk_bf16_f32 v122, 0, v122\n\t"
  "v_cvt_pk_bf16_f32 v123, 0, v123\n\t"
  "v_cvt_pk_bf16_f32 v124, 0, v124\n\t"
  "v_cvt_pk_bf16_f32 v125, 0, v125\n\t"
  "v_cvt_pk_bf16_f32 v126, 0, v126\n\t"
  "v_cvt_pk_bf16_f32 v127, 0, v127\n\t"
  "v_max3_f32 v219, v120, v121, v219\n\t"
  "v_max3_f32 v220, v122, v123, v220\n\t"
  "v_mul_f32 v124, s66, v124\n\t"
  "v_mul_f32 v125, s66, v125\n\t"
  "v_mul_f32 v126, s66, v126\n\t"
  "v_mul_f32 v127, s66, v127\n\t"
  "v_cvt_pk_bf16_f32 v124, 0, v124\n\t"
  "v_cvt_pk_bf16_f32 v125, 0, v125\n\t"
  "v_cvt_pk_bf16_f32 v126, 0, v126\n\t"
  "v_cvt_pk_bf16_f32 v127, 0, v127\n\t"
  "v_cvt_pk_bf16_f32 v128, 0, v128\n\t"
  "v_cvt_pk_bf16_f32 v129, 0, v129\n\t"
  "v_cvt_pk_bf16_f32 v130, 0, v130\n\t"
  "v_cvt_pk_bf16_f32 v131, 0, v131\n\t"
  "v_max3_f32 v219, v124, v125, v219\n\t"
  "v_max3_f32 v220, v126, v127, v220\n\t"
  "v_mul_f32 v128, s66, v128\n\t"
  "v_mul_f32 v129, s66, v129\n\t"
  "v_mul_f32 v130, s66, v130\n\t"
  "v_mul_f32 v131, s66, v131\n\t"
  "v_cvt_pk_bf16_f32 v128, 0, v128\n\t"
  "v_cvt_pk_bf16_f32 v129, 0, v129\n\t"
  "v_cvt_pk_bf16_f32 v130, 0, v130\n\t"
  "v_cvt_pk_bf16_f32 v131, 0, v131\n\t"
  "v_cvt_pk_bf16_f32 v132, 0, v132\n\t"
  "v_cvt_pk_bf16_f32 v133, 0, v133\n\t"
  "v_cvt_pk_bf16_f32 v134, 0, v134\n\t"
  "v_cvt_pk_bf16_f32 v135, 0, v135\n\t"
  "v_max3_f32 v219, v128, v129, v219\n\t"
  "v_max3_f32 v220, v130, v131, v220\n\t"
  "v_mul_f32 v132, s66, v132\n\t"
  "v_mul_f32 v133, s66, v133\n\t"
  "v_mul_f32 v134, s66, v134\n\t"
  "v_mul_f32 v135, s66, v135\n\t"
  "v_cvt_pk_bf16_f32 v132, 0, v132\n\t"
  "v_cvt_pk_bf16_f32 v133, 0, v133\n\t"
  "v_cvt_pk_bf16_f32 v134, 0, v134\n\t"
  "v_cvt_pk_bf16_f32 v135, 0, v135\n\t"
  "v_max3_f32 v219, v132, v133, v219\n\t"
  "v_max3_f32 v220, v134, v135, v220\n\t"
  "v_max_f32 v219, v219, v220\n\t"
  "v_fma_f32 v228, v219, s67, v218\n\t"
  "v_cmp_lt_f32 vcc, s68, v228\n\t"
  "s_cbranch_vccnz LATrareA_f_%=\n\t"
  "LATrareA_ret_f_%=:\n\t"
  "s_waitcnt vmcnt(0) lgkmcnt(0)\n\t"
  "s_barrier\n\t"
  "v_xor_b32 v216, 0x8000, v216\n\t"
  "ds_read_b128 v[152:155], v216 offset:0\n\t"
  "ds_read_b128 v[156:159], v216 offset:1024\n\t"
  "ds_read_b128 v[160:163], v216 offset:2048\n\t"
  "ds_read_b128 v[164:167], v216 offset:3072\n\t"
  "ds_read_b128 v[168:171], v216 offset:4096\n\t"
  "ds_read_b128 v[172:175], v216 offset:5120\n\t"
  "ds_read_b128 v[176:179], v216 offset:6144\n\t"
  "ds_read_b128 v[180:183], v216 offset:7168\n\t"
  "v_fma_f32 v104, v104, s67, v218\n\t"
  "v_fma_f32 v105, v105, s67, v218\n\t"
  "v_fma_f32 v106, v106, s67, v218\n\t"
  "v_fma_f32 v107, v107, s67, v218\n\t"
  "v_exp_f32 v104, v104\n\t"
  "v_exp_f32 v105, v105\n\t"
  "v_exp_f32 v106, v106\n\t"
  "v_exp_f32 v107, v107\n\t"
  "v_fma_f32 v108, v108, s67, v218\n\t"
  "v_fma_f32 v109, v109, s67, v218\n\t"
  "v_fma_f32 v110, v110, s67, v218\n\t"
  "v_fma_f32 v111, v111, s67, v218\n\t"
  "v_cvt_pk_bf16_f32 v136, v104, v105\n\t"
  "v_cvt_pk_bf16_f32 v137, v106, v107\n\t"
  "v_exp_f32 v108, v108\n\t"
  "v_exp_f32 v109, v109\n\t"
  "v_exp_f32 v110, v110\n\t"
  "v_exp_f32 v111, v111\n\t"
  "v_fma_f32 v112, v112, s67, v218\n\t"
  "v_fma_f32 v113, v113, s67, v218\n\t"
  "v_fma_f32 v114, v114, s67, v218\n\t"
  "v_fma_f32 v115, v115, s67, v218\n\t"
  "v_cvt_pk_bf16_f32 v138, v108, v109\n\t"
  "v_cvt_pk_bf16_f32 v139, v110, v111\n\t"
  "v_exp_f32 v112, v112\n\t"
  "v_exp_f32 v113, v113\n\t"
  "v_exp_f32 v114, v114\n\t"
  "v_exp_f32 v115, v115\n\t"
  "v_fma_f32 v116, v116, s67, v218\n\t"
  "v_fma_f32 v117, v117, s67, v218\n\t"
  "v_fma_f32 v118, v118, s67, v218\n\t"
  "v_fma_f32 v119, v119, s67, v218\n\t"
  "v_cvt_pk_bf16_f32 v140, v112, v113\n\t"
  "v_cvt_pk_bf16_f32 v141, v114, v115\n\t"
  "v_exp_f32 v116, v116\n\t"
  "v_exp_f32 v117, v117\n\t"
  "v_exp_f32 v118, v118\n\t"
  "v_exp_f32 v119, v119\n\t"
  "v_cvt_pk_bf16_f32 v142, v116, v117\n\t"
  "v_cvt_pk_bf16_f32 v143, v118, v119\n\t"
  "s_mov_b32 s58, 0\n\t"
  "LATnext_%=:\n\t"
  "s_add_u32 s53, s53, 1\n\t"
  "s_cmp_eq_u32 s53, 17\n\t"
  "s_cbranch_scc1 LATfinal_%=\n\t"
  "s_add_u32 s71, s53, 1\n\t"
  "s_cmp_eq_u32 s71, s52\n\t"
  "s_cselect_b32 s70, 1, 0\n\t"
  "s_cmp_eq_u32 s53, 16\n\t"
  "s_cselect_b32 s70, 1, s70\n\t"
  "s_waitcnt lgkmcnt(4)\n\t"
  "v_mfma_f32_32x32x16_bf16 v[104:119], v[152:155], v[72:75], 0\n\t"
  "ds_read_b128 v[184:187], v217 offset:0\n\t"
  "s_mov_b32 m0, s56\n\t"
  "s_xor_b32 s56, s56, 0x8000\n\t"
  "buffer_load_dwordx4 v223, s[40:43], s54 offen offset:0 sc1 lds\n\t"
  "v_fma_f32 v120, v120, s67, v218\n\t"
  "v_fma_f32 v121, v121, s67, v218\n\t"
  "v_fma_f32 v122, v122, s67, v218\n\t"
  "v_fma_f32 v123, v123, s67, v218\n\t"
  "v_exp_f32 v120, v120\n\t"
  "v_exp_f32 v121, v121\n\t"
  "v_mfma_f32_32x32x16_bf16 v[104:119], v[156:159], v[76:79], v[104:119]\n\t"
  "ds_read_b128 v[188:191], v216 offset:8192\n\t"
  "v_exp_f32 v122, v122\n\t"
  "v_exp_f32 v123, v123\n\t"
  "v_fma_f32 v124, v124, s67, v218\n\t"
  "v_mfma_f32_32x32x16_bf16 v[104:119], v[160:163], v[80:83], v[104:119]\n\t"
  "ds_read_b128 v[192:195], v216 offset:9216\n\t"
  "buffer_load_dwordx4 v223, s[40:43], s54 offen offset:1024 sc1 lds\n\t"
  "v_fma_f32 v125, v125, s67, v218\n\t"
  "v_fma_f32 v126, v126, s67, v218\n\t"
  "v_fma_f32 v127, v127, s67, v218\n\t"
  "v_cvt_pk_bf16_f32 v144, v120, v121\n\t"
  "v_cvt_pk_bf16_f32 v145, v122, v123\n\t"
  "v_exp_f32 v124, v124\n\t"
  "v_mfma_f32_32x32x16_bf16 v[104:119], v[164:167], v[84:87], v[104:119]\n\t"
  "ds_read_b128 v[196:199], v216 offset:10240\n\t"
  "v_exp_f32 v125, v125\n\t"
  "v_exp_f32 v126, v126\n\t"
  "v_exp_f32 v127, v127\n\t"
  "s_waitcnt lgkmcnt(4)\n\t"
  "v_mfma_f32_32x32x16_bf16 v[104:119], v[168:171], v[88:91], v[104:119]\n\t"
  "ds_read_b128 v[200:203], v216 offset:11264\n\t"
  "buffer_load_dwordx4 v223, s[40:43], s54 offen offset:2048 sc1 lds\n\t"
  "v_fma_f32 v128, v128, s67, v218\n\t"
  "v_fma_f32 v129, v129, s67, v218\n\t"
  "v_fma_f32 v130, v130, s67, v218\n\t"
  "v_fma_f32 v131, v131, s67, v218\n\t"
  "v_cvt_pk_bf16_f32 v146, v124, v125\n\t"
  "v_cvt_pk_bf16_f32 v147, v126, v127\n\t"
  "v_mfma_f32_32x32x16_bf16 v[104:119], v[172:175], v[92:95], v[104:119]\n\t"
  "ds_read_b128 v[204:207], v216 offset:12288\n\t"
  "v_exp_f32 v128, v128\n\t"
  "v_exp_f32 v129, v129\n\t"
  "v_exp_f32 v130, v130\n\t"
  "v_mfma_f32_32x32x16_bf16 v[104:119], v[176:179], v[96:99], v[104:119]\n\t"
  "ds_read_b128 v[208:211], v216 offset:13312\n\t"
  "buffer_load_dwordx4 v223, s[40:43], s54 offen offset:3072 sc1 lds\n\t"
  "s_add_u32 s54, s54, 0x8000\n\t"
  "s_cmp_eq_u32 s54, s62\n\t"
  "s_cselect_b32 s54, s44, s54\n\t"
  "s_cselect_b32 s62, -1, s62\n\t"
  "v_exp_f32 v131, v131\n\t"
  "v_fma_f32 v132, v132, s67, v218\n\t"
  "v_fma_f32 v133, v133, s67, v218\n\t"
  "v_fma_f32 v134, v134, s67, v218\n\t"
  "v_fma_f32 v135, v135, s67, v218\n\t"
  "v_mfma_f32_32x32x16_bf16 v[104:119], v[180:183], v[100:103], v[104:119]\n\t"
  "ds_read_b128 v[212:215], v216 offset:14336\n\t"
  "v_cvt_pk_bf16_f32 v148, v128, v129\n\t"
  "v_cvt_pk_bf16_f32 v149, v130, v131\n\t"
  "v_exp_f32 v132, v132\n\t"
  "v_exp_f32 v133, v133\n\t"
  "s_waitcnt lgkmcnt(4)\n\t"
  "v_mfma_f32_32x32x16_bf16 v[0:15], v[184:187], v[136:139], v[0:15]\n\t"
  "ds_read_b128 v[152:155], v216 offset:15360\n\t"
  "s_mov_b32 m0, s57\n\t"
  "s_xor_b32 s57, s57, 0x8000\n\t"
  "buffer_load_dwordx4 v223, s[40:43], s55 offen offset:0 sc1 lds\n\t"
  "v_exp_f32 v134, v134\n\t"
  "v_exp_f32 v135, v135\n\t"
  "v_cvt_pk_bf16_f32 v150, v132, v133\n\t"
  "v_cvt_pk_bf16_f32 v151, v134, v135\n\t"
  "s_cmp_eq_u32 s53, s52\n\t"
  "s_cbranch_scc1 LATseam_s_%=\n\t"
  "LATseam_ret_s_%=:\n\t"
  "s_cmp_lg_u32 s70, 0\n\t"
  "s_cbranch_scc1 LATmaskA_s_%=\n\t"
  "LATmaskA_ret_s_%=:\n\t"
  "v_cvt_pk_bf16_f32 v104, 0, v104\n\t"
  "v_mfma_f32_32x32x16_bf16 v[120:135], v[188:191], v[72:75], 0\n\t"
  "ds_read_b128 v[156:159], v217 offset:1024\n\t"
  "v_cvt_pk_bf16_f32 v105, 0, v105\n\t"
  "v_cvt_pk_bf16_f32 v106, 0, v106\n\t"
  "v_cvt_pk_bf16_f32 v107, 0, v107\n\t"
  "v_mul_f32 v104, s66, v104\n\t"
  "v_mul_f32 v105, s66, v105\n\t"
  "v_mul_f32 v106, s66, v106\n\t"
  "v_mfma_f32_32x32x16_bf16 v[120:135], v[192:195], v[76:79], v[120:135]\n\t"
  "ds_read_b128 v[160:163], v217 offset:2048\n\t"
  "buffer_load_dwordx4 v223, s[40:43], s55 offen offset:1024 sc1 lds\n\t"
  "v_mul_f32 v107, s66, v107\n\t"
  "v_cvt_pk_bf16_f32 v104, 0, v104\n\t"
  "v_cvt_pk_bf16_f32 v105, 0, v105\n\t"
  "v_cvt_pk_bf16_f32 v106, 0, v106\n\t"
  "v_cvt_pk_bf16_f32 v107, 0, v107\n\t"
  "v_cvt_pk_bf16_f32 v108, 0, v108\n\t"
  "v_mfma_f32_32x32x16_bf16 v[120:135], v[196:199], v[80:83], v[120:135]\n\t"
  "ds_read_b128 v[164:167], v217 offset:3072\n\t"
  "v_cvt_pk_bf16_f32 v109, 0, v109\n\t"
  "v_cvt_pk_bf16_f32 v110, 0, v110\n\t"
  "v_cvt_pk_bf16_f32 v111, 0, v111\n\t"
  "v_max_f32 v219, v104, v105\n\t"
  "v_max_f32 v220, v106, v107\n\t"
  "v_mul_f32 v108, s66, v108\n\t"
  "s_waitcnt lgkmcnt(4)\n\t"
  "v_mfma_f32_32x32x16_bf16 v[120:135], v[200:203], v[84:87], v[120:135]\n\t"
  "ds_read_b128 v[168:171], v217 offset:4096\n\t"
  "buffer_load_dwordx4 v223, s[40:43], s55 offen offset:2048 sc1 lds\n\t"
  "v_mul_f32 v109, s66, v109\n\t"
  "v_mul_f32 v110, s66, v110\n\t"
  "v_mul_f32 v111, s66, v111\n\t"
  "v_cvt_pk_bf16_f32 v108, 0, v108\n\t"
  "v_cvt_pk_bf16_f32 v109, 0, v109\n\t"
  "v_cvt_pk_bf16_f32 v110, 0, v110\n\t"
  "v_cvt_pk_bf16_f32 v111, 0, v111\n\t"
  "v_mfma_f32_32x32x16_bf16 v[120:135], v[204:207], v[88:91], v[120:135]\n\t"
  "ds_read_b128 v[172:175], v217 offset:5120\n\t"
  "v_cvt_pk_bf16_f32 v112, 0, v112\n\t"
  "v_cvt_pk_bf16_f32 v113, 0, v113\n\t"
  "v_cvt_pk_bf16_f32 v114, 0, v114\n\t"
  "v_cvt_pk_bf16_f32 v115, 0, v115\n\t"
  "v_max3_f32 v219, v108, v109, v219\n\t"
  "v_max3_f32 v220, v110, v111, v220\n\t"
  "v_mfma_f32_32x32x16_bf16 v[120:135], v[208:211], v[92:95], v[120:135]\n\t"
  "ds_read_b128 v[176:179], v217 offset:6144\n\t"
  "buffer_load_dwordx4 v223, s[40:43], s55 offen offset:3072 sc1 lds\n\t"
  "s_add_u32 s55, s55, 0x8000\n\t"
  "s_cmp_eq_u32 s55, s63\n\t"
  "s_cselect_b32 s55, s45, s55\n\t"
  "s_cselect_b32 s63, -1, s63\n\t"
  "v_mul_f32 v112, s66, v112\n\t"
  "v_mul_f32 v113, s66, v113\n\t"
  "v_mul_f32 v114, s66, v114\n\t"
  "v_mul_f32 v115, s66, v115\n\t"
  "v_cvt_pk_bf16_f32 v112, 0, v112\n\t"
  "v_cvt_pk_bf16_f32 v113, 0, v113\n\t"
  "v_mfma_f32_32x32x16_bf16 v[120:135], v[212:215], v[96:99], v[120:135]\n\t"
  "ds_read_b128 v[180:183], v217 offset:7168\n\t"
  "v_cvt_pk_bf16_f32 v114, 0, v114\n\t"
  "v_cvt_pk_bf16_f32 v115, 0, v115\n\t"
  "v_cvt_pk_bf16_f32 v116, 0, v116\n\t"
  "v_cvt_pk_bf16_f32 v117, 0, v117\n\t"
  "v_cvt_pk_bf16_f32 v118, 0, v118\n\t"
  "v_cvt_pk_bf16_f32 v119, 0, v119\n\t"
  "s_waitcnt lgkmcnt(4)\n\t"
  "v_mfma_f32_32x32x16_bf16 v[120:135], v[152:155], v[100:103], v[120:135]\n\t"
  "ds_read_b128 v[184:187], v217 offset:8192\n\t"
  "s_add_u32 s71, s53, 1\n\t"
  "s_cmp_eq_u32 s71, s52\n\t"
  "s_cbranch_scc1 LATqb_s_%=\n\t"
  "LATqb_ret_s_%=:\n\t"
  "v_max3_f32 v219, v112, v113, v219\n\t"
  "v_max3_f32 v220, v114, v115, v220\n\t"
  "v_mul_f32 v116, s66, v116\n\t"
  "v_mul_f32 v117, s66, v117\n\t"
  "v_mul_f32 v118, s66, v118\n\t"
  "v_mul_f32 v119, s66, v119\n\t"
  "v_cvt_pk_bf16_f32 v116, 0, v116\n\t"
  "v_mfma_f32_32x32x16_bf16 v[16:31], v[156:159], v[136:139], v[16:31]\n\t"
  "ds_read_b128 v[188:191], v217 offset:9216\n\t"
  "v_cvt_pk_bf16_f32 v117, 0, v117\n\t"
  "v_cvt_pk_bf16_f32 v118, 0, v118\n\t"
  "v_cvt_pk_bf16_f32 v119, 0, v119\n\t"
  "v_max3_f32 v219, v116, v117, v219\n\t"
  "v_max3_f32 v220, v118, v119, v220\n\t"
  "s_cmp_lg_u32 s70, 0\n\t"
  "s_cbranch_scc1 LATmaskB_s_%=\n\t"
  "LATmaskB_ret_s_%=:\n\t"
  "v_cvt_pk_bf16_f32 v120, 0, v120\n\t"
  "v_mfma_f32_32x32x16_bf16 v[32:47], v[160:163], v[136:139], v[32:47]\n\t"
  "ds_read_b128 v[192:195], v217 offset:10240\n\t"
  "v_cvt_pk_bf16_f32 v121, 0, v121\n\t"
  "v_cvt_pk_bf16_f32 v122, 0, v122\n\t"
  "v_cvt_pk_bf16_f32 v123, 0, v123\n\t"
  "v_mul_f32 v120, s66, v120\n\t"
  "v_mul_f32 v121, s66, v121\n\t"
  "v_mul_f32 v122, s66, v122\n\t"
  "v_mfma_f32_32x32x16_bf16 v[48:63], v[164:167], v[136:139], v[48:63]\n\t"
  "ds_read_b128 v[196:199], v217 offset:11264\n\t"
  "v_mul_f32 v123, s66, v123\n\t"
  "v_cvt_pk_bf16_f32 v120, 0, v120\n\t"
  "v_cvt_pk_bf16_f32 v121, 0, v121\n\t"
  "v_cvt_pk_bf16_f32 v122, 0, v122\n\t"
  "v_cvt_pk_bf16_f32 v123, 0, v123\n\t"
  "v_cvt_pk_bf16_f32 v124, 0, v124\n\t"
  "v_mfma_f32_16x16x32_bf16 v[64:67], v[68:71], v[136:139], v[64:67]\n\t"
  "ds_read_b128 v[200:203], v217 offset:12288\n\t"
  "v_cvt_pk_bf16_f32 v125, 0, v125\n\t"
  "v_cvt_pk_bf16_f32 v126, 0, v126\n\t"
  "v_cvt_pk_bf16_f32 v127, 0, v127\n\t"
  "v_max3_f32 v219, v120, v121, v219\n\t"
  "v_max3_f32 v220, v122, v123, v220\n\t"
  "v_mul_f32 v124, s66, v124\n\t"
  "v_mul_f32 v125, s66, v125\n\t"
  "s_waitcnt lgkmcnt(5)\n\t"
  "v_mfma_f32_32x32x16_bf16 v[0:15], v[168:171], v[140:143], v[0:15]\n\t"
  "ds_read_b128 v[204:207], v217 offset:13312\n\t"
  "v_mul_f32 v126, s66, v126\n\t"
  "v_mul_f32 v127, s66, v127\n\t"
  "v_cvt_pk_bf16_f32 v124, 0, v124\n\t"
  "v_cvt_pk_bf16_f32 v125, 0, v125\n\t"
  "v_cvt_pk_bf16_f32 v126, 0, v126\n\t"
  "v_cvt_pk_bf16_f32 v127, 0, v127\n\t"
  "v_mfma_f32_32x32x16_bf16 v[16:31], v[172:175], v[140:143], v[16:31]\n\t"
  "ds_read_b128 v[208:211], v217 offset:14336\n\t"
  "v_cvt_pk_bf16_f32 v128, 0, v128\n\t"
  "v_cvt_pk_bf16_f32 v129, 0, v129\n\t"
  "v_cvt_pk_bf16_f32 v130, 0, v130\n\t"
  "v_cvt_pk_bf16_f32 v131, 0, v131\n\t"
  "v_max3_f32 v219, v124, v125, v219\n\t"
  "v_max3_f32 v220, v126, v127, v220\n\t"
  "v_mfma_f32_32x32x16_bf16 v[32:47], v[176:179], v[140:143], v[32:47]\n\t"
  "ds_read_b128 v[212:215], v217 offset:15360\n\t"
  "v_mul_f32 v128, s66, v128\n\t"
  "v_mul_f32 v129, s66, v129\n\t"
  "v_mul_f32 v130, s66, v130\n\t"
  "v_mul_f32 v131, s66, v131\n\t"
  "v_cvt_pk_bf16_f32 v128, 0, v128\n\t"
  "v_cvt_pk_bf16_f32 v129, 0, v129\n\t"
  "v_mfma_f32_32x32x16_bf16 v[48:63], v[180:183], v[140:143], v[48:63]\n\t"
  "v_cvt_pk_bf16_f32 v130, 0, v130\n\t"
  "v_cvt_pk_bf16_f32 v131, 0, v131\n\t"
  "v_cvt_pk_bf16_f32 v132, 0, v132\n\t"
  "v_cvt_pk_bf16_f32 v133, 0, v133\n\t"
  "v_cvt_pk_bf16_f32 v134, 0, v134\n\t"
  "v_cvt_pk_bf16_f32 v135, 0, v135\n\t"
  "v_max3_f32 v219, v128, v129, v219\n\t"
  "v_mfma_f32_16x16x32_bf16 v[64:67], v[68:71], v[140:143], v[64:67]\n\t"
  "v_max3_f32 v220, v130, v131, v220\n\t"
  "v_mul_f32 v132, s66, v132\n\t"
  "v_mul_f32 v133, s66, v133\n\t"
  "v_mul_f32 v134, s66, v134\n\t"
  "v_mul_f32 v135, s66, v135\n\t"
  "v_cvt_pk_bf16_f32 v132, 0, v132\n\t"
  "s_waitcnt lgkmcnt(4)\n\t"
  "v_mfma_f32_32x32x16_bf16 v[0:15], v[184:187], v[144:147], v[0:15]\n\t"
  "v_cvt_pk_bf16_f32 v133, 0, v133\n\t"
  "v_cvt_pk_bf16_f32 v134, 0, v134\n\t"
  "v_cvt_pk_bf16_f32 v135, 0, v135\n\t"
  "v_max3_f32 v219, v132, v133, v219\n\t"
  "v_max3_f32 v220, v134, v135, v220\n\t"
  "v_max_f32 v219, v219, v220\n\t"
  "v_mfma_f32_32x32x16_bf16 v[16:31], v[188:191], v[144:147], v[16:31]\n\t"
  "v_fma_f32 v228, v219, s67, v218\n\t"
  "v_cmp_lt_f32 vcc, s68, v228\n\t"
  "s_cbranch_vccnz LATrareA_s_%=\n\t"
  "LATrareA_ret_s_%=:\n\t"
  "v_fma_f32 v104, v104, s67, v218\n\t"
  "v_fma_f32 v105, v105, s67, v218\n\t"
  "v_fma_f32 v106, v106, s67, v218\n\t"
  "v_fma_f32 v107, v107, s67, v218\n\t"
  "s_waitcnt vmcnt(0) lgkmcnt(0)\n\t"
  "s_barrier\n\t"
  "v_xor_b32 v216, 0x8000, v216\n\t"
  "v_xor_b32 v217, 0x8000, v217\n\t"
  "s_add_u32 s71, s52, 5\n\t"
  "s_cmp_eq_u32 s53, s71\n\t"
  "s_cbranch_scc1 LATpf_%=\n\t"
  "LATpf_ret_%=:\n\t"
  "v_mfma_f32_32x32x16_bf16 v[32:47], v[192:195], v[144:147], v[32:47]\n\t"
  "ds_read_b128 v[152:155], v216 offset:0\n\t"
  "v_exp_f32 v104, v104\n\t"
  "v_exp_f32 v105, v105\n\t"
  "v_exp_f32 v106, v106\n\t"
  "v_exp_f32 v107, v107\n\t"
  "v_mfma_f32_32x32x16_bf16 v[48:63], v[196:199], v[144:147], v[48:63]\n\t"
  "ds_read_b128 v[156:159], v216 offset:1024\n\t"
  "v_fma_f32 v108, v108, s67, v218\n\t"
  "v_fma_f32 v109, v109, s67, v218\n\t"
  "v_fma_f32 v110, v110, s67, v218\n\t"
  "v_fma_f32 v111, v111, s67, v218\n\t"
  "v_cvt_pk_bf16_f32 v136, v104, v105\n\t"
  "v_mfma_f32_16x16x32_bf16 v[64:67], v[68:71], v[144:147], v[64:67]\n\t"
  "ds_read_b128 v[160:163], v216 offset:2048\n\t"
  "v_cvt_pk_bf16_f32 v137, v106, v107\n\t"
  "v_exp_f32 v108, v108\n\t"
  "v_exp_f32 v109, v109\n\t"
  "v_exp_f32 v110, v110\n\t"
  "v_mfma_f32_32x32x16_bf16 v[0:15], v[200:203], v[148:151], v[0:15]\n\t"
  "ds_read_b128 v[164:167], v216 offset:3072\n\t"
  "v_exp_f32 v111, v111\n\t"
  "v_fma_f32 v112, v112, s67, v218\n\t"
  "v_fma_f32 v113, v113, s67, v218\n\t"
  "v_fma_f32 v114, v114, s67, v218\n\t"
  "v_mfma_f32_32x32x16_bf16 v[16:31], v[204:207], v[148:151], v[16:31]\n\t"
  "ds_read_b128 v[168:171], v216 offset:4096\n\t"
  "v_fma_f32 v115, v115, s67, v218\n\t"
  "v_cvt_pk_bf16_f32 v138, v108, v109\n\t"
  "v_cvt_pk_bf16_f32 v139, v110, v111\n\t"
  "v_exp_f32 v112, v112\n\t"
  "v_exp_f32 v113, v113\n\t"
  "v_mfma_f32_32x32x16_bf16 v[32:47], v[208:211], v[148:151], v[32:47]\n\t"
  "ds_read_b128 v[172:175], v216 offset:5120\n\t"
  "v_exp_f32 v114, v114\n\t"
  "v_exp_f32 v115, v115\n\t"
  "v_fma_f32 v116, v116, s67, v218\n\t"
  "v_fma_f32 v117, v117, s67, v218\n\t"
  "v_mfma_f32_32x32x16_bf16 v[48:63], v[212:215], v[148:151], v[48:63]\n\t"
  "ds_read_b128 v[176:179], v216 offset:6144\n\t"
  "v_fma_f32 v118, v118, s67, v218\n\t"
  "v_fma_f32 v119, v119, s67, v218\n\t"
  "v_cvt_pk_bf16_f32 v140, v112, v113\n\t"
  "v_cvt_pk_bf16_f32 v141, v114, v115\n\t"
  "v_exp_f32 v116, v116\n\t"
  "v_mfma_f32_16x16x32_bf16 v[64:67], v[68:71], v[148:151], v[64:67]\n\t"
  "ds_read_b128 v[180:183], v216 offset:7168\n\t"
  "v_exp_f32 v117, v117\n\t"
  "v_exp_f32 v118, v118\n\t"
  "v_exp_f32 v119, v119\n\t"
  "v_cvt_pk_bf16_f32 v142, v116, v117\n\t"
  "v_cvt_pk_bf16_f32 v143, v118, v119\n\t"
  "s_cmp_eq_u32 s53, s52\n\t"
  "s_cbranch_scc1 LATepi_%=\n\t"
  "s_cmp_eq_u32 s58, 0\n\t"
  "s_cbranch_scc1 LATnext_%=\n\t"
  "s_nop 7\n\t"
  "s_nop 3\n\t"
  "v_mul_f32 v0, v221, v0\n\t"
  "v_mul_f32 v1, v221, v1\n\t"
  "v_mul_f32 v2, v221, v2\n\t"
  "v_mul_f32 v3, v221, v3\n\t"
  "v_mul_f32 v4, v221, v4\n\t"
  "v_mul_f32 v5, v221, v5\n\t"
  "v_mul_f32 v6, v221, v6\n\t"
  "v_mul_f32 v7, v221, v7\n\t"
  "v_mul_f32 v8, v221, v8\n\t"
  "v_mul_f32 v9, v221, v9\n\t"
  "v_mul_f32 v10, v221, v10\n\t"
  "v_mul_f32 v11, v221, v11\n\t"
  "v_mul_f32 v12, v221, v12\n\t"
  "v_mul_f32 v13, v221, v13\n\t"
  "v_mul_f32 v14, v221, v14\n\t"
  "v_mul_f32 v15, v221, v15\n\t"
  "v_mul_f32 v16, v221, v16\n\t"
  "v_mul_f32 v17, v221, v17\n\t"
  "v_mul_f32 v18, v221, v18\n\t"
  "v_mul_f32 v19, v221, v19\n\t"
  "v_mul_f32 v20, v221, v20\n\t"
  "v_mul_f32 v21, v221, v21\n\t"
  "v_mul_f32 v22, v221, v22\n\t"
  "v_mul_f32 v23, v221, v23\n\t"
  "v_mul_f32 v24, v221, v24\n\t"
  "v_mul_f32 v25, v221, v25\n\t"
  "v_mul_f32 v26, v221, v26\n\t"
  "v_mul_f32 v27, v221, v27\n\t"
  "v_mul_f32 v28, v221, v28\n\t"
  "v_mul_f32 v29, v221, v29\n\t"
  "v_mul_f32 v30, v221, v30\n\t"
  "v_mul_f32 v31, v221, v31\n\t"
  "v_mul_f32 v32, v221, v32\n\t"
  "v_mul_f32 v33, v221, v33\n\t"
  "v_mul_f32 v34, v221, v34\n\t"
  "v_mul_f32 v35, v221, v35\n\t"
  "v_mul_f32 v36, v221, v36\n\t"
  "v_mul_f32 v37, v221, v37\n\t"
  "v_mul_f32 v38, v221, v38\n\t"
  "v_mul_f32 v39, v221, v39\n\t"
  "v_mul_f32 v40, v221, v40\n\t"
  "v_mul_f32 v41, v221, v41\n\t"
  "v_mul_f32 v42, v221, v42\n\t"
  "v_mul_f32 v43, v221, v43\n\t"
  "v_mul_f32 v44, v221, v44\n\t"
  "v_mul_f32 v45, v221, v45\n\t"
  "v_mul_f32 v46, v221, v46\n\t"
  "v_mul_f32 v47, v221, v47\n\t"
  "v_mul_f32 v48, v221, v48\n\t"
  "v_mul_f32 v49, v221, v49\n\t"
  "v_mul_f32 v50, v221, v50\n\t"
  "v_mul_f32 v51, v221, v51\n\t"
  "v_mul_f32 v52, v221, v52\n\t"
  "v_mul_f32 v53, v221, v53\n\t"
  "v_mul_f32 v54, v221, v54\n\t"
  "v_mul_f32 v55, v221, v55\n\t"
  "v_mul_f32 v56, v221, v56\n\t"
  "v_mul_f32 v57, v221, v57\n\t"
  "v_mul_f32 v58, v221, v58\n\t"
  "v_mul_f32 v59, v221, v59\n\t"
  "v_mul_f32 v60, v221, v60\n\t"
  "v_mul_f32 v61, v221, v61\n\t"
  "v_mul_f32 v62, v221, v62\n\t"
  "v_mul_f32 v63, v221, v63\n\t"
  "v_mul_f32 v64, v221, v64\n\t"
  "v_mul_f32 v65, v221, v65\n\t"
  "v_mul_f32 v66, v221, v66\n\t"
  "v_mul_f32 v67, v221, v67\n\t"
  "s_mov_b32 s58, 0\n\t"
  "s_branch LATnext_%=\n\t"
  "LATfinal_%=:\n\t"
  "ds_read_b128 v[184:187], v217 offset:0\n\t"
  "ds_read_b128 v[156:159], v217 offset:1024\n\t"
  "ds_read_b128 v[160:163], v217 offset:2048\n\t"
  "ds_read_b128 v[164:167], v217 offset:3072\n\t"
  "v_fma_f32 v120, v120, s67, v218\n\t"
  "v_fma_f32 v121, v121, s67, v218\n\t"
  "v_fma_f32 v122, v122, s67, v218\n\t"
  "v_fma_f32 v123, v123, s67, v218\n\t"
  "v_exp_f32 v120, v120\n\t"
  "v_exp_f32 v121, v121\n\t"
  "s_waitcnt lgkmcnt(3)\n\t"
  "v_mfma_f32_32x32x16_bf16 v[0:15], v[184:187], v[136:139], v[0:15]\n\t"
  "ds_read_b128 v[168:171], v217 offset:4096\n\t"
  "v_exp_f32 v122, v122\n\t"
  "v_exp_f32 v123, v123\n\t"
  "v_fma_f32 v124, v124, s67, v218\n\t"
  "s_waitcnt lgkmcnt(3)\n\t"
  "v_mfma_f32_32x32x16_bf16 v[16:31], v[156:159], v[136:139], v[16:31]\n\t"
  "ds_read_b128 v[172:175], v217 offset:5120\n\t"
  "v_fma_f32 v125, v125, s67, v218\n\t"
  "v_fma_f32 v126, v126, s67, v218\n\t"
  "v_fma_f32 v127, v127, s67, v218\n\t"
  "v_cvt_pk_bf16_f32 v144, v120, v121\n\t"
  "v_cvt_pk_bf16_f32 v145, v122, v123\n\t"
  "s_waitcnt lgkmcnt(3)\n\t"
  "v_mfma_f32_32x32x16_bf16 v[32:47], v[160:163], v[136:139], v[32:47]\n\t"
  "ds_read_b128 v[176:179], v217 offset:6144\n\t"
  "v_exp_f32 v124, v124\n\t"
  "v_exp_f32 v125, v125\n\t"
  "v_exp_f32 v126, v126\n\t"
  "s_waitcnt lgkmcnt(3)\n\t"
  "v_mfma_f32_32x32x16_bf16 v[48:63], v[164:167], v[136:139], v[48:63]\n\t"
  "ds_read_b128 v[180:183], v217 offset:7168\n\t"
  "v_exp_f32 v127, v127\n\t"
  "v_fma_f32 v128, v128, s67, v218\n\t"
  "v_fma_f32 v129, v129, s67, v218\n\t"
  "v_mfma_f32_16x16x32_bf16 v[64:67], v[68:71], v[136:139], v[64:67]\n\t"
  "ds_read_b128 v[184:187], v217 offset:8192\n\t"
  "v_fma_f32 v130, v130, s67, v218\n\t"
  "v_fma_f32 v131, v131, s67, v218\n\t"
  "v_cvt_pk_bf16_f32 v146, v124, v125\n\t"
  "v_cvt_pk_bf16_f32 v147, v126, v127\n\t"
  "v_exp_f32 v128, v128\n\t"
  "s_waitcnt lgkmcnt(3)\n\t"
  "v_mfma_f32_32x32x16_bf16 v[0:15], v[168:171], v[140:143], v[0:15]\n\t"
  "ds_read_b128 v[188:191], v217 offset:9216\n\t"
  "v_exp_f32 v129, v129\n\t"
  "v_exp_f32 v130, v130\n\t"
  "v_mfma_f32_32x32x16_bf16 v[16:31], v[172:175], v[140:143], v[16:31]\n\t"
  "ds_read_b128 v[192:195], v217 offset:10240\n\t"
  "v_exp_f32 v131, v131\n\t"
  "v_fma_f32 v132, v132, s67, v218\n\t"
  "v_fma_f32 v133, v133, s67, v218\n\t"
  "v_fma_f32 v134, v134, s67, v218\n\t"
  "s_waitcnt lgkmcnt(3)\n\t"
  "v_mfma_f32_32x32x16_bf16 v[32:47], v[176:179], v[140:143], v[32:47]\n\t"
  "ds_read_b128 v[196:199], v217 offset:11264\n\t"
  "v_fma_f32 v135, v135, s67, v218\n\t"
  "v_cvt_pk_bf16_f32 v148, v128, v129\n\t"
  "v_cvt_pk_bf16_f32 v149, v130, v131\n\t"
  "v_exp_f32 v132, v132\n\t"
  "v_mfma_f32_32x32x16_bf16 v[48:63], v[180:183], v[140:143], v[48:63]\n\t"
  "ds_read_b128 v[200:203], v217 offset:12288\n\t"
  "v_exp_f32 v133, v133\n\t"
  "v_exp_f32 v134, v134\n\t"
  "v_exp_f32 v135, v135\n\t"
  "v_mfma_f32_16x16x32_bf16 v[64:67], v[68:71], v[140:143], v[64:67]\n\t"
  "ds_read_b128 v[204:207], v217 offset:13312\n\t"
  "v_cvt_pk_bf16_f32 v150, v132, v133\n\t"
  "v_cvt_pk_bf16_f32 v151, v134, v135\n\t"
  "s_waitcnt lgkmcnt(3)\n\t"
  "v_mfma_f32_32x32x16_bf16 v[0:15], v[184:187], v[144:147], v[0:15]\n\t"
  "ds_read_b128 v[208:211], v217 offset:14336\n\t"
  "v_mfma_f32_32x32x16_bf16 v[16:31], v[188:191], v[144:147], v[16:31]\n\t"
  "ds_read_b128 v[212:215], v217 offset:15360\n\t"
  "v_mfma_f32_32x32x16_bf16 v[32:47], v[192:195], v[144:147], v[32:47]\n\t"
  "s_waitcnt lgkmcnt(3)\n\t"
  "v_mfma_f32_32x32x16_bf16 v[48:63], v[196:199], v[144:147], v[48:63]\n\t"
  "v_mfma_f32_16x16x32_bf16 v[64:67], v[68:71], v[144:147], v[64:67]\n\t"
  "v_mfma_f32_32x32x16_bf16 v[0:15], v[200:203], v[148:151], v[0:15]\n\t"
  "s_waitcnt lgkmcnt(2)\n\t"
  "v_mfma_f32_32x32x16_bf16 v[16:31], v[204:207], v[148:151], v[16:31]\n\t"
  "s_waitcnt lgkmcnt(1)\n\t"
  "v_mfma_f32_32x32x16_bf16 v[32:47], v[208:211], v[148:151], v[32:47]\n\t"
  "s_waitcnt lgkmcnt(0)\n\t"
  "v_mfma_f32_32x32x16_bf16 v[48:63], v[212:215], v[148:151], v[48:63]\n\t"
  "v_mfma_f32_16x16x32_bf16 v[64:67], v[68:71], v[148:151], v[64:67]\n\t"
  "s_mov_b32 s64, 1\n\t"
  "v_mov_b32 v226, v218\n\t"
  "LATepi_%=:\n\t"
  "s_nop 7\n\t"
  "s_nop 3\n\t"
  "v_mov_b32 v227, v64\n\t"
  "v_lshrrev_b32 v230, 1, v223\n\t"
  "v_add_u32 v230, s65, v230\n\t"
  "v_add_u32 v231, s56, v223\n\t"
  "v_add_u32 v232, s57, v223\n\t"
  "ds_write_b64 v230, v[226:227]\n\t"
  "ds_write_b128 v231, v[32:35] offset:0\n\t"
  "ds_write_b128 v231, v[36:39] offset:1024\n\t"
  "ds_write_b128 v231, v[40:43] offset:2048\n\t"
  "ds_write_b128 v231, v[44:47] offset:3072\n\t"
  "ds_write_b128 v232, v[48:51] offset:0\n\t"
  "ds_write_b128 v232, v[52:55] offset:1024\n\t"
  "ds_write_b128 v232, v[56:59] offset:2048\n\t"
  "ds_write_b128 v232, v[60:63] offset:3072\n\t"
  "v_xor_b32 v230, 0xe00, v230\n\t"
  "v_xor_b32 v231, 0x7000, v231\n\t"
  "v_xor_b32 v232, 0x7000, v232\n\t"
  "s_waitcnt lgkmcnt(0)\n\t"
  "s_barrier\n\t"
  "ds_read_b64 v[228:229], v230\n\t"
  "ds_read_b128 v[184:187], v231 offset:0\n\t"
  "ds_read_b128 v[188:191], v231 offset:1024\n\t"
  "ds_read_b128 v[192:195], v231 offset:2048\n\t"
  "ds_read_b128 v[196:199], v231 offset:3072\n\t"
  "ds_read_b128 v[200:203], v232 offset:0\n\t"
  "ds_read_b128 v[204:207], v232 offset:1024\n\t"
  "ds_read_b128 v[208:211], v232 offset:2048\n\t"
  "ds_read_b128 v[212:215], v232 offset:3072\n\t"
  "s_waitcnt lgkmcnt(8)\n\t"
  "v_min_f32 v230, v226, v228\n\t"
  "v_sub_f32 v231, v230, v226\n\t"
  "v_sub_f32 v232, v230, v228\n\t"
  "v_exp_f32 v231, v231\n\t"
  "v_exp_f32 v232, v232\n\t"
  "s_nop 0\n\t"
  "v_mul_f32 v230, v231, v227\n\t"
  "v_fma_f32 v230, v232, v229, v230\n\t"
  "v_rcp_f32 v230, v230\n\t"
  "s_nop 0\n\t"
  "v_mul_f32 v231, v231, v230\n\t"
  "v_mul_f32 v232, v232, v230\n\t"
  "s_waitcnt lgkmcnt(0)\n\t"
  "s_barrier\n\t"
  "s_cmp_eq_u32 s64, 0\n\t"
  "s_cbranch_scc1 LATnoearly_%=\n\t"
  "s_lshl_b32 s22, s30, 11\n\t"
  "s_mul_i32 s23, s30, 1280\n\t"
  "s_add_u32 s23, s23, 16384\n\t"
  "s_mul_i32 s34, s30, 3072\n\t"
  "s_add_u32 s25, s24, 327680\n\t"
  "s_mov_b32 s36, 0\n\t"
  "s_add_u32 m0, s23, 0\n\t"
  "s_nop 0\n\t"
  "buffer_load_dwordx4 v240, s[4:7], s21 offen lds\n\t"
  "s_mov_b64 exec, 0xffff\n\t"
  "buffer_load_dwordx4 v241, s[4:7], s21 offen offset:1024 lds\n\t"
  "s_mov_b64 exec, -1\n\t"
  "s_add_u32 s21, s21, 40960\n\t"
  "s_add_u32 m0, s23, 26624\n\t"
  "s_nop 0\n\t"
  "buffer_load_dwordx4 v240, s[4:7], s21 offen lds\n\t"
  "s_mov_b64 exec, 0xffff\n\t"
  "buffer_load_dwordx4 v241, s[4:7], s21 offen offset:1024 lds\n\t"
  "s_mov_b64 exec, -1\n\t"
  "s_add_u32 s21, s21, 40960\n\t"
  "s_cmp_ge_u32 s30, 4\n\t"
  "s_cbranch_scc1 LATOskipA_%=\n\t"
  "s_add_u32 m0, s22, 0\n\t"
  "s_nop 0\n\t"
  "buffer_load_dwordx4 v223, s[48:51], s20 offen sc1 lds\n\t"
  "buffer_load_dwordx4 v223, s[48:51], s20 offen offset:1024 sc1 lds\n\t"
  "s_add_u32 s20, s20, 2048\n\t"
  "s_add_u32 m0, s22, 26624\n\t"
  "s_nop 0\n\t"
  "buffer_load_dwordx4 v223, s[48:51], s20 offen sc1 lds\n\t"
  "buffer_load_dwordx4 v223, s[48:51], s20 offen offset:1024 sc1 lds\n\t"
  "s_add_u32 s20, s20, 2048\n\t"
  "s_add_u32 m0, s22, 53248\n\t"
  "s_nop 0\n\t"
  "buffer_load_dwordx4 v223, s[48:51], s20 offen sc1 lds\n\t"
  "buffer_load_dwordx4 v223, s[48:51], s20 offen offset:1024 sc1 lds\n\t"
  "s_add_u32 s20, s20, 2048\n\t"
  "s_add_u32 m0, s22, 79872\n\t"
  "s_nop 0\n\t"
  "buffer_load_dwordx4 v223, s[48:51], s20 offen sc1 lds\n\t"
  "buffer_load_dwordx4 v223, s[48:51], s20 offen offset:1024 sc1 lds\n\t"
  "s_add_u32 s20, s20, 2048\n\t"
  "s_cmp_eq_u32 s20, s26\n\t"
  "s_cselect_b32 s35, 32768, 0\n\t"
  "s_sub_u32 s20, s20, s35\n\t"
  "s_branch LATOdoneA_%=\n\t"
  "LATOskipA_%=:\n\t"
  "s_add_u32 s20, s20, 8192\n\t"
  "s_cmp_eq_u32 s20, s26\n\t"
  "s_cselect_b32 s35, 32768, 0\n\t"
  "s_sub_u32 s20, s20, s35\n\t"
  "LATOdoneA_%=:\n\t"
  "LATnoearly_%=:\n\t"
  "v_mul_f32 v184, v232, v184\n\t"
  "v_mul_f32 v185, v232, v185\n\t"
  "v_mul_f32 v186, v232, v186\n\t"
  "v_mul_f32 v187, v232, v187\n\t"
  "v_mul_f32 v188, v232, v188\n\t"
  "v_mul_f32 v189, v232, v189\n\t"
  "v_mul_f32 v190, v232, v190\n\t"
  "v_mul_f32 v191, v232, v191\n\t"
  "v_mul_f32 v192, v232, v192\n\t"
  "v_mul_f32 v193, v232, v193\n\t"
  "v_mul_f32 v194, v232, v194\n\t"
  "v_mul_f32 v195, v232, v195\n\t"
  "v_mul_f32 v196, v232, v196\n\t"
  "v_mul_f32 v197, v232, v197\n\t"
  "v_mul_f32 v198, v232, v198\n\t"
  "v_mul_f32 v199, v232, v199\n\t"
  "v_fma_f32 v0, v0, v231, v184\n\t"
  "v_fma_f32 v1, v1, v231, v185\n\t"
  "v_fma_f32 v2, v2, v231, v186\n\t"
  "v_fma_f32 v3, v3, v231, v187\n\t"
  "v_fma_f32 v4, v4, v231, v188\n\t"
  "v_fma_f32 v5, v5, v231, v189\n\t"
  "v_fma_f32 v6, v6, v231, v190\n\t"
  "v_fma_f32 v7, v7, v231, v191\n\t"
  "v_fma_f32 v8, v8, v231, v192\n\t"
  "v_fma_f32 v9, v9, v231, v193\n\t"
  "v_fma_f32 v10, v10, v231, v194\n\t"
  "v_fma_f32 v11, v11, v231, v195\n\t"
  "v_fma_f32 v12, v12, v231, v196\n\t"
  "v_fma_f32 v13, v13, v231, v197\n\t"
  "v_fma_f32 v14, v14, v231, v198\n\t"
  "v_fma_f32 v15, v15, v231, v199\n\t"
  "v_cvt_pk_bf16_f32 v184, v0, v1\n\t"
  "v_cvt_pk_bf16_f32 v185, v2, v3\n\t"
  "v_cvt_pk_bf16_f32 v186, v4, v5\n\t"
  "v_cvt_pk_bf16_f32 v187, v6, v7\n\t"
  "v_cvt_pk_bf16_f32 v188, v8, v9\n\t"
  "v_cvt_pk_bf16_f32 v189, v10, v11\n\t"
  "v_cvt_pk_bf16_f32 v190, v12, v13\n\t"
  "v_cvt_pk_bf16_f32 v191, v14, v15\n\t"
  "buffer_store_dwordx4 v[184:187], v223, s[48:51], s60 offen offset:0 sc1\n\t"
  "buffer_store_dwordx4 v[188:191], v223, s[48:51], s60 offen offset:1024 sc1\n\t"
  "v_mul_f32 v200, v232, v200\n\t"
  "v_mul_f32 v201, v232, v201\n\t"
  "v_mul_f32 v202, v232, v202\n\t"
  "v_mul_f32 v203, v232, v203\n\t"
  "v_mul_f32 v204, v232, v204\n\t"
  "v_mul_f32 v205, v232, v205\n\t"
  "v_mul_f32 v206, v232, v206\n\t"
  "v_mul_f32 v207, v232, v207\n\t"
  "v_mul_f32 v208, v232, v208\n\t"
  "v_mul_f32 v209, v232, v209\n\t"
  "v_mul_f32 v210, v232, v210\n\t"
  "v_mul_f32 v211, v232, v211\n\t"
  "v_mul_f32 v212, v232, v212\n\t"
  "v_mul_f32 v213, v232, v213\n\t"
  "v_mul_f32 v214, v232, v214\n\t"
  "v_mul_f32 v215, v232, v215\n\t"
  "v_fma_f32 v16, v16, v231, v200\n\t"
  "v_fma_f32 v17, v17, v231, v201\n\t"
  "v_fma_f32 v18, v18, v231, v202\n\t"
  "v_fma_f32 v19, v19, v231, v203\n\t"
  "v_fma_f32 v20, v20, v231, v204\n\t"
  "v_fma_f32 v21, v21, v231, v205\n\t"
  "v_fma_f32 v22, v22, v231, v206\n\t"
  "v_fma_f32 v23, v23, v231, v207\n\t"
  "v_fma_f32 v24, v24, v231, v208\n\t"
  "v_fma_f32 v25, v25, v231, v209\n\t"
  "v_fma_f32 v26, v26, v231, v210\n\t"
  "v_fma_f32 v27, v27, v231, v211\n\t"
  "v_fma_f32 v28, v28, v231, v212\n\t"
  "v_fma_f32 v29, v29, v231, v213\n\t"
  "v_fma_f32 v30, v30, v231, v214\n\t"
  "v_fma_f32 v31, v31, v231, v215\n\t"
  "v_cvt_pk_bf16_f32 v200, v16, v17\n\t"
  "v_cvt_pk_bf16_f32 v201, v18, v19\n\t"
  "v_cvt_pk_bf16_f32 v202, v20, v21\n\t"
  "v_cvt_pk_bf16_f32 v203, v22, v23\n\t"
  "v_cvt_pk_bf16_f32 v204, v24, v25\n\t"
  "v_cvt_pk_bf16_f32 v205, v26, v27\n\t"
  "v_cvt_pk_bf16_f32 v206, v28, v29\n\t"
  "v_cvt_pk_bf16_f32 v207, v30, v31\n\t"
  "buffer_store_dwordx4 v[200:203], v223, s[48:51], s60 offen offset:2048 sc1\n\t"
  "buffer_store_dwordx4 v[204:207], v223, s[48:51], s60 offen offset:3072 sc1\n\t"
  "s_cmp_lg_u32 s64, 0\n\t"
  "s_cbranch_scc1 LATfin_%=\n\t"
  "s_mov_b32 s60, s61\n\t"
  "v_mov_b32 v0, 0\n\t"
  "v_mov_b32 v1, 0\n\t"
  "v_mov_b32 v2, 0\n\t"
  "v_mov_b32 v3, 0\n\t"
  "v_mov_b32 v4, 0\n\t"
  "v_mov_b32 v5, 0\n\t"
  "v_mov_b32 v6, 0\n\t"
  "v_mov_b32 v7, 0\n\t"
  "v_mov_b32 v8, 0\n\t"
  "v_mov_b32 v9, 0\n\t"
  "v_mov_b32 v10, 0\n\t"
  "v_mov_b32 v11, 0\n\t"
  "v_mov_b32 v12, 0\n\t"
  "v_mov_b32 v13, 0\n\t"
  "v_mov_b32 v14, 0\n\t"
  "v_mov_b32 v15, 0\n\t"
  "v_mov_b32 v16, 0\n\t"
  "v_mov_b32 v17, 0\n\t"
  "v_mov_b32 v18, 0\n\t"
  "v_mov_b32 v19, 0\n\t"
  "v_mov_b32 v20, 0\n\t"
  "v_mov_b32 v21, 0\n\t"
  "v_mov_b32 v22, 0\n\t"
  "v_mov_b32 v23, 0\n\t"
  "v_mov_b32 v24, 0\n\t"
  "v_mov_b32 v25, 0\n\t"
  "v_mov_b32 v26, 0\n\t"
  "v_mov_b32 v27, 0\n\t"
  "v_mov_b32 v28, 0\n\t"
  "v_mov_b32 v29, 0\n\t"
  "v_mov_b32 v30, 0\n\t"
  "v_mov_b32 v31, 0\n\t"
  "v_mov_b32 v32, 0\n\t"
  "v_mov_b32 v33, 0\n\t"
  "v_mov_b32 v34, 0\n\t"
  "v_mov_b32 v35, 0\n\t"
  "v_mov_b32 v36, 0\n\t"
  "v_mov_b32 v37, 0\n\t"
  "v_mov_b32 v38, 0\n\t"
  "v_mov_b32 v39, 0\n\t"
  "v_mov_b32 v40, 0\n\t"
  "v_mov_b32 v41, 0\n\t"
  "v_mov_b32 v42, 0\n\t"
  "v_mov_b32 v43, 0\n\t"
  "v_mov_b32 v44, 0\n\t"
  "v_mov_b32 v45, 0\n\t"
  "v_mov_b32 v46, 0\n\t"
  "v_mov_b32 v47, 0\n\t"
  "v_mov_b32 v48, 0\n\t"
  "v_mov_b32 v49, 0\n\t"
  "v_mov_b32 v50, 0\n\t"
  "v_mov_b32 v51, 0\n\t"
  "v_mov_b32 v52, 0\n\t"
  "v_mov_b32 v53, 0\n\t"
  "v_mov_b32 v54, 0\n\t"
  "v_mov_b32 v55, 0\n\t"
  "v_mov_b32 v56, 0\n\t"
  "v_mov_b32 v57, 0\n\t"
  "v_mov_b32 v58, 0\n\t"
  "v_mov_b32 v59, 0\n\t"
  "v_mov_b32 v60, 0\n\t"
  "v_mov_b32 v61, 0\n\t"
  "v_mov_b32 v62, 0\n\t"
  "v_mov_b32 v63, 0\n\t"
  "v_mov_b32 v64, 0\n\t"
  "v_mov_b32 v65, 0\n\t"
  "v_mov_b32 v66, 0\n\t"
  "v_mov_b32 v67, 0\n\t"
  "s_mov_b32 s58, 0\n\t"
  "s_branch LATnext_%=\n\t"
  "LATfin_%=:\n\t"
  "v_add_u32 v230, s80, v223\n\t"
  "ds_write_b128 v230, v[184:187] offset:0\n\t"
  "ds_write_b128 v230, v[188:191] offset:1024\n\t"
  "ds_write_b128 v230, v[200:203] offset:26624\n\t"
  "ds_write_b128 v230, v[204:207] offset:27648\n\t"
  "s_branch LATend_%=\n\t"
  "LATmaskA_f_%=:\n\t"
  "v_cmp_gt_i32 s[72:73], 0, v222\n\t"
  "v_cmp_gt_i32 s[74:75], 1, v222\n\t"
  "v_cmp_gt_i32 s[76:77], 2, v222\n\t"
  "v_cmp_gt_i32 s[78:79], 3, v222\n\t"
  "v_cndmask_b32 v104, v104, v224, s[72:73]\n\t"
  "v_cndmask_b32 v105, v105, v224, s[74:75]\n\t"
  "v_cndmask_b32 v106, v106, v224, s[76:77]\n\t"
  "v_cndmask_b32 v107, v107, v224, s[78:79]\n\t"
  "v_cmp_gt_i32 s[72:73], 8, v222\n\t"
  "v_cmp_gt_i32 s[74:75], 9, v222\n\t"
  "v_cmp_gt_i32 s[76:77], 10, v222\n\t"
  "v_cmp_gt_i32 s[78:79], 11, v222\n\t"
  "v_cndmask_b32 v108, v108, v224, s[72:73]\n\t"
  "v_cndmask_b32 v109, v109, v224, s[74:75]\n\t"
  "v_cndmask_b32 v110, v110, v224, s[76:77]\n\t"
  "v_cndmask_b32 v111, v111, v224, s[78:79]\n\t"
  "v_cmp_gt_i32 s[72:73], 16, v222\n\t"
  "v_cmp_gt_i32 s[74:75], 17, v222\n\t"
  "v_cmp_gt_i32 s[76:77], 18, v222\n\t"
  "v_cmp_gt_i32 s[78:79], 19, v222\n\t"
  "v_cndmask_b32 v112, v112, v224, s[72:73]\n\t"
  "v_cndmask_b32 v113, v113, v224, s[74:75]\n\t"
  "v_cndmask_b32 v114, v114, v224, s[76:77]\n\t"
  "v_cndmask_b32 v115, v115, v224, s[78:79]\n\t"
  "v_cmp_gt_i32 s[72:73], 24, v222\n\t"
  "v_cmp_gt_i32 s[74:75], 25, v222\n\t"
  "v_cmp_gt_i32 s[76:77], 26, v222\n\t"
  "v_cmp_gt_i32 s[78:79], 27, v222\n\t"
  "v_cndmask_b32 v116, v116, v224, s[72:73]\n\t"
  "v_cndmask_b32 v117, v117, v224, s[74:75]\n\t"
  "v_cndmask_b32 v118, v118, v224, s[76:77]\n\t"
  "v_cndmask_b32 v119, v119, v224, s[78:79]\n\t"
  "s_branch LATmaskA_ret_f_%=\n\t"
  "LATmaskB_f_%=:\n\t"
  "v_cmp_gt_i32 s[72:73], 32, v222\n\t"
  "v_cmp_gt_i32 s[74:75], 33, v222\n\t"
  "v_cmp_gt_i32 s[76:77], 34, v222\n\t"
  "v_cmp_gt_i32 s[78:79], 35, v222\n\t"
  "v_cndmask_b32 v120, v120, v224, s[72:73]\n\t"
  "v_cndmask_b32 v121, v121, v224, s[74:75]\n\t"
  "v_cndmask_b32 v122, v122, v224, s[76:77]\n\t"
  "v_cndmask_b32 v123, v123, v224, s[78:79]\n\t"
  "v_cmp_gt_i32 s[72:73], 40, v222\n\t"
  "v_cmp_gt_i32 s[74:75], 41, v222\n\t"
  "v_cmp_gt_i32 s[76:77], 42, v222\n\t"
  "v_cmp_gt_i32 s[78:79], 43, v222\n\t"
  "v_cndmask_b32 v124, v124, v224, s[72:73]\n\t"
  "v_cndmask_b32 v125, v125, v224, s[74:75]\n\t"
  "v_cndmask_b32 v126, v126, v224, s[76:77]\n\t"
  "v_cndmask_b32 v127, v127, v224, s[78:79]\n\t"
  "v_cmp_gt_i32 s[72:73], 48, v222\n\t"
  "v_cmp_gt_i32 s[74:75], 49, v222\n\t"
  "v_cmp_gt_i32 s[76:77], 50, v222\n\t"
  "v_cmp_gt_i32 s[78:79], 51, v222\n\t"
  "v_cndmask_b32 v128, v128, v224, s[72:73]\n\t"
  "v_cndmask_b32 v129, v129, v224, s[74:75]\n\t"
  "v_cndmask_b32 v130, v130, v224, s[76:77]\n\t"
  "v_cndmask_b32 v131, v131, v224, s[78:79]\n\t"
  "v_cmp_gt_i32 s[72:73], 56, v222\n\t"
  "v_cmp_gt_i32 s[74:75], 57, v222\n\t"
  "v_cmp_gt_i32 s[76:77], 58, v222\n\t"
  "v_cmp_gt_i32 s[78:79], 59, v222\n\t"
  "v_cndmask_b32 v132, v132, v224, s[72:73]\n\t"
  "v_cndmask_b32 v133, v133, v224, s[74:75]\n\t"
  "v_cndmask_b32 v134, v134, v224, s[76:77]\n\t"
  "v_cndmask_b32 v135, v135, v224, s[78:79]\n\t"
  "s_branch LATmaskB_ret_f_%=\n\t"
  "LATrareA_f_%=:\n\t"
  "v_mov_b32 v228, v219\n\t"
  "v_mov_b32 v229, v219\n\t"
  "s_nop 1\n\t"
  "v_permlane32_swap_b32 v228, v229\n\t"
  "s_nop 1\n\t"
  "v_max_f32 v228, v228, v229\n\t"
  "v_mul_f32 v228, s69, v228\n\t"
  "v_min_f32 v229, v228, v218\n\t"
  "v_sub_f32 v228, v229, v218\n\t"
  "v_exp_f32 v221, v228\n\t"
  "v_mov_b32 v218, v229\n\t"
  "s_mov_b32 s58, 1\n\t"
  "s_branch LATrareA_ret_f_%=\n\t"
  "LATqb_f_%=:\n\t"
  "ds_read_b128 v[72:75], v225 offset:0\n\t"
  "ds_read_b128 v[76:79], v225 offset:1024\n\t"
  "ds_read_b128 v[80:83], v225 offset:2048\n\t"
  "ds_read_b128 v[84:87], v225 offset:3072\n\t"
  "ds_read_b128 v[88:91], v225 offset:4096\n\t"
  "ds_read_b128 v[92:95], v225 offset:5120\n\t"
  "ds_read_b128 v[96:99], v225 offset:6144\n\t"
  "ds_read_b128 v[100:103], v225 offset:7168\n\t"
  "s_branch LATqb_ret_f_%=\n\t"
  "LATpf_%=:\n\t"
  "global_load_dword v236, v[234:235], off\n\t"
  "s_branch LATpf_ret_%=\n\t"
  "LATseam_s_%=:\n\t"
  "v_mov_b32 v226, v218\n\t"
  "v_mov_b32 v218, 0x7149f2ca\n\t"
  "s_branch LATseam_ret_s_%=\n\t"
  "LATmaskA_s_%=:\n\t"
  "v_cmp_gt_i32 s[72:73], 0, v222\n\t"
  "v_cmp_gt_i32 s[74:75], 1, v222\n\t"
  "v_cmp_gt_i32 s[76:77], 2, v222\n\t"
  "v_cmp_gt_i32 s[78:79], 3, v222\n\t"
  "v_cndmask_b32 v104, v104, v224, s[72:73]\n\t"
  "v_cndmask_b32 v105, v105, v224, s[74:75]\n\t"
  "v_cndmask_b32 v106, v106, v224, s[76:77]\n\t"
  "v_cndmask_b32 v107, v107, v224, s[78:79]\n\t"
  "v_cmp_gt_i32 s[72:73], 8, v222\n\t"
  "v_cmp_gt_i32 s[74:75], 9, v222\n\t"
  "v_cmp_gt_i32 s[76:77], 10, v222\n\t"
  "v_cmp_gt_i32 s[78:79], 11, v222\n\t"
  "v_cndmask_b32 v108, v108, v224, s[72:73]\n\t"
  "v_cndmask_b32 v109, v109, v224, s[74:75]\n\t"
  "v_cndmask_b32 v110, v110, v224, s[76:77]\n\t"
  "v_cndmask_b32 v111, v111, v224, s[78:79]\n\t"
  "v_cmp_gt_i32 s[72:73], 16, v222\n\t"
  "v_cmp_gt_i32 s[74:75], 17, v222\n\t"
  "v_cmp_gt_i32 s[76:77], 18, v222\n\t"
  "v_cmp_gt_i32 s[78:79], 19, v222\n\t"
  "v_cndmask_b32 v112, v112, v224, s[72:73]\n\t"
  "v_cndmask_b32 v113, v113, v224, s[74:75]\n\t"
  "v_cndmask_b32 v114, v114, v224, s[76:77]\n\t"
  "v_cndmask_b32 v115, v115, v224, s[78:79]\n\t"
  "v_cmp_gt_i32 s[72:73], 24, v222\n\t"
  "v_cmp_gt_i32 s[74:75], 25, v222\n\t"
  "v_cmp_gt_i32 s[76:77], 26, v222\n\t"
  "v_cmp_gt_i32 s[78:79], 27, v222\n\t"
  "v_cndmask_b32 v116, v116, v224, s[72:73]\n\t"
  "v_cndmask_b32 v117, v117, v224, s[74:75]\n\t"
  "v_cndmask_b32 v118, v118, v224, s[76:77]\n\t"
  "v_cndmask_b32 v119, v119, v224, s[78:79]\n\t"
  "s_branch LATmaskA_ret_s_%=\n\t"
  "LATmaskB_s_%=:\n\t"
  "v_cmp_gt_i32 s[72:73], 32, v222\n\t"
  "v_cmp_gt_i32 s[74:75], 33, v222\n\t"
  "v_cmp_gt_i32 s[76:77], 34, v222\n\t"
  "v_cmp_gt_i32 s[78:79], 35, v222\n\t"
  "v_cndmask_b32 v120, v120, v224, s[72:73]\n\t"
  "v_cndmask_b32 v121, v121, v224, s[74:75]\n\t"
  "v_cndmask_b32 v122, v122, v224, s[76:77]\n\t"
  "v_cndmask_b32 v123, v123, v224, s[78:79]\n\t"
  "v_cmp_gt_i32 s[72:73], 40, v222\n\t"
  "v_cmp_gt_i32 s[74:75], 41, v222\n\t"
  "v_cmp_gt_i32 s[76:77], 42, v222\n\t"
  "v_cmp_gt_i32 s[78:79], 43, v222\n\t"
  "v_cndmask_b32 v124, v124, v224, s[72:73]\n\t"
  "v_cndmask_b32 v125, v125, v224, s[74:75]\n\t"
  "v_cndmask_b32 v126, v126, v224, s[76:77]\n\t"
  "v_cndmask_b32 v127, v127, v224, s[78:79]\n\t"
  "v_cmp_gt_i32 s[72:73], 48, v222\n\t"
  "v_cmp_gt_i32 s[74:75], 49, v222\n\t"
  "v_cmp_gt_i32 s[76:77], 50, v222\n\t"
  "v_cmp_gt_i32 s[78:79], 51, v222\n\t"
  "v_cndmask_b32 v128, v128, v224, s[72:73]\n\t"
  "v_cndmask_b32 v129, v129, v224, s[74:75]\n\t"
  "v_cndmask_b32 v130, v130, v224, s[76:77]\n\t"
  "v_cndmask_b32 v131, v131, v224, s[78:79]\n\t"
  "v_cmp_gt_i32 s[72:73], 56, v222\n\t"
  "v_cmp_gt_i32 s[74:75], 57, v222\n\t"
  "v_cmp_gt_i32 s[76:77], 58, v222\n\t"
  "v_cmp_gt_i32 s[78:79], 59, v222\n\t"
  "v_cndmask_b32 v132, v132, v224, s[72:73]\n\t"
  "v_cndmask_b32 v133, v133, v224, s[74:75]\n\t"
  "v_cndmask_b32 v134, v134, v224, s[76:77]\n\t"
  "v_cndmask_b32 v135, v135, v224, s[78:79]\n\t"
  "s_branch LATmaskB_ret_s_%=\n\t"
  "LATrareA_s_%=:\n\t"
  "v_mov_b32 v228, v219\n\t"
  "v_mov_b32 v229, v219\n\t"
  "s_nop 1\n\t"
  "v_permlane32_swap_b32 v228, v229\n\t"
  "s_nop 1\n\t"
  "v_max_f32 v228, v228, v229\n\t"
  "v_mul_f32 v228, s69, v228\n\t"
  "v_min_f32 v229, v228, v218\n\t"
  "v_sub_f32 v228, v229, v218\n\t"
  "v_exp_f32 v221, v228\n\t"
  "v_mov_b32 v218, v229\n\t"
  "s_mov_b32 s58, 1\n\t"
  "s_branch LATrareA_ret_s_%=\n\t"
  "LATqb_s_%=:\n\t"
  "ds_read_b128 v[72:75], v225 offset:0\n\t"
  "ds_read_b128 v[76:79], v225 offset:1024\n\t"
  "ds_read_b128 v[80:83], v225 offset:2048\n\t"
  "ds_read_b128 v[84:87], v225 offset:3072\n\t"
  "ds_read_b128 v[88:91], v225 offset:4096\n\t"
  "ds_read_b128 v[92:95], v225 offset:5120\n\t"
  "ds_read_b128 v[96:99], v225 offset:6144\n\t"
  "ds_read_b128 v[100:103], v225 offset:7168\n\t"
  "s_branch LATqb_ret_s_%=\n\t"
  "LATend_%=:\n\t"
  "s_add_u32 m0, s23, 53248\n\t"
  "s_nop 0\n\t"
  "buffer_load_dwordx4 v240, s[4:7], s21 offen lds\n\t"
  "s_mov_b64 exec, 0xffff\n\t"
  "buffer_load_dwordx4 v241, s[4:7], s21 offen offset:1024 lds\n\t"
  "s_mov_b64 exec, -1\n\t"
  "s_add_u32 s21, s21, 40960\n\t"
  "s_add_u32 m0, s23, 79872\n\t"
  "s_nop 0\n\t"
  "buffer_load_dwordx4 v240, s[4:7], s21 offen lds\n\t"
  "s_mov_b64 exec, 0xffff\n\t"
  "buffer_load_dwordx4 v241, s[4:7], s21 offen offset:1024 lds\n\t"
  "s_mov_b64 exec, -1\n\t"
  "s_add_u32 s21, s21, 40960\n\t"
  "s_cmp_eq_u32 s21, s27\n\t"
  "s_cselect_b32 s35, 655360, 0\n\t"
  "s_sub_u32 s21, s21, s35\n\t"
  "s_add_u32 m0, s23, 106496\n\t"
  "s_nop 0\n\t"
  "buffer_load_dwordx4 v240, s[4:7], s21 offen lds\n\t"
  "s_mov_b64 exec, 0xffff\n\t"
  "buffer_load_dwordx4 v241, s[4:7], s21 offen offset:1024 lds\n\t"
  "s_mov_b64 exec, -1\n\t"
  "s_add_u32 s21, s21, 40960\n\t"
  "s_add_u32 m0, s23, 133120\n\t"
  "s_nop 0\n\t"
  "buffer_load_dwordx4 v240, s[4:7], s21 offen lds\n\t"
  "s_mov_b64 exec, 0xffff\n\t"
  "buffer_load_dwordx4 v241, s[4:7], s21 offen offset:1024 lds\n\t"
  "s_mov_b64 exec, -1\n\t"
  "s_add_u32 s21, s21, 40960\n\t"
  "s_and_b32 s37, s29, 0xffffff80\n\t"
  "v_lshrrev_b32 v155, 2, v223\n\t"
  "v_and_b32 v155, 12, v155\n\t"
  "v_mov_b32 v164, s28\n\t"
  "v_mov_b32 v165, 0\n\t"
  "s_waitcnt vmcnt(12) lgkmcnt(0)\n\t"
  "s_barrier\n\t"
  "v_add_u32 v152, 0, v237\n\t"
  "v_add_u32 v153, 0, v238\n\t"
  "v_add_u32 v154, 0, v239\n\t"
  "ds_read_b128 v[80:83], v152 offset:0\n\t"
  "ds_read_b64_tr_b16 v[112:113], v153 offset:0\n\t"
  "ds_read_b64_tr_b16 v[114:115], v153 offset:512\n\t"
  "ds_read_b64_tr_b16 v[116:117], v154 offset:0\n\t"
  "ds_read_b64_tr_b16 v[118:119], v154 offset:512\n\t"
  "ds_read_b128 v[84:87], v152 offset:256\n\t"
  "ds_read_b64_tr_b16 v[120:121], v153 offset:1024\n\t"
  "ds_read_b64_tr_b16 v[122:123], v153 offset:1536\n\t"
  "ds_read_b64_tr_b16 v[124:125], v154 offset:1024\n\t"
  "ds_read_b64_tr_b16 v[126:127], v154 offset:1536\n\t"
  "ds_read_b128 v[88:91], v152 offset:2048\n\t"
  "ds_read_b64_tr_b16 v[128:129], v153 offset:2048\n\t"
  "ds_read_b64_tr_b16 v[130:131], v153 offset:2560\n\t"
  "ds_read_b128 v[92:95], v152 offset:2304\n\t"
  "s_waitcnt vmcnt(12) lgkmcnt(0)\n\t"
  "s_barrier\n\t"
  "v_add_u32 v152, 26624, v237\n\t"
  "v_add_u32 v153, 26624, v238\n\t"
  "v_add_u32 v154, 26624, v239\n\t"
  "v_mfma_f32_16x16x32_bf16 v[0:3], v[112:115], v[80:83], 0\n\t"
  "ds_read_b128 v[96:99], v152 offset:0\n\t"
  "v_mfma_f32_16x16x32_bf16 v[4:7], v[116:119], v[80:83], 0\n\t"
  "ds_read_b64_tr_b16 v[132:133], v153 offset:0\n\t"
  "v_mfma_f32_16x16x32_bf16 v[8:11], v[120:123], v[80:83], 0\n\t"
  "ds_read_b64_tr_b16 v[134:135], v153 offset:512\n\t"
  "v_mfma_f32_16x16x32_bf16 v[12:15], v[124:127], v[80:83], 0\n\t"
  "ds_read_b64_tr_b16 v[136:137], v154 offset:0\n\t"
  "v_mfma_f32_16x16x32_bf16 v[16:19], v[128:131], v[80:83], 0\n\t"
  "ds_read_b64_tr_b16 v[138:139], v154 offset:512\n\t"
  "v_mfma_f32_16x16x32_bf16 v[20:23], v[112:115], v[84:87], 0\n\t"
  "ds_read_b128 v[100:103], v152 offset:256\n\t"
  "v_mfma_f32_16x16x32_bf16 v[24:27], v[116:119], v[84:87], 0\n\t"
  "ds_read_b64_tr_b16 v[140:141], v153 offset:1024\n\t"
  "v_mfma_f32_16x16x32_bf16 v[28:31], v[120:123], v[84:87], 0\n\t"
  "ds_read_b64_tr_b16 v[142:143], v153 offset:1536\n\t"
  "v_mfma_f32_16x16x32_bf16 v[32:35], v[124:127], v[84:87], 0\n\t"
  "ds_read_b64_tr_b16 v[144:145], v154 offset:1024\n\t"
  "v_mfma_f32_16x16x32_bf16 v[36:39], v[128:131], v[84:87], 0\n\t"
  "ds_read_b64_tr_b16 v[146:147], v154 offset:1536\n\t"
  "v_mfma_f32_16x16x32_bf16 v[40:43], v[112:115], v[88:91], 0\n\t"
  "ds_read_b128 v[104:107], v152 offset:2048\n\t"
  "v_mfma_f32_16x16x32_bf16 v[44:47], v[116:119], v[88:91], 0\n\t"
  "ds_read_b64_tr_b16 v[148:149], v153 offset:2048\n\t"
  "v_mfma_f32_16x16x32_bf16 v[48:51], v[120:123], v[88:91], 0\n\t"
  "ds_read_b64_tr_b16 v[150:151], v153 offset:2560\n\t"
  "v_mfma_f32_16x16x32_bf16 v[52:55], v[124:127], v[88:91], 0\n\t"
  "ds_read_b128 v[108:111], v152 offset:2304\n\t"
  "v_mfma_f32_16x16x32_bf16 v[56:59], v[128:131], v[88:91], 0\n\t"
  "v_mfma_f32_16x16x32_bf16 v[60:63], v[112:115], v[92:95], 0\n\t"
  "v_mfma_f32_16x16x32_bf16 v[64:67], v[116:119], v[92:95], 0\n\t"
  "v_mfma_f32_16x16x32_bf16 v[68:71], v[120:123], v[92:95], 0\n\t"
  "s_add_u32 m0, s23, 0\n\t"
  "s_nop 0\n\t"
  "buffer_load_dwordx4 v240, s[4:7], s21 offen lds\n\t"
  "v_mfma_f32_16x16x32_bf16 v[72:75], v[124:127], v[92:95], 0\n\t"
  "s_mov_b64 exec, 0xffff\n\t"
  "buffer_load_dwordx4 v241, s[4:7], s21 offen offset:1024 lds\n\t"
  "s_mov_b64 exec, -1\n\t"
  "s_add_u32 s21, s21, 40960\n\t"
  "v_mfma_f32_16x16x32_bf16 v[76:79], v[128:131], v[92:95], 0\n\t"
  "s_waitcnt vmcnt(10)\n\t"
  "s_waitcnt vmcnt(8) lgkmcnt(0)\n\t"
  "s_barrier\n\t"
  "s_cmp_lg_u32 s30, 0\n\t"
  "s_cbranch_scc1 LOPnopub_%=\n\t"
  "s_mov_b64 exec, 1\n\t"
  "buffer_store_dword v164, v165, s[12:15], s29 offen sc1\n\t"
  "s_mov_b64 exec, -1\n\t"
  "LOPnopub_%=:\n\t"
  "v_add_u32 v152, 53248, v237\n\t"
  "v_add_u32 v153, 53248, v238\n\t"
  "v_add_u32 v154, 53248, v239\n\t"
  "v_mfma_f32_16x16x32_bf16 v[0:3], v[132:135], v[96:99], v[0:3]\n\t"
  "ds_read_b128 v[80:83], v152 offset:0\n\t"
  "v_mfma_f32_16x16x32_bf16 v[4:7], v[136:139], v[96:99], v[4:7]\n\t"
  "ds_read_b64_tr_b16 v[112:113], v153 offset:0\n\t"
  "v_mfma_f32_16x16x32_bf16 v[8:11], v[140:143], v[96:99], v[8:11]\n\t"
  "ds_read_b64_tr_b16 v[114:115], v153 offset:512\n\t"
  "v_mfma_f32_16x16x32_bf16 v[12:15], v[144:147], v[96:99], v[12:15]\n\t"
  "ds_read_b64_tr_b16 v[116:117], v154 offset:0\n\t"
  "v_mfma_f32_16x16x32_bf16 v[16:19], v[148:151], v[96:99], v[16:19]\n\t"
  "ds_read_b64_tr_b16 v[118:119], v154 offset:512\n\t"
  "v_mfma_f32_16x16x32_bf16 v[20:23], v[132:135], v[100:103], v[20:23]\n\t"
  "ds_read_b128 v[84:87], v152 offset:256\n\t"
  "v_mfma_f32_16x16x32_bf16 v[24:27], v[136:139], v[100:103], v[24:27]\n\t"
  "ds_read_b64_tr_b16 v[120:121], v153 offset:1024\n\t"
  "v_mfma_f32_16x16x32_bf16 v[28:31], v[140:143], v[100:103], v[28:31]\n\t"
  "ds_read_b64_tr_b16 v[122:123], v153 offset:1536\n\t"
  "v_mfma_f32_16x16x32_bf16 v[32:35], v[144:147], v[100:103], v[32:35]\n\t"
  "ds_read_b64_tr_b16 v[124:125], v154 offset:1024\n\t"
  "v_mfma_f32_16x16x32_bf16 v[36:39], v[148:151], v[100:103], v[36:39]\n\t"
  "ds_read_b64_tr_b16 v[126:127], v154 offset:1536\n\t"
  "v_mfma_f32_16x16x32_bf16 v[40:43], v[132:135], v[104:107], v[40:43]\n\t"
  "ds_read_b128 v[88:91], v152 offset:2048\n\t"
  "v_mfma_f32_16x16x32_bf16 v[44:47], v[136:139], v[104:107], v[44:47]\n\t"
  "ds_read_b64_tr_b16 v[128:129], v153 offset:2048\n\t"
  "v_mfma_f32_16x16x32_bf16 v[48:51], v[140:143], v[104:107], v[48:51]\n\t"
  "ds_read_b64_tr_b16 v[130:131], v153 offset:2560\n\t"
  "v_mfma_f32_16x16x32_bf16 v[52:55], v[144:147], v[104:107], v[52:55]\n\t"
  "ds_read_b128 v[92:95], v152 offset:2304\n\t"
  "v_mfma_f32_16x16x32_bf16 v[56:59], v[148:151], v[104:107], v[56:59]\n\t"
  "v_mfma_f32_16x16x32_bf16 v[60:63], v[132:135], v[108:111], v[60:63]\n\t"
  "v_mfma_f32_16x16x32_bf16 v[64:67], v[136:139], v[108:111], v[64:67]\n\t"
  "v_mfma_f32_16x16x32_bf16 v[68:71], v[140:143], v[108:111], v[68:71]\n\t"
  "s_add_u32 m0, s23, 26624\n\t"
  "s_nop 0\n\t"
  "buffer_load_dwordx4 v240, s[4:7], s21 offen lds\n\t"
  "v_mfma_f32_16x16x32_bf16 v[72:75], v[144:147], v[108:111], v[72:75]\n\t"
  "s_mov_b64 exec, 0xffff\n\t"
  "buffer_load_dwordx4 v241, s[4:7], s21 offen offset:1024 lds\n\t"
  "s_mov_b64 exec, -1\n\t"
  "s_add_u32 s21, s21, 40960\n\t"
  "s_cmp_eq_u32 s21, s27\n\t"
  "s_cselect_b32 s35, 655360, 0\n\t"
  "s_sub_u32 s21, s21, s35\n\t"
  "v_mfma_f32_16x16x32_bf16 v[76:79], v[148:151], v[108:111], v[76:79]\n\t"
  "s_waitcnt vmcnt(8) lgkmcnt(0)\n\t"
  "s_barrier\n\t"
  "v_add_u32 v152, 79872, v237\n\t"
  "v_add_u32 v153, 79872, v238\n\t"
  "v_add_u32 v154, 79872, v239\n\t"
  "v_mfma_f32_16x16x32_bf16 v[0:3], v[112:115], v[80:83], v[0:3]\n\t"
  "ds_read_b128 v[96:99], v152 offset:0\n\t"
  "v_mfma_f32_16x16x32_bf16 v[4:7], v[116:119], v[80:83], v[4:7]\n\t"
  "ds_read_b64_tr_b16 v[132:133], v153 offset:0\n\t"
  "v_mfma_f32_16x16x32_bf16 v[8:11], v[120:123], v[80:83], v[8:11]\n\t"
  "ds_read_b64_tr_b16 v[134:135], v153 offset:512\n\t"
  "v_mfma_f32_16x16x32_bf16 v[12:15], v[124:127], v[80:83], v[12:15]\n\t"
  "ds_read_b64_tr_b16 v[136:137], v154 offset:0\n\t"
  "v_mfma_f32_16x16x32_bf16 v[16:19], v[128:131], v[80:83], v[16:19]\n\t"
  "ds_read_b64_tr_b16 v[138:139], v154 offset:512\n\t"
  "v_mfma_f32_16x16x32_bf16 v[20:23], v[112:115], v[84:87], v[20:23]\n\t"
  "ds_read_b128 v[100:103], v152 offset:256\n\t"
  "v_mfma_f32_16x16x32_bf16 v[24:27], v[116:119], v[84:87], v[24:27]\n\t"
  "ds_read_b64_tr_b16 v[140:141], v153 offset:1024\n\t"
  "v_mfma_f32_16x16x32_bf16 v[28:31], v[120:123], v[84:87], v[28:31]\n\t"
  "ds_read_b64_tr_b16 v[142:143], v153 offset:1536\n\t"
  "v_mfma_f32_16x16x32_bf16 v[32:35], v[124:127], v[84:87], v[32:35]\n\t"
  "ds_read_b64_tr_b16 v[144:145], v154 offset:1024\n\t"
  "v_mfma_f32_16x16x32_bf16 v[36:39], v[128:131], v[84:87], v[36:39]\n\t"
  "ds_read_b64_tr_b16 v[146:147], v154 offset:1536\n\t"
  "s_add_u32 m0, s23, 53248\n\t"
  "s_nop 0\n\t"
  "buffer_load_dwordx4 v240, s[4:7], s21 offen lds\n\t"
  "v_mfma_f32_16x16x32_bf16 v[40:43], v[112:115], v[88:91], v[40:43]\n\t"
  "ds_read_b128 v[104:107], v152 offset:2048\n\t"
  "s_mov_b64 exec, 0xffff\n\t"
  "buffer_load_dwordx4 v241, s[4:7], s21 offen offset:1024 lds\n\t"
  "s_mov_b64 exec, -1\n\t"
  "s_add_u32 s21, s21, 40960\n\t"
  "v_mfma_f32_16x16x32_bf16 v[44:47], v[116:119], v[88:91], v[44:47]\n\t"
  "ds_read_b64_tr_b16 v[148:149], v153 offset:2048\n\t"
  "buffer_load_dword v156, v155, s[12:15], s37 offen sc1\n\t"
  "v_mfma_f32_16x16x32_bf16 v[48:51], v[120:123], v[88:91], v[48:51]\n\t"
  "ds_read_b64_tr_b16 v[150:151], v153 offset:2560\n\t"
  "s_waitcnt vmcnt(0)\n\t"
  "LOPcc_%=:\n\t"
  "v_cmp_ne_u32 vcc, s28, v156\n\t"
  "s_cbranch_vccnz LOPcs_%=\n\t"
  "LOPco_%=:\n\t"
  "v_mfma_f32_16x16x32_bf16 v[52:55], v[124:127], v[88:91], v[52:55]\n\t"
  "ds_read_b128 v[108:111], v152 offset:2304\n\t"
  "s_add_u32 m0, s22, 106496\n\t"
  "s_nop 0\n\t"
  "buffer_load_dwordx4 v223, s[48:51], s20 offen sc1 lds\n\t"
  "v_mfma_f32_16x16x32_bf16 v[56:59], v[128:131], v[88:91], v[56:59]\n\t"
  "buffer_load_dwordx4 v223, s[48:51], s20 offen offset:1024 sc1 lds\n\t"
  "s_add_u32 s20, s20, 2048\n\t"
  "v_mfma_f32_16x16x32_bf16 v[60:63], v[112:115], v[92:95], v[60:63]\n\t"
  "s_add_u32 m0, s22, 133120\n\t"
  "s_nop 0\n\t"
  "buffer_load_dwordx4 v223, s[48:51], s20 offen sc1 lds\n\t"
  "v_mfma_f32_16x16x32_bf16 v[64:67], v[116:119], v[92:95], v[64:67]\n\t"
  "buffer_load_dwordx4 v223, s[48:51], s20 offen offset:1024 sc1 lds\n\t"
  "s_add_u32 s20, s20, 2048\n\t"
  "v_mfma_f32_16x16x32_bf16 v[68:71], v[120:123], v[92:95], v[68:71]\n\t"
  "s_add_u32 m0, s22, 0\n\t"
  "s_nop 0\n\t"
  "buffer_load_dwordx4 v223, s[48:51], s20 offen sc1 lds\n\t"
  "v_mfma_f32_16x16x32_bf16 v[72:75], v[124:127], v[92:95], v[72:75]\n\t"
  "buffer_load_dwordx4 v223, s[48:51], s20 offen offset:1024 sc1 lds\n\t"
  "s_add_u32 s20, s20, 2048\n\t"
  "v_mfma_f32_16x16x32_bf16 v[76:79], v[128:131], v[92:95], v[76:79]\n\t"
  "s_waitcnt vmcnt(4) lgkmcnt(0)\n\t"
  "s_barrier\n\t"
  "v_add_u32 v152, 106496, v237\n\t"
  "v_add_u32 v153, 106496, v238\n\t"
  "v_add_u32 v154, 106496, v239\n\t"
  "v_mfma_f32_16x16x32_bf16 v[0:3], v[132:135], v[96:99], v[0:3]\n\t"
  "ds_read_b128 v[80:83], v152 offset:0\n\t"
  "v_mfma_f32_16x16x32_bf16 v[4:7], v[136:139], v[96:99], v[4:7]\n\t"
  "ds_read_b64_tr_b16 v[112:113], v153 offset:0\n\t"
  "v_mfma_f32_16x16x32_bf16 v[8:11], v[140:143], v[96:99], v[8:11]\n\t"
  "ds_read_b64_tr_b16 v[114:115], v153 offset:512\n\t"
  "v_mfma_f32_16x16x32_bf16 v[12:15], v[144:147], v[96:99], v[12:15]\n\t"
  "ds_read_b64_tr_b16 v[116:117], v154 offset:0\n\t"
  "v_mfma_f32_16x16x32_bf16 v[16:19], v[148:151], v[96:99], v[16:19]\n\t"
  "ds_read_b64_tr_b16 v[118:119], v154 offset:512\n\t"
  "v_mfma_f32_16x16x32_bf16 v[20:23], v[132:135], v[100:103], v[20:23]\n\t"
  "ds_read_b128 v[84:87], v152 offset:256\n\t"
  "v_mfma_f32_16x16x32_bf16 v[24:27], v[136:139], v[100:103], v[24:27]\n\t"
  "ds_read_b64_tr_b16 v[120:121], v153 offset:1024\n\t"
  "v_mfma_f32_16x16x32_bf16 v[28:31], v[140:143], v[100:103], v[28:31]\n\t"
  "ds_read_b64_tr_b16 v[122:123], v153 offset:1536\n\t"
  "v_mfma_f32_16x16x32_bf16 v[32:35], v[144:147], v[100:103], v[32:35]\n\t"
  "ds_read_b64_tr_b16 v[124:125], v154 offset:1024\n\t"
  "v_mfma_f32_16x16x32_bf16 v[36:39], v[148:151], v[100:103], v[36:39]\n\t"
  "ds_read_b64_tr_b16 v[126:127], v154 offset:1536\n\t"
  "v_mfma_f32_16x16x32_bf16 v[40:43], v[132:135], v[104:107], v[40:43]\n\t"
  "ds_read_b128 v[88:91], v152 offset:2048\n\t"
  "v_mfma_f32_16x16x32_bf16 v[44:47], v[136:139], v[104:107], v[44:47]\n\t"
  "ds_read_b64_tr_b16 v[128:129], v153 offset:2048\n\t"
  "v_mfma_f32_16x16x32_bf16 v[48:51], v[140:143], v[104:107], v[48:51]\n\t"
  "ds_read_b64_tr_b16 v[130:131], v153 offset:2560\n\t"
  "v_mfma_f32_16x16x32_bf16 v[52:55], v[144:147], v[104:107], v[52:55]\n\t"
  "ds_read_b128 v[92:95], v152 offset:2304\n\t"
  "s_add_u32 m0, s23, 79872\n\t"
  "s_nop 0\n\t"
  "buffer_load_dwordx4 v240, s[4:7], s21 offen lds\n\t"
  "v_mfma_f32_16x16x32_bf16 v[56:59], v[148:151], v[104:107], v[56:59]\n\t"
  "s_mov_b64 exec, 0xffff\n\t"
  "buffer_load_dwordx4 v241, s[4:7], s21 offen offset:1024 lds\n\t"
  "s_mov_b64 exec, -1\n\t"
  "s_add_u32 s21, s21, 40960\n\t"
  "v_mfma_f32_16x16x32_bf16 v[60:63], v[132:135], v[108:111], v[60:63]\n\t"
  "s_add_u32 m0, s22, 26624\n\t"
  "s_nop 0\n\t"
  "buffer_load_dwordx4 v223, s[48:51], s20 offen sc1 lds\n\t"
  "v_mfma_f32_16x16x32_bf16 v[64:67], v[136:139], v[108:111], v[64:67]\n\t"
  "buffer_load_dwordx4 v223, s[48:51], s20 offen offset:1024 sc1 lds\n\t"
  "s_add_u32 s20, s20, 2048\n\t"
  "s_cmp_eq_u32 s20, s26\n\t"
  "s_cselect_b32 s35, 32768, 0\n\t"
  "s_sub_u32 s20, s20, s35\n\t"
  "v_mfma_f32_16x16x32_bf16 v[68:71], v[140:143], v[108:111], v[68:71]\n\t"
  "s_add_u32 m0, s22, 53248\n\t"
  "s_nop 0\n\t"
  "buffer_load_dwordx4 v223, s[48:51], s20 offen sc1 lds\n\t"
  "v_mfma_f32_16x16x32_bf16 v[72:75], v[144:147], v[108:111], v[72:75]\n\t"
  "buffer_load_dwordx4 v223, s[48:51], s20 offen offset:1024 sc1 lds\n\t"
  "s_add_u32 s20, s20, 2048\n\t"
  "v_mfma_f32_16x16x32_bf16 v[76:79], v[148:151], v[108:111], v[76:79]\n\t"
  "s_waitcnt vmcnt(8) lgkmcnt(0)\n\t"
  "s_barrier\n\t"
  "v_add_u32 v152, 133120, v237\n\t"
  "v_add_u32 v153, 133120, v238\n\t"
  "v_add_u32 v154, 133120, v239\n\t"
  "v_mfma_f32_16x16x32_bf16 v[0:3], v[112:115], v[80:83], v[0:3]\n\t"
  "ds_read_b128 v[96:99], v152 offset:0\n\t"
  "v_mfma_f32_16x16x32_bf16 v[4:7], v[116:119], v[80:83], v[4:7]\n\t"
  "ds_read_b64_tr_b16 v[132:133], v153 offset:0\n\t"
  "v_mfma_f32_16x16x32_bf16 v[8:11], v[120:123], v[80:83], v[8:11]\n\t"
  "ds_read_b64_tr_b16 v[134:135], v153 offset:512\n\t"
  "v_mfma_f32_16x16x32_bf16 v[12:15], v[124:127], v[80:83], v[12:15]\n\t"
  "ds_read_b64_tr_b16 v[136:137], v154 offset:0\n\t"
  "v_mfma_f32_16x16x32_bf16 v[16:19], v[128:131], v[80:83], v[16:19]\n\t"
  "ds_read_b64_tr_b16 v[138:139], v154 offset:512\n\t"
  "v_mfma_f32_16x16x32_bf16 v[20:23], v[112:115], v[84:87], v[20:23]\n\t"
  "ds_read_b128 v[100:103], v152 offset:256\n\t"
  "v_mfma_f32_16x16x32_bf16 v[24:27], v[116:119], v[84:87], v[24:27]\n\t"
  "ds_read_b64_tr_b16 v[140:141], v153 offset:1024\n\t"
  "v_mfma_f32_16x16x32_bf16 v[28:31], v[120:123], v[84:87], v[28:31]\n\t"
  "ds_read_b64_tr_b16 v[142:143], v153 offset:1536\n\t"
  "v_mfma_f32_16x16x32_bf16 v[32:35], v[124:127], v[84:87], v[32:35]\n\t"
  "ds_read_b64_tr_b16 v[144:145], v154 offset:1024\n\t"
  "v_mfma_f32_16x16x32_bf16 v[36:39], v[128:131], v[84:87], v[36:39]\n\t"
  "ds_read_b64_tr_b16 v[146:147], v154 offset:1536\n\t"
  "v_mfma_f32_16x16x32_bf16 v[40:43], v[112:115], v[88:91], v[40:43]\n\t"
  "ds_read_b128 v[104:107], v152 offset:2048\n\t"
  "v_mfma_f32_16x16x32_bf16 v[44:47], v[116:119], v[88:91], v[44:47]\n\t"
  "ds_read_b64_tr_b16 v[148:149], v153 offset:2048\n\t"
  "v_mfma_f32_16x16x32_bf16 v[48:51], v[120:123], v[88:91], v[48:51]\n\t"
  "ds_read_b64_tr_b16 v[150:151], v153 offset:2560\n\t"
  "v_mfma_f32_16x16x32_bf16 v[52:55], v[124:127], v[88:91], v[52:55]\n\t"
  "ds_read_b128 v[108:111], v152 offset:2304\n\t"
  "s_add_u32 m0, s23, 106496\n\t"
  "s_nop 0\n\t"
  "buffer_load_dwordx4 v240, s[4:7], s21 offen lds\n\t"
  "v_mfma_f32_16x16x32_bf16 v[56:59], v[128:131], v[88:91], v[56:59]\n\t"
  "s_mov_b64 exec, 0xffff\n\t"
  "buffer_load_dwordx4 v241, s[4:7], s21 offen offset:1024 lds\n\t"
  "s_mov_b64 exec, -1\n\t"
  "s_add_u32 s21, s21, 40960\n\t"
  "v_mfma_f32_16x16x32_bf16 v[60:63], v[112:115], v[92:95], v[60:63]\n\t"
  "s_add_u32 m0, s22, 79872\n\t"
  "s_nop 0\n\t"
  "buffer_load_dwordx4 v223, s[48:51], s20 offen sc1 lds\n\t"
  "v_mfma_f32_16x16x32_bf16 v[64:67], v[116:119], v[92:95], v[64:67]\n\t"
  "buffer_load_dwordx4 v223, s[48:51], s20 offen offset:1024 sc1 lds\n\t"
  "s_add_u32 s20, s20, 2048\n\t"
  "v_mfma_f32_16x16x32_bf16 v[68:71], v[120:123], v[92:95], v[68:71]\n\t"
  "s_add_u32 m0, s22, 106496\n\t"
  "s_nop 0\n\t"
  "buffer_load_dwordx4 v223, s[48:51], s20 offen sc1 lds\n\t"
  "v_mfma_f32_16x16x32_bf16 v[72:75], v[124:127], v[92:95], v[72:75]\n\t"
  "buffer_load_dwordx4 v223, s[48:51], s20 offen offset:1024 sc1 lds\n\t"
  "s_add_u32 s20, s20, 2048\n\t"
  "v_mfma_f32_16x16x32_bf16 v[76:79], v[128:131], v[92:95], v[76:79]\n\t"
  "s_waitcnt vmcnt(12) lgkmcnt(0)\n\t"
  "s_barrier\n\t"
  "v_add_u32 v152, 0, v237\n\t"
  "v_add_u32 v153, 0, v238\n\t"
  "v_add_u32 v154, 0, v239\n\t"
  "v_mfma_f32_16x16x32_bf16 v[0:3], v[132:135], v[96:99], v[0:3]\n\t"
  "ds_read_b128 v[80:83], v152 offset:0\n\t"
  "v_mfma_f32_16x16x32_bf16 v[4:7], v[136:139], v[96:99], v[4:7]\n\t"
  "ds_read_b64_tr_b16 v[112:113], v153 offset:0\n\t"
  "v_mfma_f32_16x16x32_bf16 v[8:11], v[140:143], v[96:99], v[8:11]\n\t"
  "ds_read_b64_tr_b16 v[114:115], v153 offset:512\n\t"
  "v_mfma_f32_16x16x32_bf16 v[12:15], v[144:147], v[96:99], v[12:15]\n\t"
  "ds_read_b64_tr_b16 v[116:117], v154 offset:0\n\t"
  "v_mfma_f32_16x16x32_bf16 v[16:19], v[148:151], v[96:99], v[16:19]\n\t"
  "ds_read_b64_tr_b16 v[118:119], v154 offset:512\n\t"
  "v_mfma_f32_16x16x32_bf16 v[20:23], v[132:135], v[100:103], v[20:23]\n\t"
  "ds_read_b128 v[84:87], v152 offset:256\n\t"
  "v_mfma_f32_16x16x32_bf16 v[24:27], v[136:139], v[100:103], v[24:27]\n\t"
  "ds_read_b64_tr_b16 v[120:121], v153 offset:1024\n\t"
  "v_mfma_f32_16x16x32_bf16 v[28:31], v[140:143], v[100:103], v[28:31]\n\t"
  "ds_read_b64_tr_b16 v[122:123], v153 offset:1536\n\t"
  "v_mfma_f32_16x16x32_bf16 v[32:35], v[144:147], v[100:103], v[32:35]\n\t"
  "ds_read_b64_tr_b16 v[124:125], v154 offset:1024\n\t"
  "v_mfma_f32_16x16x32_bf16 v[36:39], v[148:151], v[100:103], v[36:39]\n\t"
  "ds_read_b64_tr_b16 v[126:127], v154 offset:1536\n\t"
  "v_mfma_f32_16x16x32_bf16 v[40:43], v[132:135], v[104:107], v[40:43]\n\t"
  "ds_read_b128 v[88:91], v152 offset:2048\n\t"
  "v_mfma_f32_16x16x32_bf16 v[44:47], v[136:139], v[104:107], v[44:47]\n\t"
  "ds_read_b64_tr_b16 v[128:129], v153 offset:2048\n\t"
  "v_mfma_f32_16x16x32_bf16 v[48:51], v[140:143], v[104:107], v[48:51]\n\t"
  "ds_read_b64_tr_b16 v[130:131], v153 offset:2560\n\t"
  "v_mfma_f32_16x16x32_bf16 v[52:55], v[144:147], v[104:107], v[52:55]\n\t"
  "ds_read_b128 v[92:95], v152 offset:2304\n\t"
  "v_mfma_f32_16x16x32_bf16 v[56:59], v[148:151], v[104:107], v[56:59]\n\t"
  "v_mfma_f32_16x16x32_bf16 v[60:63], v[132:135], v[108:111], v[60:63]\n\t"
  "s_add_u32 m0, s23, 133120\n\t"
  "s_nop 0\n\t"
  "buffer_load_dwordx4 v240, s[4:7], s21 offen lds\n\t"
  "v_mfma_f32_16x16x32_bf16 v[64:67], v[136:139], v[108:111], v[64:67]\n\t"
  "s_mov_b64 exec, 0xffff\n\t"
  "buffer_load_dwordx4 v241, s[4:7], s21 offen offset:1024 lds\n\t"
  "s_mov_b64 exec, -1\n\t"
  "s_add_u32 s21, s21, 40960\n\t"
  "s_cmp_eq_u32 s21, s27\n\t"
  "s_cselect_b32 s35, 655360, 0\n\t"
  "s_sub_u32 s21, s21, s35\n\t"
  "v_mfma_f32_16x16x32_bf16 v[68:71], v[140:143], v[108:111], v[68:71]\n\t"
  "s_add_u32 m0, s22, 133120\n\t"
  "s_nop 0\n\t"
  "buffer_load_dwordx4 v223, s[48:51], s20 offen sc1 lds\n\t"
  "v_mfma_f32_16x16x32_bf16 v[72:75], v[144:147], v[108:111], v[72:75]\n\t"
  "buffer_load_dwordx4 v223, s[48:51], s20 offen offset:1024 sc1 lds\n\t"
  "s_add_u32 s20, s20, 2048\n\t"
  "s_cmp_eq_u32 s20, s26\n\t"
  "s_cselect_b32 s35, 32768, 0\n\t"
  "s_sub_u32 s20, s20, s35\n\t"
  "v_mfma_f32_16x16x32_bf16 v[76:79], v[148:151], v[108:111], v[76:79]\n\t"
  "s_waitcnt vmcnt(12) lgkmcnt(0)\n\t"
  "s_barrier\n\t"
  "v_add_u32 v152, 26624, v237\n\t"
  "v_add_u32 v153, 26624, v238\n\t"
  "v_add_u32 v154, 26624, v239\n\t"
  "v_mfma_f32_16x16x32_bf16 v[0:3], v[112:115], v[80:83], v[0:3]\n\t"
  "ds_read_b128 v[96:99], v152 offset:0\n\t"
  "v_mfma_f32_16x16x32_bf16 v[4:7], v[116:119], v[80:83], v[4:7]\n\t"
  "ds_read_b64_tr_b16 v[132:133], v153 offset:0\n\t"
  "v_mfma_f32_16x16x32_bf16 v[8:11], v[120:123], v[80:83], v[8:11]\n\t"
  "ds_read_b64_tr_b16 v[134:135], v153 offset:512\n\t"
  "v_mfma_f32_16x16x32_bf16 v[12:15], v[124:127], v[80:83], v[12:15]\n\t"
  "ds_read_b64_tr_b16 v[136:137], v154 offset:0\n\t"
  "v_mfma_f32_16x16x32_bf16 v[16:19], v[128:131], v[80:83], v[16:19]\n\t"
  "ds_read_b64_tr_b16 v[138:139], v154 offset:512\n\t"
  "v_mfma_f32_16x16x32_bf16 v[20:23], v[112:115], v[84:87], v[20:23]\n\t"
  "ds_read_b128 v[100:103], v152 offset:256\n\t"
  "v_mfma_f32_16x16x32_bf16 v[24:27], v[116:119], v[84:87], v[24:27]\n\t"
  "ds_read_b64_tr_b16 v[140:141], v153 offset:1024\n\t"
  "v_mfma_f32_16x16x32_bf16 v[28:31], v[120:123], v[84:87], v[28:31]\n\t"
  "ds_read_b64_tr_b16 v[142:143], v153 offset:1536\n\t"
  "v_mfma_f32_16x16x32_bf16 v[32:35], v[124:127], v[84:87], v[32:35]\n\t"
  "ds_read_b64_tr_b16 v[144:145], v154 offset:1024\n\t"
  "v_mfma_f32_16x16x32_bf16 v[36:39], v[128:131], v[84:87], v[36:39]\n\t"
  "ds_read_b64_tr_b16 v[146:147], v154 offset:1536\n\t"
  "v_mfma_f32_16x16x32_bf16 v[40:43], v[112:115], v[88:91], v[40:43]\n\t"
  "ds_read_b128 v[104:107], v152 offset:2048\n\t"
  "v_mfma_f32_16x16x32_bf16 v[44:47], v[116:119], v[88:91], v[44:47]\n\t"
  "ds_read_b64_tr_b16 v[148:149], v153 offset:2048\n\t"
  "v_mfma_f32_16x16x32_bf16 v[48:51], v[120:123], v[88:91], v[48:51]\n\t"
  "ds_read_b64_tr_b16 v[150:151], v153 offset:2560\n\t"
  "v_mfma_f32_16x16x32_bf16 v[52:55], v[124:127], v[88:91], v[52:55]\n\t"
  "ds_read_b128 v[108:111], v152 offset:2304\n\t"
  "v_mfma_f32_16x16x32_bf16 v[56:59], v[128:131], v[88:91], v[56:59]\n\t"
  "v_mfma_f32_16x16x32_bf16 v[60:63], v[112:115], v[92:95], v[60:63]\n\t"
  "s_add_u32 m0, s23, 0\n\t"
  "s_nop 0\n\t"
  "buffer_load_dwordx4 v240, s[4:7], s21 offen lds\n\t"
  "v_mfma_f32_16x16x32_bf16 v[64:67], v[116:119], v[92:95], v[64:67]\n\t"
  "s_mov_b64 exec, 0xffff\n\t"
  "buffer_load_dwordx4 v241, s[4:7], s21 offen offset:1024 lds\n\t"
  "s_mov_b64 exec, -1\n\t"
  "s_add_u32 s21, s21, 40960\n\t"
  "v_mfma_f32_16x16x32_bf16 v[68:71], v[120:123], v[92:95], v[68:71]\n\t"
  "s_add_u32 m0, s22, 0\n\t"
  "s_nop 0\n\t"
  "buffer_load_dwordx4 v223, s[48:51], s20 offen sc1 lds\n\t"
  "v_mfma_f32_16x16x32_bf16 v[72:75], v[124:127], v[92:95], v[72:75]\n\t"
  "buffer_load_dwordx4 v223, s[48:51], s20 offen offset:1024 sc1 lds\n\t"
  "s_add_u32 s20, s20, 2048\n\t"
  "v_mfma_f32_16x16x32_bf16 v[76:79], v[128:131], v[92:95], v[76:79]\n\t"
  "s_waitcnt vmcnt(14) lgkmcnt(0)\n\t"
  "s_barrier\n\t"
  "v_add_u32 v152, 53248, v237\n\t"
  "v_add_u32 v153, 53248, v238\n\t"
  "v_add_u32 v154, 53248, v239\n\t"
  "v_mfma_f32_16x16x32_bf16 v[0:3], v[132:135], v[96:99], v[0:3]\n\t"
  "ds_read_b128 v[80:83], v152 offset:0\n\t"
  "v_mfma_f32_16x16x32_bf16 v[4:7], v[136:139], v[96:99], v[4:7]\n\t"
  "ds_read_b64_tr_b16 v[112:113], v153 offset:0\n\t"
  "v_mfma_f32_16x16x32_bf16 v[8:11], v[140:143], v[96:99], v[8:11]\n\t"
  "ds_read_b64_tr_b16 v[114:115], v153 offset:512\n\t"
  "v_mfma_f32_16x16x32_bf16 v[12:15], v[144:147], v[96:99], v[12:15]\n\t"
  "ds_read_b64_tr_b16 v[116:117], v154 offset:0\n\t"
  "v_mfma_f32_16x16x32_bf16 v[16:19], v[148:151], v[96:99], v[16:19]\n\t"
  "ds_read_b64_tr_b16 v[118:119], v154 offset:512\n\t"
  "v_mfma_f32_16x16x32_bf16 v[20:23], v[132:135], v[100:103], v[20:23]\n\t"
  "ds_read_b128 v[84:87], v152 offset:256\n\t"
  "v_mfma_f32_16x16x32_bf16 v[24:27], v[136:139], v[100:103], v[24:27]\n\t"
  "ds_read_b64_tr_b16 v[120:121], v153 offset:1024\n\t"
  "v_mfma_f32_16x16x32_bf16 v[28:31], v[140:143], v[100:103], v[28:31]\n\t"
  "ds_read_b64_tr_b16 v[122:123], v153 offset:1536\n\t"
  "v_mfma_f32_16x16x32_bf16 v[32:35], v[144:147], v[100:103], v[32:35]\n\t"
  "ds_read_b64_tr_b16 v[124:125], v154 offset:1024\n\t"
  "v_mfma_f32_16x16x32_bf16 v[36:39], v[148:151], v[100:103], v[36:39]\n\t"
  "ds_read_b64_tr_b16 v[126:127], v154 offset:1536\n\t"
  "v_mfma_f32_16x16x32_bf16 v[40:43], v[132:135], v[104:107], v[40:43]\n\t"
  "ds_read_b128 v[88:91], v152 offset:2048\n\t"
  "v_mfma_f32_16x16x32_bf16 v[44:47], v[136:139], v[104:107], v[44:47]\n\t"
  "ds_read_b64_tr_b16 v[128:129], v153 offset:2048\n\t"
  "v_mfma_f32_16x16x32_bf16 v[48:51], v[140:143], v[104:107], v[48:51]\n\t"
  "ds_read_b64_tr_b16 v[130:131], v153 offset:2560\n\t"
  "v_mfma_f32_16x16x32_bf16 v[52:55], v[144:147], v[104:107], v[52:55]\n\t"
  "ds_read_b128 v[92:95], v152 offset:2304\n\t"
  "v_mfma_f32_16x16x32_bf16 v[56:59], v[148:151], v[104:107], v[56:59]\n\t"
  "v_mfma_f32_16x16x32_bf16 v[60:63], v[132:135], v[108:111], v[60:63]\n\t"
  "s_add_u32 m0, s23, 26624\n\t"
  "s_nop 0\n\t"
  "buffer_load_dwordx4 v240, s[4:7], s21 offen lds\n\t"
  "v_mfma_f32_16x16x32_bf16 v[64:67], v[136:139], v[108:111], v[64:67]\n\t"
  "s_mov_b64 exec, 0xffff\n\t"
  "buffer_load_dwordx4 v241, s[4:7], s21 offen offset:1024 lds\n\t"
  "s_mov_b64 exec, -1\n\t"
  "s_add_u32 s21, s21, 40960\n\t"
  "v_mfma_f32_16x16x32_bf16 v[68:71], v[140:143], v[108:111], v[68:71]\n\t"
  "s_add_u32 m0, s22, 26624\n\t"
  "s_nop 0\n\t"
  "buffer_load_dwordx4 v223, s[48:51], s20 offen sc1 lds\n\t"
  "v_mfma_f32_16x16x32_bf16 v[72:75], v[144:147], v[108:111], v[72:75]\n\t"
  "buffer_load_dwordx4 v223, s[48:51], s20 offen offset:1024 sc1 lds\n\t"
  "s_add_u32 s20, s20, 2048\n\t"
  "v_mfma_f32_16x16x32_bf16 v[76:79], v[148:151], v[108:111], v[76:79]\n\t"
  "s_waitcnt vmcnt(14) lgkmcnt(0)\n\t"
  "s_barrier\n\t"
  "v_add_u32 v152, 79872, v237\n\t"
  "v_add_u32 v153, 79872, v238\n\t"
  "v_add_u32 v154, 79872, v239\n\t"
  "v_mfma_f32_16x16x32_bf16 v[0:3], v[112:115], v[80:83], v[0:3]\n\t"
  "ds_read_b128 v[96:99], v152 offset:0\n\t"
  "v_mfma_f32_16x16x32_bf16 v[4:7], v[116:119], v[80:83], v[4:7]\n\t"
  "ds_read_b64_tr_b16 v[132:133], v153 offset:0\n\t"
  "v_mfma_f32_16x16x32_bf16 v[8:11], v[120:123], v[80:83], v[8:11]\n\t"
  "ds_read_b64_tr_b16 v[134:135], v153 offset:512\n\t"
  "v_mfma_f32_16x16x32_bf16 v[12:15], v[124:127], v[80:83], v[12:15]\n\t"
  "ds_read_b64_tr_b16 v[136:137], v154 offset:0\n\t"
  "v_mfma_f32_16x16x32_bf16 v[16:19], v[128:131], v[80:83], v[16:19]\n\t"
  "ds_read_b64_tr_b16 v[138:139], v154 offset:512\n\t"
  "v_mfma_f32_16x16x32_bf16 v[20:23], v[112:115], v[84:87], v[20:23]\n\t"
  "ds_read_b128 v[100:103], v152 offset:256\n\t"
  "v_mfma_f32_16x16x32_bf16 v[24:27], v[116:119], v[84:87], v[24:27]\n\t"
  "ds_read_b64_tr_b16 v[140:141], v153 offset:1024\n\t"
  "v_mfma_f32_16x16x32_bf16 v[28:31], v[120:123], v[84:87], v[28:31]\n\t"
  "ds_read_b64_tr_b16 v[142:143], v153 offset:1536\n\t"
  "v_mfma_f32_16x16x32_bf16 v[32:35], v[124:127], v[84:87], v[32:35]\n\t"
  "ds_read_b64_tr_b16 v[144:145], v154 offset:1024\n\t"
  "v_mfma_f32_16x16x32_bf16 v[36:39], v[128:131], v[84:87], v[36:39]\n\t"
  "ds_read_b64_tr_b16 v[146:147], v154 offset:1536\n\t"
  "v_mfma_f32_16x16x32_bf16 v[40:43], v[112:115], v[88:91], v[40:43]\n\t"
  "ds_read_b128 v[104:107], v152 offset:2048\n\t"
  "v_mfma_f32_16x16x32_bf16 v[44:47], v[116:119], v[88:91], v[44:47]\n\t"
  "ds_read_b64_tr_b16 v[148:149], v153 offset:2048\n\t"
  "v_mfma_f32_16x16x32_bf16 v[48:51], v[120:123], v[88:91], v[48:51]\n\t"
  "ds_read_b64_tr_b16 v[150:151], v153 offset:2560\n\t"
  "v_mfma_f32_16x16x32_bf16 v[52:55], v[124:127], v[88:91], v[52:55]\n\t"
  "ds_read_b128 v[108:111], v152 offset:2304\n\t"
  "v_mfma_f32_16x16x32_bf16 v[56:59], v[128:131], v[88:91], v[56:59]\n\t"
  "v_mfma_f32_16x16x32_bf16 v[60:63], v[112:115], v[92:95], v[60:63]\n\t"
  "s_add_u32 m0, s23, 53248\n\t"
  "s_nop 0\n\t"
  "buffer_load_dwordx4 v240, s[4:7], s21 offen lds\n\t"
  "v_mfma_f32_16x16x32_bf16 v[64:67], v[116:119], v[92:95], v[64:67]\n\t"
  "s_mov_b64 exec, 0xffff\n\t"
  "buffer_load_dwordx4 v241, s[4:7], s21 offen offset:1024 lds\n\t"
  "s_mov_b64 exec, -1\n\t"
  "s_add_u32 s21, s21, 40960\n\t"
  "v_mfma_f32_16x16x32_bf16 v[68:71], v[120:123], v[92:95], v[68:71]\n\t"
  "s_add_u32 m0, s22, 53248\n\t"
  "s_nop 0\n\t"
  "buffer_load_dwordx4 v223, s[48:51], s20 offen sc1 lds\n\t"
  "v_mfma_f32_16x16x32_bf16 v[72:75], v[124:127], v[92:95], v[72:75]\n\t"
  "buffer_load_dwordx4 v223, s[48:51], s20 offen offset:1024 sc1 lds\n\t"
  "s_add_u32 s20, s20, 2048\n\t"
  "v_mfma_f32_16x16x32_bf16 v[76:79], v[128:131], v[92:95], v[76:79]\n\t"
  "s_waitcnt vmcnt(16) lgkmcnt(0)\n\t"
  "s_barrier\n\t"
  "v_add_u32 v152, 106496, v237\n\t"
  "v_add_u32 v153, 106496, v238\n\t"
  "v_add_u32 v154, 106496, v239\n\t"
  "v_mfma_f32_16x16x32_bf16 v[0:3], v[132:135], v[96:99], v[0:3]\n\t"
  "ds_read_b128 v[80:83], v152 offset:0\n\t"
  "v_mfma_f32_16x16x32_bf16 v[4:7], v[136:139], v[96:99], v[4:7]\n\t"
  "ds_read_b64_tr_b16 v[112:113], v153 offset:0\n\t"
  "v_mfma_f32_16x16x32_bf16 v[8:11], v[140:143], v[96:99], v[8:11]\n\t"
  "ds_read_b64_tr_b16 v[114:115], v153 offset:512\n\t"
  "v_mfma_f32_16x16x32_bf16 v[12:15], v[144:147], v[96:99], v[12:15]\n\t"
  "ds_read_b64_tr_b16 v[116:117], v154 offset:0\n\t"
  "v_mfma_f32_16x16x32_bf16 v[16:19], v[148:151], v[96:99], v[16:19]\n\t"
  "ds_read_b64_tr_b16 v[118:119], v154 offset:512\n\t"
  "v_mfma_f32_16x16x32_bf16 v[20:23], v[132:135], v[100:103], v[20:23]\n\t"
  "ds_read_b128 v[84:87], v152 offset:256\n\t"
  "v_mfma_f32_16x16x32_bf16 v[24:27], v[136:139], v[100:103], v[24:27]\n\t"
  "ds_read_b64_tr_b16 v[120:121], v153 offset:1024\n\t"
  "v_mfma_f32_16x16x32_bf16 v[28:31], v[140:143], v[100:103], v[28:31]\n\t"
  "ds_read_b64_tr_b16 v[122:123], v153 offset:1536\n\t"
  "v_mfma_f32_16x16x32_bf16 v[32:35], v[144:147], v[100:103], v[32:35]\n\t"
  "ds_read_b64_tr_b16 v[124:125], v154 offset:1024\n\t"
  "v_mfma_f32_16x16x32_bf16 v[36:39], v[148:151], v[100:103], v[36:39]\n\t"
  "ds_read_b64_tr_b16 v[126:127], v154 offset:1536\n\t"
  "v_mfma_f32_16x16x32_bf16 v[40:43], v[132:135], v[104:107], v[40:43]\n\t"
  "ds_read_b128 v[88:91], v152 offset:2048\n\t"
  "v_mfma_f32_16x16x32_bf16 v[44:47], v[136:139], v[104:107], v[44:47]\n\t"
  "ds_read_b64_tr_b16 v[128:129], v153 offset:2048\n\t"
  "v_mfma_f32_16x16x32_bf16 v[48:51], v[140:143], v[104:107], v[48:51]\n\t"
  "ds_read_b64_tr_b16 v[130:131], v153 offset:2560\n\t"
  "v_mfma_f32_16x16x32_bf16 v[52:55], v[144:147], v[104:107], v[52:55]\n\t"
  "ds_read_b128 v[92:95], v152 offset:2304\n\t"
  "v_mfma_f32_16x16x32_bf16 v[56:59], v[148:151], v[104:107], v[56:59]\n\t"
  "v_mfma_f32_16x16x32_bf16 v[60:63], v[132:135], v[108:111], v[60:63]\n\t"
  "s_add_u32 m0, s23, 79872\n\t"
  "s_nop 0\n\t"
  "buffer_load_dwordx4 v240, s[4:7], s21 offen lds\n\t"
  "v_mfma_f32_16x16x32_bf16 v[64:67], v[136:139], v[108:111], v[64:67]\n\t"
  "s_mov_b64 exec, 0xffff\n\t"
  "buffer_load_dwordx4 v241, s[4:7], s21 offen offset:1024 lds\n\t"
  "s_mov_b64 exec, -1\n\t"
  "s_add_u32 s21, s21, 40960\n\t"
  "s_cmp_eq_u32 s21, s27\n\t"
  "s_cselect_b32 s35, 655360, 0\n\t"
  "s_sub_u32 s21, s21, s35\n\t"
  "v_mfma_f32_16x16x32_bf16 v[68:71], v[140:143], v[108:111], v[68:71]\n\t"
  "s_add_u32 m0, s22, 79872\n\t"
  "s_nop 0\n\t"
  "buffer_load_dwordx4 v223, s[48:51], s20 offen sc1 lds\n\t"
  "v_mfma_f32_16x16x32_bf16 v[72:75], v[144:147], v[108:111], v[72:75]\n\t"
  "buffer_load_dwordx4 v223, s[48:51], s20 offen offset:1024 sc1 lds\n\t"
  "s_add_u32 s20, s20, 2048\n\t"
  "s_cmp_eq_u32 s20, s26\n\t"
  "s_cselect_b32 s35, 32768, 0\n\t"
  "s_sub_u32 s20, s20, s35\n\t"
  "v_mfma_f32_16x16x32_bf16 v[76:79], v[148:151], v[108:111], v[76:79]\n\t"
  "s_waitcnt vmcnt(16) lgkmcnt(0)\n\t"
  "s_barrier\n\t"
  "v_add_u32 v152, 133120, v237\n\t"
  "v_add_u32 v153, 133120, v238\n\t"
  "v_add_u32 v154, 133120, v239\n\t"
  "v_mfma_f32_16x16x32_bf16 v[0:3], v[112:115], v[80:83], v[0:3]\n\t"
  "ds_read_b128 v[96:99], v152 offset:0\n\t"
  "v_mfma_f32_16x16x32_bf16 v[4:7], v[116:119], v[80:83], v[4:7]\n\t"
  "ds_read_b64_tr_b16 v[132:133], v153 offset:0\n\t"
  "v_mfma_f32_16x16x32_bf16 v[8:11], v[120:123], v[80:83], v[8:11]\n\t"
  "ds_read_b64_tr_b16 v[134:135], v153 offset:512\n\t"
  "v_mfma_f32_16x16x32_bf16 v[12:15], v[124:127], v[80:83], v[12:15]\n\t"
  "ds_read_b64_tr_b16 v[136:137], v154 offset:0\n\t"
  "v_mfma_f32_16x16x32_bf16 v[16:19], v[128:131], v[80:83], v[16:19]\n\t"
  "ds_read_b64_tr_b16 v[138:139], v154 offset:512\n\t"
  "v_mfma_f32_16x16x32_bf16 v[20:23], v[112:115], v[84:87], v[20:23]\n\t"
  "ds_read_b128 v[100:103], v152 offset:256\n\t"
  "v_mfma_f32_16x16x32_bf16 v[24:27], v[116:119], v[84:87], v[24:27]\n\t"
  "ds_read_b64_tr_b16 v[140:141], v153 offset:1024\n\t"
  "v_mfma_f32_16x16x32_bf16 v[28:31], v[120:123], v[84:87], v[28:31]\n\t"
  "ds_read_b64_tr_b16 v[142:143], v153 offset:1536\n\t"
  "v_mfma_f32_16x16x32_bf16 v[32:35], v[124:127], v[84:87], v[32:35]\n\t"
  "ds_read_b64_tr_b16 v[144:145], v154 offset:1024\n\t"
  "v_mfma_f32_16x16x32_bf16 v[36:39], v[128:131], v[84:87], v[36:39]\n\t"
  "ds_read_b64_tr_b16 v[146:147], v154 offset:1536\n\t"
  "v_mfma_f32_16x16x32_bf16 v[40:43], v[112:115], v[88:91], v[40:43]\n\t"
  "ds_read_b128 v[104:107], v152 offset:2048\n\t"
  "v_mfma_f32_16x16x32_bf16 v[44:47], v[116:119], v[88:91], v[44:47]\n\t"
  "ds_read_b64_tr_b16 v[148:149], v153 offset:2048\n\t"
  "v_mfma_f32_16x16x32_bf16 v[48:51], v[120:123], v[88:91], v[48:51]\n\t"
  "ds_read_b64_tr_b16 v[150:151], v153 offset:2560\n\t"
  "v_mfma_f32_16x16x32_bf16 v[52:55], v[124:127], v[88:91], v[52:55]\n\t"
  "ds_read_b128 v[108:111], v152 offset:2304\n\t"
  "v_mfma_f32_16x16x32_bf16 v[56:59], v[128:131], v[88:91], v[56:59]\n\t"
  "v_mfma_f32_16x16x32_bf16 v[60:63], v[112:115], v[92:95], v[60:63]\n\t"
  "v_mfma_f32_16x16x32_bf16 v[64:67], v[116:119], v[92:95], v[64:67]\n\t"
  "s_add_u32 m0, s34, 106496\n\t"
  "s_add_u32 s35, s31, s32\n\t"
  "buffer_load_dwordx4 v243, s[16:19], s31 offen lds\n\t"
  "v_mfma_f32_16x16x32_bf16 v[68:71], v[120:123], v[92:95], v[68:71]\n\t"
  "buffer_load_dwordx4 v243, s[16:19], s35 offen offset:1024 lds\n\t"
  "s_add_u32 s35, s35, s32\n\t"
  "s_add_u32 s31, s31, s33\n\t"
  "v_mfma_f32_16x16x32_bf16 v[72:75], v[124:127], v[92:95], v[72:75]\n\t"
  "buffer_load_dwordx4 v243, s[16:19], s35 offen offset:2048 lds\n\t"
  "v_mfma_f32_16x16x32_bf16 v[76:79], v[128:131], v[92:95], v[76:79]\n\t"
  "s_waitcnt vmcnt(15) lgkmcnt(0)\n\t"
  "s_barrier\n\t"
  "v_add_u32 v152, 0, v237\n\t"
  "v_add_u32 v153, 0, v238\n\t"
  "v_add_u32 v154, 0, v239\n\t"
  "v_mfma_f32_16x16x32_bf16 v[0:3], v[132:135], v[96:99], v[0:3]\n\t"
  "ds_read_b128 v[80:83], v152 offset:0\n\t"
  "v_mfma_f32_16x16x32_bf16 v[4:7], v[136:139], v[96:99], v[4:7]\n\t"
  "ds_read_b64_tr_b16 v[112:113], v153 offset:0\n\t"
  "v_mfma_f32_16x16x32_bf16 v[8:11], v[140:143], v[96:99], v[8:11]\n\t"
  "ds_read_b64_tr_b16 v[114:115], v153 offset:512\n\t"
  "v_mfma_f32_16x16x32_bf16 v[12:15], v[144:147], v[96:99], v[12:15]\n\t"
  "ds_read_b64_tr_b16 v[116:117], v154 offset:0\n\t"
  "v_mfma_f32_16x16x32_bf16 v[16:19], v[148:151], v[96:99], v[16:19]\n\t"
  "ds_read_b64_tr_b16 v[118:119], v154 offset:512\n\t"
  "v_mfma_f32_16x16x32_bf16 v[20:23], v[132:135], v[100:103], v[20:23]\n\t"
  "ds_read_b128 v[84:87], v152 offset:256\n\t"
  "v_mfma_f32_16x16x32_bf16 v[24:27], v[136:139], v[100:103], v[24:27]\n\t"
  "ds_read_b64_tr_b16 v[120:121], v153 offset:1024\n\t"
  "v_mfma_f32_16x16x32_bf16 v[28:31], v[140:143], v[100:103], v[28:31]\n\t"
  "ds_read_b64_tr_b16 v[122:123], v153 offset:1536\n\t"
  "v_mfma_f32_16x16x32_bf16 v[32:35], v[144:147], v[100:103], v[32:35]\n\t"
  "ds_read_b64_tr_b16 v[124:125], v154 offset:1024\n\t"
  "v_mfma_f32_16x16x32_bf16 v[36:39], v[148:151], v[100:103], v[36:39]\n\t"
  "ds_read_b64_tr_b16 v[126:127], v154 offset:1536\n\t"
  "v_mfma_f32_16x16x32_bf16 v[40:43], v[132:135], v[104:107], v[40:43]\n\t"
  "ds_read_b128 v[88:91], v152 offset:2048\n\t"
  "v_mfma_f32_16x16x32_bf16 v[44:47], v[136:139], v[104:107], v[44:47]\n\t"
  "ds_read_b64_tr_b16 v[128:129], v153 offset:2048\n\t"
  "v_mfma_f32_16x16x32_bf16 v[48:51], v[140:143], v[104:107], v[48:51]\n\t"
  "ds_read_b64_tr_b16 v[130:131], v153 offset:2560\n\t"
  "v_mfma_f32_16x16x32_bf16 v[52:55], v[144:147], v[104:107], v[52:55]\n\t"
  "ds_read_b128 v[92:95], v152 offset:2304\n\t"
  "v_mfma_f32_16x16x32_bf16 v[56:59], v[148:151], v[104:107], v[56:59]\n\t"
  "v_mfma_f32_16x16x32_bf16 v[60:63], v[132:135], v[108:111], v[60:63]\n\t"
  "v_mfma_f32_16x16x32_bf16 v[64:67], v[136:139], v[108:111], v[64:67]\n\t"
  "s_add_u32 m0, s34, 133120\n\t"
  "s_add_u32 s35, s31, s32\n\t"
  "buffer_load_dwordx4 v243, s[16:19], s31 offen lds\n\t"
  "v_mfma_f32_16x16x32_bf16 v[68:71], v[140:143], v[108:111], v[68:71]\n\t"
  "buffer_load_dwordx4 v243, s[16:19], s35 offen offset:1024 lds\n\t"
  "s_add_u32 s35, s35, s32\n\t"
  "s_add_u32 s31, s31, s33\n\t"
  "v_mfma_f32_16x16x32_bf16 v[72:75], v[144:147], v[108:111], v[72:75]\n\t"
  "buffer_load_dwordx4 v243, s[16:19], s35 offen offset:2048 lds\n\t"
  "v_mfma_f32_16x16x32_bf16 v[76:79], v[148:151], v[108:111], v[76:79]\n\t"
  "s_waitcnt vmcnt(14) lgkmcnt(0)\n\t"
  "s_barrier\n\t"
  "v_add_u32 v152, 26624, v237\n\t"
  "v_add_u32 v153, 26624, v238\n\t"
  "v_add_u32 v154, 26624, v239\n\t"
  "v_mfma_f32_16x16x32_bf16 v[0:3], v[112:115], v[80:83], v[0:3]\n\t"
  "ds_read_b128 v[96:99], v152 offset:0\n\t"
  "v_mfma_f32_16x16x32_bf16 v[4:7], v[116:119], v[80:83], v[4:7]\n\t"
  "ds_read_b64_tr_b16 v[132:133], v153 offset:0\n\t"
  "v_mfma_f32_16x16x32_bf16 v[8:11], v[120:123], v[80:83], v[8:11]\n\t"
  "ds_read_b64_tr_b16 v[134:135], v153 offset:512\n\t"
  "v_mfma_f32_16x16x32_bf16 v[12:15], v[124:127], v[80:83], v[12:15]\n\t"
  "ds_read_b64_tr_b16 v[136:137], v154 offset:0\n\t"
  "v_mfma_f32_16x16x32_bf16 v[16:19], v[128:131], v[80:83], v[16:19]\n\t"
  "ds_read_b64_tr_b16 v[138:139], v154 offset:512\n\t"
  "v_mfma_f32_16x16x32_bf16 v[20:23], v[112:115], v[84:87], v[20:23]\n\t"
  "ds_read_b128 v[100:103], v152 offset:256\n\t"
  "v_mfma_f32_16x16x32_bf16 v[24:27], v[116:119], v[84:87], v[24:27]\n\t"
  "ds_read_b64_tr_b16 v[140:141], v153 offset:1024\n\t"
  "v_mfma_f32_16x16x32_bf16 v[28:31], v[120:123], v[84:87], v[28:31]\n\t"
  "ds_read_b64_tr_b16 v[142:143], v153 offset:1536\n\t"
  "v_mfma_f32_16x16x32_bf16 v[32:35], v[124:127], v[84:87], v[32:35]\n\t"
  "ds_read_b64_tr_b16 v[144:145], v154 offset:1024\n\t"
  "v_mfma_f32_16x16x32_bf16 v[36:39], v[128:131], v[84:87], v[36:39]\n\t"
  "ds_read_b64_tr_b16 v[146:147], v154 offset:1536\n\t"
  "v_mfma_f32_16x16x32_bf16 v[40:43], v[112:115], v[88:91], v[40:43]\n\t"
  "ds_read_b128 v[104:107], v152 offset:2048\n\t"
  "v_mfma_f32_16x16x32_bf16 v[44:47], v[116:119], v[88:91], v[44:47]\n\t"
  "ds_read_b64_tr_b16 v[148:149], v153 offset:2048\n\t"
  "v_mfma_f32_16x16x32_bf16 v[48:51], v[120:123], v[88:91], v[48:51]\n\t"
  "ds_read_b64_tr_b16 v[150:151], v153 offset:2560\n\t"
  "v_mfma_f32_16x16x32_bf16 v[52:55], v[124:127], v[88:91], v[52:55]\n\t"
  "ds_read_b128 v[108:111], v152 offset:2304\n\t"
  "v_mfma_f32_16x16x32_bf16 v[56:59], v[128:131], v[88:91], v[56:59]\n\t"
  "v_mfma_f32_16x16x32_bf16 v[60:63], v[112:115], v[92:95], v[60:63]\n\t"
  "v_mfma_f32_16x16x32_bf16 v[64:67], v[116:119], v[92:95], v[64:67]\n\t"
  "s_add_u32 m0, s34, 0\n\t"
  "s_add_u32 s35, s31, s32\n\t"
  "buffer_load_dwordx4 v243, s[16:19], s31 offen lds\n\t"
  "v_mfma_f32_16x16x32_bf16 v[68:71], v[120:123], v[92:95], v[68:71]\n\t"
  "buffer_load_dwordx4 v243, s[16:19], s35 offen offset:1024 lds\n\t"
  "s_add_u32 s35, s35, s32\n\t"
  "s_add_u32 s31, s31, s33\n\t"
  "v_mfma_f32_16x16x32_bf16 v[72:75], v[124:127], v[92:95], v[72:75]\n\t"
  "buffer_load_dwordx4 v243, s[16:19], s35 offen offset:2048 lds\n\t"
  "v_mfma_f32_16x16x32_bf16 v[76:79], v[128:131], v[92:95], v[76:79]\n\t"
  "s_waitcnt vmcnt(13) lgkmcnt(0)\n\t"
  "s_barrier\n\t"
  "v_add_u32 v152, 53248, v237\n\t"
  "v_add_u32 v153, 53248, v238\n\t"
  "v_add_u32 v154, 53248, v239\n\t"
  "v_mfma_f32_16x16x32_bf16 v[0:3], v[132:135], v[96:99], v[0:3]\n\t"
  "ds_read_b128 v[80:83], v152 offset:0\n\t"
  "v_mfma_f32_16x16x32_bf16 v[4:7], v[136:139], v[96:99], v[4:7]\n\t"
  "ds_read_b64_tr_b16 v[112:113], v153 offset:0\n\t"
  "v_mfma_f32_16x16x32_bf16 v[8:11], v[140:143], v[96:99], v[8:11]\n\t"
  "ds_read_b64_tr_b16 v[114:115], v153 offset:512\n\t"
  "v_mfma_f32_16x16x32_bf16 v[12:15], v[144:147], v[96:99], v[12:15]\n\t"
  "ds_read_b64_tr_b16 v[116:117], v154 offset:0\n\t"
  "v_mfma_f32_16x16x32_bf16 v[16:19], v[148:151], v[96:99], v[16:19]\n\t"
  "ds_read_b64_tr_b16 v[118:119], v154 offset:512\n\t"
  "v_mfma_f32_16x16x32_bf16 v[20:23], v[132:135], v[100:103], v[20:23]\n\t"
  "ds_read_b128 v[84:87], v152 offset:256\n\t"
  "v_mfma_f32_16x16x32_bf16 v[24:27], v[136:139], v[100:103], v[24:27]\n\t"
  "ds_read_b64_tr_b16 v[120:121], v153 offset:1024\n\t"
  "v_mfma_f32_16x16x32_bf16 v[28:31], v[140:143], v[100:103], v[28:31]\n\t"
  "ds_read_b64_tr_b16 v[122:123], v153 offset:1536\n\t"
  "v_mfma_f32_16x16x32_bf16 v[32:35], v[144:147], v[100:103], v[32:35]\n\t"
  "ds_read_b64_tr_b16 v[124:125], v154 offset:1024\n\t"
  "v_mfma_f32_16x16x32_bf16 v[36:39], v[148:151], v[100:103], v[36:39]\n\t"
  "ds_read_b64_tr_b16 v[126:127], v154 offset:1536\n\t"
  "v_mfma_f32_16x16x32_bf16 v[40:43], v[132:135], v[104:107], v[40:43]\n\t"
  "ds_read_b128 v[88:91], v152 offset:2048\n\t"
  "v_mfma_f32_16x16x32_bf16 v[44:47], v[136:139], v[104:107], v[44:47]\n\t"
  "ds_read_b64_tr_b16 v[128:129], v153 offset:2048\n\t"
  "v_mfma_f32_16x16x32_bf16 v[48:51], v[140:143], v[104:107], v[48:51]\n\t"
  "ds_read_b64_tr_b16 v[130:131], v153 offset:2560\n\t"
  "v_mfma_f32_16x16x32_bf16 v[52:55], v[144:147], v[104:107], v[52:55]\n\t"
  "ds_read_b128 v[92:95], v152 offset:2304\n\t"
  "v_mfma_f32_16x16x32_bf16 v[56:59], v[148:151], v[104:107], v[56:59]\n\t"
  "v_mfma_f32_16x16x32_bf16 v[60:63], v[132:135], v[108:111], v[60:63]\n\t"
  "v_mfma_f32_16x16x32_bf16 v[64:67], v[136:139], v[108:111], v[64:67]\n\t"
  "s_add_u32 m0, s34, 26624\n\t"
  "s_add_u32 s35, s31, s32\n\t"
  "buffer_load_dwordx4 v243, s[16:19], s31 offen lds\n\t"
  "v_mfma_f32_16x16x32_bf16 v[68:71], v[140:143], v[108:111], v[68:71]\n\t"
  "buffer_load_dwordx4 v243, s[16:19], s35 offen offset:1024 lds\n\t"
  "s_add_u32 s35, s35, s32\n\t"
  "s_add_u32 s31, s31, s33\n\t"
  "v_mfma_f32_16x16x32_bf16 v[72:75], v[144:147], v[108:111], v[72:75]\n\t"
  "buffer_load_dwordx4 v243, s[16:19], s35 offen offset:2048 lds\n\t"
  "v_mfma_f32_16x16x32_bf16 v[76:79], v[148:151], v[108:111], v[76:79]\n\t"
  "s_waitcnt vmcnt(12) lgkmcnt(0)\n\t"
  "s_barrier\n\t"
  "v_add_u32 v152, 79872, v237\n\t"
  "v_add_u32 v153, 79872, v238\n\t"
  "v_add_u32 v154, 79872, v239\n\t"
  "v_mfma_f32_16x16x32_bf16 v[0:3], v[112:115], v[80:83], v[0:3]\n\t"
  "ds_read_b128 v[96:99], v152 offset:0\n\t"
  "v_mfma_f32_16x16x32_bf16 v[4:7], v[116:119], v[80:83], v[4:7]\n\t"
  "ds_read_b64_tr_b16 v[132:133], v153 offset:0\n\t"
  "v_mfma_f32_16x16x32_bf16 v[8:11], v[120:123], v[80:83], v[8:11]\n\t"
  "ds_read_b64_tr_b16 v[134:135], v153 offset:512\n\t"
  "v_mfma_f32_16x16x32_bf16 v[12:15], v[124:127], v[80:83], v[12:15]\n\t"
  "ds_read_b64_tr_b16 v[136:137], v154 offset:0\n\t"
  "v_mfma_f32_16x16x32_bf16 v[16:19], v[128:131], v[80:83], v[16:19]\n\t"
  "ds_read_b64_tr_b16 v[138:139], v154 offset:512\n\t"
  "v_mfma_f32_16x16x32_bf16 v[20:23], v[112:115], v[84:87], v[20:23]\n\t"
  "ds_read_b128 v[100:103], v152 offset:256\n\t"
  "v_mfma_f32_16x16x32_bf16 v[24:27], v[116:119], v[84:87], v[24:27]\n\t"
  "ds_read_b64_tr_b16 v[140:141], v153 offset:1024\n\t"
  "v_mfma_f32_16x16x32_bf16 v[28:31], v[120:123], v[84:87], v[28:31]\n\t"
  "ds_read_b64_tr_b16 v[142:143], v153 offset:1536\n\t"
  "v_mfma_f32_16x16x32_bf16 v[32:35], v[124:127], v[84:87], v[32:35]\n\t"
  "ds_read_b64_tr_b16 v[144:145], v154 offset:1024\n\t"
  "v_mfma_f32_16x16x32_bf16 v[36:39], v[128:131], v[84:87], v[36:39]\n\t"
  "ds_read_b64_tr_b16 v[146:147], v154 offset:1536\n\t"
  "v_mfma_f32_16x16x32_bf16 v[40:43], v[112:115], v[88:91], v[40:43]\n\t"
  "ds_read_b128 v[104:107], v152 offset:2048\n\t"
  "v_mfma_f32_16x16x32_bf16 v[44:47], v[116:119], v[88:91], v[44:47]\n\t"
  "ds_read_b64_tr_b16 v[148:149], v153 offset:2048\n\t"
  "v_mfma_f32_16x16x32_bf16 v[48:51], v[120:123], v[88:91], v[48:51]\n\t"
  "ds_read_b64_tr_b16 v[150:151], v153 offset:2560\n\t"
  "v_mfma_f32_16x16x32_bf16 v[52:55], v[124:127], v[88:91], v[52:55]\n\t"
  "ds_read_b128 v[108:111], v152 offset:2304\n\t"
  "v_mfma_f32_16x16x32_bf16 v[56:59], v[128:131], v[88:91], v[56:59]\n\t"
  "v_mfma_f32_16x16x32_bf16 v[60:63], v[112:115], v[92:95], v[60:63]\n\t"
  "s_add_u32 m0, s34, 53248\n\t"
  "s_add_u32 s35, s31, s32\n\t"
  "buffer_load_dwordx4 v243, s[16:19], s31 offen lds\n\t"
  "v_mfma_f32_16x16x32_bf16 v[64:67], v[116:119], v[92:95], v[64:67]\n\t"
  "buffer_load_dwordx4 v243, s[16:19], s35 offen offset:1024 lds\n\t"
  "s_add_u32 s35, s35, s32\n\t"
  "s_add_u32 s31, s31, s33\n\t"
  "v_mfma_f32_16x16x32_bf16 v[68:71], v[120:123], v[92:95], v[68:71]\n\t"
  "s_lshr_b32 s35, s22, 1\n\t"
  "s_add_u32 s35, s35, 3072\n\t"
  "buffer_load_dwordx4 v223, s[12:15], s35 offen offset:2048 sc1 lds\n\t"
  "v_mfma_f32_16x16x32_bf16 v[72:75], v[124:127], v[92:95], v[72:75]\n\t"
  "s_and_b32 s35, s22, 0x800\n\t"
  "s_lshr_b32 s35, s35, 1\n\t"
  "s_add_u32 m0, s35, 159744\n\t"
  "s_add_u32 s35, s35, 13312\n\t"
  "buffer_load_dwordx4 v223, s[12:15], s35 offen sc1 lds\n\t"
  "v_mfma_f32_16x16x32_bf16 v[76:79], v[128:131], v[92:95], v[76:79]\n\t"
  "s_waitcnt lgkmcnt(0)\n\t"
  "s_barrier\n\t"
  "v_mfma_f32_16x16x32_bf16 v[0:3], v[132:135], v[96:99], v[0:3]\n\t"
  "v_mfma_f32_16x16x32_bf16 v[4:7], v[136:139], v[96:99], v[4:7]\n\t"
  "v_mfma_f32_16x16x32_bf16 v[8:11], v[140:143], v[96:99], v[8:11]\n\t"
  "v_mfma_f32_16x16x32_bf16 v[12:15], v[144:147], v[96:99], v[12:15]\n\t"
  "v_mfma_f32_16x16x32_bf16 v[16:19], v[148:151], v[96:99], v[16:19]\n\t"
  "v_mfma_f32_16x16x32_bf16 v[20:23], v[132:135], v[100:103], v[20:23]\n\t"
  "v_mfma_f32_16x16x32_bf16 v[24:27], v[136:139], v[100:103], v[24:27]\n\t"
  "v_mfma_f32_16x16x32_bf16 v[28:31], v[140:143], v[100:103], v[28:31]\n\t"
  "v_mfma_f32_16x16x32_bf16 v[32:35], v[144:147], v[100:103], v[32:35]\n\t"
  "v_mfma_f32_16x16x32_bf16 v[36:39], v[148:151], v[100:103], v[36:39]\n\t"
  "v_mfma_f32_16x16x32_bf16 v[40:43], v[132:135], v[104:107], v[40:43]\n\t"
  "v_mfma_f32_16x16x32_bf16 v[44:47], v[136:139], v[104:107], v[44:47]\n\t"
  "v_mfma_f32_16x16x32_bf16 v[48:51], v[140:143], v[104:107], v[48:51]\n\t"
  "v_mfma_f32_16x16x32_bf16 v[52:55], v[144:147], v[104:107], v[52:55]\n\t"
  "v_mfma_f32_16x16x32_bf16 v[56:59], v[148:151], v[104:107], v[56:59]\n\t"
  "v_mfma_f32_16x16x32_bf16 v[60:63], v[132:135], v[108:111], v[60:63]\n\t"
  "v_mfma_f32_16x16x32_bf16 v[64:67], v[136:139], v[108:111], v[64:67]\n\t"
  "v_mfma_f32_16x16x32_bf16 v[68:71], v[140:143], v[108:111], v[68:71]\n\t"
  "v_mfma_f32_16x16x32_bf16 v[72:75], v[144:147], v[108:111], v[72:75]\n\t"
  "v_mfma_f32_16x16x32_bf16 v[76:79], v[148:151], v[108:111], v[76:79]\n\t"
  "s_nop 7\n\t"
  "v_cvt_pk_bf16_f32 v164, v0, v1\n\t"
  "v_cvt_pk_bf16_f32 v165, v2, v3\n\t"
  "v_cvt_pk_bf16_f32 v166, v20, v21\n\t"
  "v_cvt_pk_bf16_f32 v167, v22, v23\n\t"
  "buffer_store_dwordx4 v[164:167], v242, s[8:11], s24 offen offset:0 sc1\n\t"
  "v_cvt_pk_bf16_f32 v168, v4, v5\n\t"
  "v_cvt_pk_bf16_f32 v169, v6, v7\n\t"
  "v_cvt_pk_bf16_f32 v170, v24, v25\n\t"
  "v_cvt_pk_bf16_f32 v171, v26, v27\n\t"
  "buffer_store_dwordx4 v[168:171], v242, s[8:11], s24 offen offset:256 sc1\n\t"
  "v_cvt_pk_bf16_f32 v172, v8, v9\n\t"
  "v_cvt_pk_bf16_f32 v173, v10, v11\n\t"
  "v_cvt_pk_bf16_f32 v174, v28, v29\n\t"
  "v_cvt_pk_bf16_f32 v175, v30, v31\n\t"
  "buffer_store_dwordx4 v[172:175], v242, s[8:11], s24 offen offset:512 sc1\n\t"
  "v_cvt_pk_bf16_f32 v176, v12, v13\n\t"
  "v_cvt_pk_bf16_f32 v177, v14, v15\n\t"
  "v_cvt_pk_bf16_f32 v178, v32, v33\n\t"
  "v_cvt_pk_bf16_f32 v179, v34, v35\n\t"
  "buffer_store_dwordx4 v[176:179], v242, s[8:11], s24 offen offset:768 sc1\n\t"
  "v_cvt_pk_bf16_f32 v180, v16, v17\n\t"
  "v_cvt_pk_bf16_f32 v181, v18, v19\n\t"
  "v_cvt_pk_bf16_f32 v182, v36, v37\n\t"
  "v_cvt_pk_bf16_f32 v183, v38, v39\n\t"
  "buffer_store_dwordx4 v[180:183], v242, s[8:11], s24 offen offset:1024 sc1\n\t"
  "v_cvt_pk_bf16_f32 v184, v40, v41\n\t"
  "v_cvt_pk_bf16_f32 v185, v42, v43\n\t"
  "v_cvt_pk_bf16_f32 v186, v60, v61\n\t"
  "v_cvt_pk_bf16_f32 v187, v62, v63\n\t"
  "buffer_store_dwordx4 v[184:187], v242, s[8:11], s25 offen offset:0 sc1\n\t"
  "v_cvt_pk_bf16_f32 v188, v44, v45\n\t"
  "v_cvt_pk_bf16_f32 v189, v46, v47\n\t"
  "v_cvt_pk_bf16_f32 v190, v64, v65\n\t"
  "v_cvt_pk_bf16_f32 v191, v66, v67\n\t"
  "buffer_store_dwordx4 v[188:191], v242, s[8:11], s25 offen offset:256 sc1\n\t"
  "v_cvt_pk_bf16_f32 v192, v48, v49\n\t"
  "v_cvt_pk_bf16_f32 v193, v50, v51\n\t"
  "v_cvt_pk_bf16_f32 v194, v68, v69\n\t"
  "v_cvt_pk_bf16_f32 v195, v70, v71\n\t"
  "buffer_store_dwordx4 v[192:195], v242, s[8:11], s25 offen offset:512 sc1\n\t"
  "v_cvt_pk_bf16_f32 v196, v52, v53\n\t"
  "v_cvt_pk_bf16_f32 v197, v54, v55\n\t"
  "v_cvt_pk_bf16_f32 v198, v72, v73\n\t"
  "v_cvt_pk_bf16_f32 v199, v74, v75\n\t"
  "buffer_store_dwordx4 v[196:199], v242, s[8:11], s25 offen offset:768 sc1\n\t"
  "v_cvt_pk_bf16_f32 v200, v56, v57\n\t"
  "v_cvt_pk_bf16_f32 v201, v58, v59\n\t"
  "v_cvt_pk_bf16_f32 v202, v76, v77\n\t"
  "v_cvt_pk_bf16_f32 v203, v78, v79\n\t"
  "buffer_store_dwordx4 v[200:203], v242, s[8:11], s25 offen offset:1024 sc1\n\t"
  "s_branch LOPend_%=\n\t"
  "LOPcs_%=:\n\t"
  "buffer_load_dword v156, v155, s[12:15], s37 offen sc1\n\t"
  "s_waitcnt vmcnt(0)\n\t"
  "s_sleep 1\n\t"
  "s_add_u32 s36, s36, 1\n\t"
  "s_cmp_lt_u32 s36, 131072\n\t"
  "s_cbranch_scc1 LOPcc_%=\n\t"
  "s_branch LOPco_%=\n\t"
  "LOPend_%=:\n\t"
  "s_nop 1\n\t"
  : "+{v[72:87]}"(q0), "+{v[88:103]}"(q1), "+{v216}"(kaddr), "+{v217}"(vaddr), "+{s60}"(ca), "+{v68}"(ones),
    "+{s20}"(aoff), "+{s21}"(woff), "+{s31}"(foff)
  : "{v222}"(lim), "{v223}"(doff), "{v225}"(qbaddr), "{s[40:43]}"(sk), "{s44}"(kb), "{s[48:51]}"(sc),
    "{s52}"(na), "{s59}"(sbase), "{s61}"(cb), "{v[234:235]}"(pfa), "{s80}"(odst),
    "{v237}"(ba), "{v238}"(bw0), "{v239}"(bw1), "{v240}"(vw0), "{v241}"(vw1), "{v242}"(po), "{v243}"(vf),
    "{s[4:7]}"(sw), "{s[8:11]}"(spt), "{s[12:15]}"(sf), "{s[16:19]}"(sfw), "{s24}"(poff), "{s26}"(aend), "{s27}"(wend), "{s28}"(tag),
    "{s29}"(cfl), "{s30}"(uw), "{s32}"(fj), "{s33}"(fi)
  : "v0", "v1", "v2", "v3", "v4", "v5", "v6", "v7", "v8", "v9", "v10", "v11", "v12", "v13", "v14", "v15", "v16", "v17", "v18", "v19", "v20", "v21", "v22", "v23", "v24", "v25", "v26", "v27", "v28", "v29", "v30", "v31", "v32", "v33", "v34", "v35", "v36", "v37", "v38", "v39", "v40", "v41", "v42", "v43", "v44", "v45", "v46", "v47", "v48", "v49", "v50", "v51", "v52", "v53", "v54", "v55", "v56", "v57", "v58", "v59", "v60", "v61", "v62", "v63", "v64", "v65", "v66", "v67", "v69", "v70", "v71", "v104", "v105", "v106", "v107", "v108", "v109", "v110", "v111", "v112", "v113", "v114", "v115", "v116", "v117", "v118", "v119", "v120", "v121", "v122", "v123", "v124", "v125", "v126", "v127", "v128", "v129", "v130", "v131", "v132", "v133", "v134", "v135", "v136", "v137", "v138", "v139", "v140", "v141", "v142", "v143", "v144", "v145", "v146", "v147", "v148", "v149", "v150", "v151", "v152", "v153", "v154", "v155", "v156", "v157", "v158", "v159", "v160", "v161", "v162", "v163", "v164", "v165", "v166", "v167", "v168", "v169", "v170", "v171", "v172", "v173", "v174", "v175", "v176", "v177", "v178", "v179", "v180", "v181", "v182", "v183", "v184", "v185", "v186", "v187", "v188", "v189", "v190", "v191", "v192", "v193", "v194", "v195", "v196", "v197", "v198", "v199", "v200", "v201", "v202", "v203", "v204", "v205", "v206", "v207", "v208", "v209", "v210", "v211", "v212", "v213", "v214", "v215", "v218", "v219", "v220", "v221", "v224", "v226", "v227", "v228", "v229", "v230", "v231", "v232", "v233", "v236", "s22", "s23", "s25", "s34", "s35", "s36", "s37", "s45", "s46", "s47", "s53", "s54", "s55", "s56", "s57", "s58", "s62", "s63", "s64", "s65", "s66", "s67", "s68", "s69", "s70", "s71", "s72", "s73", "s74", "s75", "s76", "s77", "s78", "s79", "vcc", "scc", "memory");
}
DI int lane_id() { unsigned l; asm volatile("v_mbcnt_lo_u32_b32 %0, -1, 0\n\tv_mbcnt_hi_u32_b32 %0, -1, %0" : "=v"(l)); return (int)l; }

#define LD128(srd, off) __builtin_amdgcn_raw_buffer_load_b128((srd), (off), 0, 16)
#define LD64(srd, off) __builtin_amdgcn_raw_buffer_load_b64((srd), (off), 0, 16)
#define LD32(srd, off) __builtin_amdgcn_raw_buffer_load_b32((srd), (off), 0, 16)
#define ST128(v, srd, off) __builtin_amdgcn_raw_buffer_store_b128((v), (srd), (off), 0, 16)
#define ST64(v, srd, off) __builtin_amdgcn_raw_buffer_store_b64((v), (srd), (off), 0, 16)
#define ST32(v, srd, off) __builtin_amdgcn_raw_buffer_store_b32((v), (srd), (off), 0, 16)
constexpr int SPIN_MAX = 1 << 17;
constexpr int H2REP = 2, UPSLEEP = 12, DNSLEEP = 4;
constexpr unsigned T_ATT = 3 * 26624, T_CAND = T_ATT + 20480, T_RED = 4 * 26624, T_PUB = 0;

DI void tail_phase(const M1Args& A, unsigned tag, int xcd, int idx, int wid, int lane) {
  const int bid = blockIdx.x, tid = wid * 64 + lane, h = xcd * 4 + (idx >> 3), xq = idx & 7;
  const int hh = lane >> 5, l32 = lane & 31;
  const bool up = bid < 54, down = bid >= 54 && bid < 74;
  srd_t sf = mk_srd(A.ws + WS_FLG, FL_BYTES);
  const int rbl = h >> 2, rbk = ((rbl < 4) ? xq : 15 - xq) * 4 + (rbl & 3), qs = h & 3;
  const int row = rbk * 32 + (wid >> 2) * 16 + qs * 4 + (wid & 3);
  int spins = 0;
  typedef __attribute__((address_space(1))) u16 gu16;
  const gu16 *px_ = (const gu16*)A.x, *pwr = (const gu16*)A.wr; gu16* pout = (gu16*)A.out;
  asm volatile("" : "+s"(px_), "+s"(pwr), "+s"(pout));
  if (tid == 0) *(volatile LAS unsigned*)(uintptr_t)(T_CAND + 512) = 0u;
  WAITV(0);
  BARRIER();
  if (tid == 0) ST32(tag, sf, FL_P + xq * 128 + h * 4);
  float xf_[10], wf_[10], mf[4][10];
  auto side = [&]() {
    const u32x2 xa = *(const GAS u32x2*)(px_ + (size_t)row * DM + 4 * lane), xb = *(const GAS u32x2*)(px_ + (size_t)row * DM + 256 + 4 * lane);
    const u32x2 wa_ = *(const GAS u32x2*)(pwr + 4 * lane), wb_ = *(const GAS u32x2*)(pwr + 256 + 4 * lane);
    const unsigned xw[5] = {xa[0], xa[1], xb[0], xb[1], *(const GAS unsigned*)(px_ + (size_t)row * DM + 512 + 2 * lane)};
    const unsigned ww[5] = {wa_[0], wa_[1], wb_[0], wb_[1], *(const GAS unsigned*)(pwr + 512 + 2 * lane)};
    auto ma = [](unsigned b) { const unsigned pc = b >> 10; return (pc < 8 ? 2 * 26624 + pc * 3072 + 2048 : 6 * 26624 + (pc - 8) * 1024) + (b & 1023); };
    u32x4 mq[4][2]; u32x2 mh[4];
#pragma unroll
    for (int vv = 0; vv < 4; ++vv) {
      mq[vv][0] = *(const LAS u32x4*)(uintptr_t)ma(vv * 2560 + lane * 16); mq[vv][1] = *(const LAS u32x4*)(uintptr_t)ma(vv * 2560 + 1024 + lane * 16);
      mh[vv] = *(const LAS u32x2*)(uintptr_t)ma(vv * 2560 + 2048 + lane * 8);
    }
    for (;;) {
      unsigned bad = 0;
#pragma unroll
      for (int vv = 0; vv < 4; ++vv) {
        bad |= (mq[vv][0][1] ^ tag) | (mq[vv][0][3] ^ tag) | (mq[vv][1][1] ^ tag) | (mq[vv][1][3] ^ tag) | (mh[vv][1] ^ tag);
        asm volatile("" : "+v"(bad));
      }
      if (!__any(bad != 0) || ++spins > SPIN_MAX) break;
      asm volatile("" ::: "memory");
#pragma unroll
      for (int vv = 0; vv < 4; ++vv) {
        mq[vv][0] = LD128(sf, FL_MG + (2 + vv) * 2560 + lane * 16); mq[vv][1] = LD128(sf, FL_MG + (2 + vv) * 2560 + 1024 + lane * 16);
        mh[vv] = LD64(sf, FL_MG + (2 + vv) * 2560 + 2048 + lane * 8);
      }
    }
#pragma unroll
    for (int vv = 0; vv < 4; ++vv) {
      const unsigned m5[5] = {mq[vv][0][0], mq[vv][0][2], mq[vv][1][0], mq[vv][1][2], mh[vv][0]};
#pragma unroll
      for (int g = 0; g < 5; ++g) { mf[vv][2 * g] = bflo(m5[g]); mf[vv][2 * g + 1] = bfhi(m5[g]); asm volatile("" : "+v"(mf[vv][2 * g]), "+v"(mf[vv][2 * g + 1])); }
    }
#pragma unroll
    for (int g = 0; g < 5; ++g) {
      xf_[2 * g] = bflo(xw[g]); xf_[2 * g + 1] = bfhi(xw[g]); wf_[2 * g] = bflo(ww[g]); wf_[2 * g + 1] = bfhi(ww[g]);
      asm volatile("" : "+v"(xf_[2 * g]), "+v"(xf_[2 * g + 1]), "+v"(wf_[2 * g]), "+v"(wf_[2 * g + 1]));
    }
  };
  bf16x8 wfr[14];
  {
    srd_t spt = mk_srd(A.ws + WS_PART, 8 * SEQ * DM * 2);
    u32x4 w[2][8];
    const unsigned off = (unsigned)((rbk * 4 + qs) * 81920 + tid * 16);
    volatile LAS unsigned* post = (volatile LAS unsigned*)(uintptr_t)(T_CAND + 512);
    unsigned have = 0;
    auto poll = [&]() {
      unsigned m = (unsigned)__ballot(LD32(sf, FL_P + xq * 128 + l32 * 4) == tag);
      m &= m >> 1; m &= m >> 2;
      unsigned rdy = m & 0x11111111u;
      if (++spins > SPIN_MAX) rdy = 0x11111111u;
      if (lane == 0) *post = rdy;
      return rdy;
    };
    if (wid == 7) poll();
    side();
  if (up || down) {
    const unsigned wa = wid * 3072 + (hh * 8 + ((lane & 15) >> 2)) * 64 + ((lane >> 4) & 1) * 32 + (lane & 3) * 8;
#pragma unroll
    for (int i = 0; i < 14; ++i) {
      const unsigned a = wa + ((4 + i / 3) % 6) * 26624 + (i % 3) * 1024;
      wfr[i] = cat8(lds_tr(a), lds_tr(a + 256));
    }
#pragma unroll
    for (int i = 0; i < 14; ++i) asm volatile("" : "+v"(wfr[i]));
  }
    for (;;) {
      asm volatile("" ::: "memory");
      const unsigned rdy = wid == 7 ? poll() : rfl(*post);
      const unsigned todo = rdy & ~have;
#pragma unroll
      for (int sp = 0; sp < 8; ++sp) {
        if (todo & (1u << (4 * sp))) {
          w[0][sp] = __builtin_amdgcn_raw_buffer_load_b128(spt, off, sp * 10240, 16);
          if (wid < 2) w[1][sp] = __builtin_amdgcn_raw_buffer_load_b128(spt, off + 8192, sp * 10240, 16);
        }
      }
      have |= todo;
      if (have == 0x11111111u) break;
      __builtin_amdgcn_s_sleep(2);
    }
#pragma unroll
    for (int rep = 0; rep < 2; ++rep) {
      if (rep == 0 || wid < 2) {
        const int it = tid + rep * 512, ql = it & 3, cq = it >> 2;
        f32x4 a0 = {0, 0, 0, 0}, a1 = {0, 0, 0, 0};
#pragma unroll
        for (int sp = 0; sp < 8; ++sp) {
          a0[0] += bflo(w[rep][sp][0]); a0[1] += bfhi(w[rep][sp][0]); a0[2] += bflo(w[rep][sp][1]); a0[3] += bfhi(w[rep][sp][1]);
          a1[0] += bflo(w[rep][sp][2]); a1[1] += bfhi(w[rep][sp][2]); a1[2] += bflo(w[rep][sp][3]); a1[3] += bfhi(w[rep][sp][3]);
        }
        *(LAS f32x4*)(uintptr_t)(T_ATT + (ql * DM + cq * 4) * 4) = a0;
        *(LAS f32x4*)(uintptr_t)(T_ATT + ((4 + ql) * DM + cq * 4) * 4) = a1;
      }
    }
  }
  BARRIER();
  float v[10]; unsigned ow[5], x1w[5], h2w[5];
  {
    const f32x4 a0 = *(const LAS f32x4*)(uintptr_t)(T_ATT + (wid * DM + 4 * lane) * 4), a1 = *(const LAS f32x4*)(uintptr_t)(T_ATT + (wid * DM + 256 + 4 * lane) * 4);
    const f32x2 a2 = *(const LAS f32x2*)(uintptr_t)(T_ATT + (wid * DM + 512 + 2 * lane) * 4);
    const float a[10] = {a0[0], a0[1], a0[2], a0[3], a1[0], a1[1], a1[2], a1[3], a2[0], a2[1]};
#pragma unroll
    for (int e = 0; e < 10; ++e) v[e] = rbh(xf_[e] + rbh(mf[0][e] * rbh(a[e])));
  }
  unsigned rbits;
  {
    float mean, den; ln_stats(v, mean, den);
    const float rden = __builtin_amdgcn_rcpf(den);
    float rs = 0, hv[10];
#pragma unroll
    for (int e = 0; e < 10; ++e) { hv[e] = rbh(rbh(rbh(rbh(v[e] - mean) * rden) * mf[2][e]) + mf[1][e]); }
#pragma unroll
    for (int g = 0; g < 5; ++g) rs += hv[2 * g] * wf_[2 * g] + hv[2 * g + 1] * wf_[2 * g + 1];
    rbits = cvtpk(wave_sum(rs), 0.f) & 0xffffu;
    const unsigned ok = (rbits & 0x8000u) ? (~rbits & 0xffffu) : (rbits | 0x8000u);
    if (lane == 0) { *(LAS u16*)(uintptr_t)(T_CAND + 256 + wid * 2) = (u16)ok; *(LAS unsigned*)(uintptr_t)(T_CAND + 288 + wid * 4) = (ok << 16) | (2047u - row); }
    BARRIER();
    if (tid < 32) ST64((u32x2{*(const LAS unsigned*)(uintptr_t)(T_CAND + 256 + (tid & 3) * 4), tag}), sf, FL_R + (tid >> 2) * 8192 + (rbk * 4 + qs) * 32 + (tid & 3) * 8);
    __builtin_amdgcn_sched_barrier(0);
#pragma unroll
    for (int e = 0; e < 10; ++e) asm volatile("" : "+v"(hv[e]));
#pragma unroll
    for (int g = 0; g < 5; ++g) {
      h2w[g] = cvtpk(hv[2 * g], hv[2 * g + 1]);
      ow[g] = cvtpk(v[2 * g] + rbh(mf[3][2 * g] * hv[2 * g]), v[2 * g + 1] + rbh(mf[3][2 * g + 1] * hv[2 * g + 1]));
      x1w[g] = cvtpk(v[2 * g], v[2 * g + 1]);
      asm volatile("" : "+v"(ow[g]), "+v"(x1w[g]));
    }
  }
  u32x4 ph[3], px[3];
  {
    const unsigned hb = T_PUB + wid * 6144;
    *(LAS u32x4*)(uintptr_t)(hb + ((lane >> 3 & 3) * 48 + 32 + (lane >> 1 & 3) * 4 + 2 + (lane & 1)) * 16) = u32x4{0u, tag, 0u, tag};
    auto piece = [](unsigned quad) { const unsigned k16 = quad >> 2, w4 = (k16 * 26) >> 8, pp = k16 - 10 * w4;
                                     return ((w4 * 3 + (pp >> 2)) * 16 + (quad & 1) * 8 + ((quad >> 1) & 1) * 4 + (pp & 3)) * 16; };
    *(LAS u32x4*)(uintptr_t)(hb + piece(lane)) = u32x4{h2w[0], tag, h2w[1], tag};
    *(LAS u32x4*)(uintptr_t)(hb + piece(64 + lane)) = u32x4{h2w[2], tag, h2w[3], tag};
    *(LAS u32x2*)(uintptr_t)(hb + piece(128 + (lane >> 1)) + (lane & 1) * 8) = u32x2{h2w[4], tag};
    *(LAS u32x2*)(uintptr_t)(hb + 3072 + lane * 8) = u32x2{x1w[4], tag};
    asm volatile("s_waitcnt lgkmcnt(0)" ::: "memory");
#pragma unroll
    for (int j = 0; j < 3; ++j) ph[j] = *(const LAS u32x4*)(uintptr_t)(hb + j * 1024 + lane * 16);
    px[0] = u32x4{x1w[0], tag, x1w[1], tag}; px[1] = u32x4{x1w[2], tag, x1w[3], tag}; px[2] = *(const LAS u32x4*)(uintptr_t)(hb + 3072 + l32 * 16);
  }
  {
    const u32x4 ka = *(const LAS u32x4*)(uintptr_t)(T_CAND + 288), kb = *(const LAS u32x4*)(uintptr_t)(T_CAND + 304);
    unsigned r0 = 2047u - (unsigned)((tid >> 3) * 32 + (tid & 1) * 16 + ((tid >> 1) & 3) * 4);
    asm volatile("" : "+v"(r0));
    u32x4 g0;
    for (;;) {
      asm volatile("" ::: "memory");
      g0 = LD128(sf, FL_R + xcd * 8192 + tid * 16);
      if (!__any(((g0[1] ^ tag) | (g0[3] ^ tag)) != 0) || ++spins > SPIN_MAX) break;
      __builtin_amdgcn_s_sleep(3);
    }
    const unsigned key[4] = {(g0[0] << 16) | r0, (g0[0] & 0xffff0000u) | (r0 - 1), (g0[2] << 16) | (r0 - 2), (g0[2] & 0xffff0000u) | (r0 - 3)};
    unsigned cnt = 0;
#pragma unroll
    for (int j = 0; j < 8; ++j) {
      const unsigned own = j < 4 ? ka[j & 3] : kb[j & 3];
      int c = 0;
#pragma unroll
      for (int i = 0; i < 4; ++i) c += __builtin_popcountll(__ballot(key[i] > own));
      asm("v_writelane_b32 %0, %1, %2" : "+v"(cnt) : "s"(c), "i"(j));
    }
    if (lane < 8) *(LAS unsigned*)(uintptr_t)(T_CAND + (lane * 8 + wid) * 4) = cnt;
    BARRIER();
    const u32x4 ca = *(const LAS u32x4*)(uintptr_t)(T_CAND + wid * 32), cb = *(const LAS u32x4*)(uintptr_t)(T_CAND + wid * 32 + 16);
    const unsigned rank = rfl(ca[0] + ca[1] + ca[2] + ca[3] + cb[0] + cb[1] + cb[2] + cb[3]);
    if (rank < 8) {
#pragma unroll
      for (int rp = 0; rp < H2REP; ++rp)
#pragma unroll
        for (int j = 0; j < 3; ++j) ST128(ph[j], sf, FL_H2 + rp * 24576 + rank * 3072 + j * 1024 + lane * 16);
      ST128(px[0], sf, FL_X1 + rank * 2560 + lane * 16);
      ST128(px[1], sf, FL_X1 + rank * 2560 + 1024 + lane * 16);
      if (lane < 32) ST128(px[2], sf, FL_X1 + rank * 2560 + 2048 + lane * 16);
      if (lane == 0) ST64((u32x2{(rbits << 16) | (unsigned)row, tag}), sf, FL_SEL + rank * 8);
    } else if (!(up || down)) {
      __builtin_amdgcn_s_sleep(64);
      *(GAS u32x2*)(pout + (size_t)row * DM + 4 * lane) = u32x2{ow[0], ow[1]};
      *(GAS u32x2*)(pout + (size_t)row * DM + 256 + 4 * lane) = u32x2{ow[2], ow[3]};
      *(GAS unsigned*)(pout + (size_t)row * DM + 512 + 2 * lane) = ow[4];
    }
    if (bid == 100 && tid == 0) ST32(tag, sf, FL_EPOCH);
    if (up || down) {
      const int grp = up ? lane & 3 : (lane >> 3) & 3, tok = (tid >> 5) & 7, col = (bid - 54) * 32 + l32;
      u32x2 gbw, x1g, h2g, slg; float sg = 0.f;
      if (down) {
        gbw = LD64(sf, FL_MG + (5 * 320 + (col >> 1)) * 8);
        const unsigned k16 = col >> 4, w4 = (k16 * 26) >> 8, pp = k16 - 10 * w4;
        const unsigned h2a = tok * 3072 + ((w4 * 3 + (pp >> 2)) * 16 + ((col >> 2) & 1) * 8 + ((col >> 3) & 1) * 4 + (pp & 3)) * 16 + ((col >> 1) & 1) * 8;
        for (;;) {
          asm volatile("" ::: "memory");
          __builtin_amdgcn_s_sleep(UPSLEEP);
          x1g = LD64(sf, FL_X1 + (tok * 320 + (col >> 1)) * 8); h2g = LD64(sf, FL_H2 + h2a); slg = LD64(sf, FL_SEL + tok * 8);
          if (!__any(((x1g[1] ^ tag) | (h2g[1] ^ tag) | (slg[1] ^ tag)) != 0) || ++spins > SPIN_MAX) break;
        }
        sg = rbh(1.f / (1.f + __expf(-bfhi(slg[0]))));
      }
      const int np = up ? 10 : 14, k0 = up ? (wid & 3) * 10 : wid * 14;
      const unsigned g2 = up ? 128 : 256;
      u32x4 xf[4];
      for (;;) {
        asm volatile("" ::: "memory");
        __builtin_amdgcn_s_sleep(DNSLEEP);
        unsigned bad = 0;
        u32x4 ga[4][2];
#pragma unroll
        for (int i = 0; i < 4; ++i) {
          const unsigned off = up ? FL_H2 + (bid % H2REP) * 24576 + (l32 >> 2) * 3072 + ((wid & 3) * 3 + i) * 256 + hh * 64 + grp * 16
                                  : FL_ACT + min(k0 + min(4 * i + grp, 13), 107) * 512 + hh * 128 + (lane & 7) * 16;
          if (i < 3 || down) { ga[i][0] = LD128(sf, off); ga[i][1] = LD128(sf, off + g2); } else { ga[i][0] = ga[0][0]; ga[i][1] = ga[0][1]; }
        }
#pragma unroll
        for (int i = 0; i < 4; ++i) {
          bad |= (ga[i][0][1] ^ tag) | (ga[i][0][3] ^ tag) | (ga[i][1][1] ^ tag) | (ga[i][1][3] ^ tag);
          asm volatile("" : "+v"(bad));
          xf[i] = u32x4{ga[i][0][0], ga[i][0][2], ga[i][1][0], ga[i][1][2]};
        }
        if (!__any(bad != 0) || ++spins > SPIN_MAX) break;
      }
      auto msk = [&](int i) { const bool on = grp == (i & 3); u32x4 m;
#pragma unroll
        for (int e = 0; e < 4; ++e) m[e] = on ? xf[i >> 2][e] : 0u;
        return __builtin_bit_cast(bf16x8, m); };
      f32x16 acc = f32x16{};
#pragma unroll
      for (int i = 0; i < 10; ++i) acc = MFMA32(wfr[i], msk(i), acc);
      if (down) {
#pragma unroll
        for (int i = 10; i < 14; ++i) acc = MFMA32(wfr[i], msk(i), acc);
#pragma unroll
        for (int e = 0; e < 16; ++e) acc[e] += __int_as_float(__builtin_amdgcn_update_dpp(0, __float_as_int(acc[e]), 0x128, 0xf, 0xf, false));
      } else {
#pragma unroll
        for (int e = 0; e < 16; ++e) acc[e] += __int_as_float(__builtin_amdgcn_update_dpp(0, __float_as_int(acc[e]), 0xb1, 0xf, 0xf, false));
      }
      if ((grp & 1) == 0) {
#pragma unroll
        for (int q = 0; q < 4; ++q)
          *(LAS f32x4*)(uintptr_t)(T_RED + (((wid * 2 + (grp >> 1)) * 8 + (up ? l32 >> 2 : lane & 7)) * 32 + q * 8 + hh * 4) * 4) = f32x4{acc[4 * q], acc[4 * q + 1], acc[4 * q + 2], acc[4 * q + 3]};
      }
      BARRIER();
      if (up) {
        if (wid < 2) {
          const int pc = tid >> 1, n0 = (pc >> 5) * 16 + ((pc >> 3) & 1) * 8 + ((pc >> 4) & 1) * 4 + (tid & 1) * 2;
          f32x2 s0 = {0, 0}, s1 = {0, 0};
#pragma unroll
          for (int w = 0; w < 8; ++w) {
            s0 += *(const LAS f32x2*)(uintptr_t)(T_RED + (w * 256 + (pc & 7) * 32 + n0) * 4);
            s1 += *(const LAS f32x2*)(uintptr_t)(T_RED + ((8 + w) * 256 + (pc & 7) * 32 + n0) * 4);
          }
          const float g0_ = rbh(s0[0]), g1_ = rbh(s0[1]);
          const unsigned pk = cvtpk(rbh(g0_ / (1.f + __expf(-g0_))) * rbh(s1[0]), rbh(g1_ / (1.f + __expf(-g1_))) * rbh(s1[1]));
          const unsigned pn = (unsigned)__builtin_amdgcn_update_dpp(0, (int)pk, 0xb1, 0xf, 0xf, false);
          if ((lane & 1) == 0) ST128((u32x4{pk, tag, pn, tag}), sf, FL_ACT + bid * 1024 + pc * 16);
        }
      } else if (wid < 4) {
        float s0 = 0, s1 = 0;
#pragma unroll
        for (int w = 0; w < 8; ++w) {
          s0 += *(const LAS float*)(uintptr_t)(T_RED + (w * 256 + tid) * 4);
          s1 += *(const LAS float*)(uintptr_t)(T_RED + ((8 + w) * 256 + tid) * 4);
        }
        const float y2 = rbh(rbh(s0 + s1) * sg);
        const bool od = col & 1;
        const float tt = rbh((od ? bfhi(h2g[0]) : bflo(h2g[0])) + y2);
        const float o = (od ? bfhi(x1g[0]) : bflo(x1g[0])) + rbh((od ? bfhi(gbw[0]) : bflo(gbw[0])) * tt);
        pout[(size_t)(slg[0] & 2047u) * DM + col] = (u16)(cvtpk(o, 0.f) & 0xffffu);
      }
    }
    if ((up || down) && rank >= 8) {
      *(GAS u32x2*)(pout + (size_t)row * DM + 4 * lane) = u32x2{ow[0], ow[1]};
      *(GAS u32x2*)(pout + (size_t)row * DM + 256 + 4 * lane) = u32x2{ow[2], ow[3]};
      *(GAS unsigned*)(pout + (size_t)row * DM + 512 + 2 * lane) = ow[4];
    }
  }
}

__global__ __launch_bounds__(512, 2) void k_m1(M1Args A) {
  const int bid = blockIdx.x, xcd = bid & 7, idx = bid >> 3;
  const int wid = rfl(threadIdx.x >> 6);
  unsigned tag;
  asm volatile("s_load_dword %0, %1, %2" : "=s"(tag) : "s"(A.ws), "i"((unsigned)(WS_FLG + FL_EPOCH)) : "memory");
  if (wid == 7) {
    unsigned long long pc; unsigned len;
    asm volatile("s_getpc_b64 %0\nLCODE_BEGIN:\n\ts_mov_b32 %1, LCODE_END-LCODE_BEGIN" : "=s"(pc), "=s"(len));
    const unsigned off = min((unsigned)(idx * 64 + lane_id()) * 128u, len);
    asm volatile("global_load_dword v253, %0, %1" :: "v"(off), "s"(pc) : "v253", "memory");
  }
  front_phase(A, tag, xcd, idx, wid, lane_id());
  attn_phase(A, tag, xcd, idx >> 3, idx & 7, wid, lane_id());
  tail_phase(A, tag, xcd, idx, wid, lane_id());
  asm volatile("LCODE_END:");
}

extern "C" void kernel_launch(void* const* d_in, const int* in_sizes, int n_in, void* d_out, int out_size, void* d_ws, size_t ws_size,
                              hipStream_t stream) {
  const u16* x = (const u16*)d_in[0];
  const u16* ckv = (const u16*)d_in[1];
  const u16* wq = (const u16*)d_in[2];
  const u16* wuk = (const u16*)d_in[3];
  const u16* wuv = (const u16*)d_in[4];
  const u16* wo = (const u16*)d_in[5];
  const u16* wr = (const u16*)d_in[7];
  const u16* wg = (const u16*)d_in[8];
  const u16* wu = (const u16*)d_in[9];
  const u16* wd = (const u16*)d_in[10];
  const u16* c = (const u16*)d_in[11];
  const u16* wmod = (const u16*)d_in[12];
  char* ws = (char*)d_ws;
  (void)hipFuncSetAttribute((const void*)k_m1, hipFuncAttributeMaxDynamicSharedMemorySize, M1_LDS);
  M1Args A{x, ckv, wq, wuk, wuv, c, wmod, wo, wr, wg, wu, wd, (u16*)d_out, ws};
  k_m1<<<256, 512, M1_LDS, stream>>>(A);
}
```
